```python
import torch

def _softmax(x, axis=-1):
    return torch.softmax(x, dim=axis)
def _gelu(x):
    return torch.nn.functional.gelu(x, approximate="tanh")
def _silu(x):
    return torch.nn.functional.silu(x)
def _sigmoid(x):
    return torch.sigmoid(x)
def _layernorm(x, axis=-1, eps=1e-5):
    m = x.mean(dim=axis, keepdim=True)
    v = x.var(dim=axis, keepdim=True, unbiased=False)
    return (x - m) / torch.sqrt(v + eps)
def _scatter_add(tgt, idx, src, axis=0):
    return tgt.clone().index_add_(axis, idx, src)
def _segment_sum(src, seg, n):
    out = torch.zeros((n,) + src.shape[1:], dtype=src.dtype)
    return out.index_add_(0, seg, src)
def _pad(x, pw):
    flat = tuple(v for pair in reversed(pw) for v in pair)
    return torch.nn.functional.pad(x, flat)

kernel_name = 'sequential_preLN__sliding_local_dilated__dense_activation_silu__prompt'

def setup_inputs():

    _w = (lambda a: (lambda t: t.to(torch.bfloat16) if t.is_floating_point() else t)(torch.as_tensor(a)))
    def _A_setup():
        B, S, D = 1, 2048, 576
        return {
            "x": _w((lambda *s: torch.randn(*s, dtype=torch.bfloat16) * 0.05)(B, S, D)),
            "wqkv": _w((lambda *s: torch.randn(*s, dtype=torch.bfloat16) * 0.05)(D, 3 * D)),
            "wo": _w((lambda *s: torch.randn(*s, dtype=torch.bfloat16) * 0.05)(D, D)),
        }
    _A_in = _A_setup()

    def _B_setup():
        B, S, D, F = 1, 2048, 576, 4608
        return {
            "x": _w((lambda *s: torch.randn(*s, dtype=torch.bfloat16) * 0.05)(B, S, D)),
            "w1": _w((lambda *s: torch.randn(*s, dtype=torch.bfloat16) * 0.8)(D, F) * (D ** -0.5)),
            "w2": _w((lambda *s: torch.randn(*s, dtype=torch.bfloat16) * 0.8)(F, D) * (F ** -0.5)),
        }
    _B_in = _B_setup()

    return {
        "A_x": _A_in["x"],
        "A_wqkv": _A_in["wqkv"],
        "A_wo": _A_in["wo"],
        "B_x": _B_in["x"],
        "B_w1": _B_in["w1"],
        "B_w2": _B_in["w2"],
    }

def reference(A_x, A_wqkv, A_wo, B_x, B_w1, B_w2):
    def _A_ref(x, wqkv, wo):
        B, S, D = x.shape
        H = 32; Dh = D // H
        W, dil = 8, 2
        qkv = (lambda x, *d: x.permute(*d))((x @ wqkv).reshape(B, S, 3, H, Dh), 2, 0, 3, 1, 4)
        q, k, v = qkv[0], qkv[1], qkv[2]
        scale = Dh ** -0.5
        pos = torch.arange(S)
        off = torch.arange(W) * dil
        idx = pos[:, None] - off[None, :]
        mask = idx >= 0
        idx_c = torch.where(mask, idx, 0).reshape(-1)
        kg = k[:, :, idx_c, :].reshape(B, H, S, W, Dh)
        vg = v[:, :, idx_c, :].reshape(B, H, S, W, Dh)
        logits = torch.einsum("bhsd,bhswd->bhsw", q, kg) * scale
        logits = torch.where(mask[None, None, :, :], logits, -1e9)
        attn = _softmax(logits, axis=-1)
        o = torch.einsum("bhsw,bhswd->bhsd", attn, vg)
        return (lambda x, *d: x.permute(*d))(o, 0, 2, 1, 3).reshape(B, S, D) @ wo

    def _B_ref(x, w1, w2):
        h = torch.einsum("bsd,df->bsf", x, w1)
        h = _silu(h)
        out = torch.einsum("bsf,fd->bsd", h, w2)
        return out

    x = A_x
    h = x + _A_ref(_layernorm(x, axis=-1), A_wqkv, A_wo)
    h2 = h + _B_ref(_layernorm(h, axis=-1), B_w1, B_w2)
    return h2
```

```cpp
#include <hip/hip_runtime.h>
#include <hip/hip_bf16.h>
#include <stdint.h>

typedef unsigned short u16;
typedef __attribute__((ext_vector_type(8))) short s16x8;
typedef __attribute__((ext_vector_type(4))) float f32x4;

constexpr int S = 2048, D = 576, H = 32, DH = 18, F = 4608;

__device__ __forceinline__ float bf2f(u16 v) { return __uint_as_float(((unsigned)v) << 16); }
__device__ __forceinline__ u16 f2bf(float f) { __bf16 b = (__bf16)f; return __builtin_bit_cast(u16, b); }

__global__ void ln_kernel(const u16* __restrict__ x, u16* __restrict__ y) {
    int row = blockIdx.x * 4 + (threadIdx.x >> 6);
    int lane = threadIdx.x & 63;
    float v[9]; float s = 0.f;
    for (int i = 0; i < 9; ++i) { v[i] = bf2f(x[row * D + lane + 64 * i]); s += v[i]; }
    for (int o = 32; o; o >>= 1) s += __shfl_xor(s, o);
    float m = s / D; float q = 0.f;
    for (int i = 0; i < 9; ++i) { float d = v[i] - m; q += d * d; }
    for (int o = 32; o; o >>= 1) q += __shfl_xor(q, o);
    float r = rsqrtf(q / D + 1e-5f);
    for (int i = 0; i < 9; ++i) y[row * D + lane + 64 * i] = f2bf((v[i] - m) * r);
}

template <int MODE>
__global__ __launch_bounds__(256) void gemm_kernel(const u16* __restrict__ A, const u16* __restrict__ B, u16* __restrict__ C,
                                                   const u16* __restrict__ R, int N, int K) {
    __shared__ u16 As[64][40];
    __shared__ u16 Bs[64][40];
    int bm = blockIdx.y * 64, bn = blockIdx.x * 64;
    int t = threadIdx.x, lane = t & 63, w = t >> 6, wm = (w >> 1) * 32, wn = (w & 1) * 32;
    f32x4 acc[2][2] = {};
    for (int k0 = 0; k0 < K; k0 += 32) {
        { int r = t >> 2, c = (t & 3) * 8;
          *(s16x8*)&As[r][c] = *(const s16x8*)&A[(size_t)(bm + r) * K + k0 + c]; }
        { int kr = t >> 3, c = (t & 7) * 8;
          s16x8 v = *(const s16x8*)&B[(size_t)(k0 + kr) * N + bn + c];
          for (int j = 0; j < 8; ++j) Bs[c + j][kr] = v[j]; }
        __syncthreads();
        s16x8 a[2], b[2];
        for (int i = 0; i < 2; ++i) {
            a[i] = *(s16x8*)&As[wm + i * 16 + (lane & 15)][(lane >> 4) * 8];
            b[i] = *(s16x8*)&Bs[wn + i * 16 + (lane & 15)][(lane >> 4) * 8];
        }
        for (int i = 0; i < 2; ++i) for (int j = 0; j < 2; ++j)
            acc[i][j] = __builtin_amdgcn_mfma_f32_16x16x32_bf16(__builtin_bit_cast(__attribute__((ext_vector_type(8))) __bf16, a[i]), __builtin_bit_cast(__attribute__((ext_vector_type(8))) __bf16, b[j]), acc[i][j], 0, 0, 0);
        __syncthreads();
    }
    for (int i = 0; i < 2; ++i) for (int j = 0; j < 2; ++j) for (int r = 0; r < 4; ++r) {
        int row = bm + wm + i * 16 + (lane >> 4) * 4 + r, col = bn + wn + j * 16 + (lane & 15);
        float v = acc[i][j][r];
        if (MODE == 1) v = v / (1.f + __expf(-v));
        if (MODE == 2) v += bf2f(R[(size_t)row * N + col]);
        C[(size_t)row * N + col] = f2bf(v);
    }
}

__global__ void attn_kernel(const u16* __restrict__ qkv, u16* __restrict__ o) {
    int idx = blockIdx.x * 256 + threadIdx.x;
    int h = idx & 31, s = idx >> 5;
    float q[DH];
    for (int d = 0; d < DH; ++d) q[d] = bf2f(qkv[(size_t)s * 3 * D + h * DH + d]);
    float l[8]; float mx = -1e30f;
    const float scale = 0.23570226039551584f;
    for (int j = 0; j < 8; ++j) {
        int p = s - 2 * j;
        float a = 0.f;
        if (p >= 0) { for (int d = 0; d < DH; ++d) a += q[d] * bf2f(qkv[(size_t)p * 3 * D + D + h * DH + d]); a *= scale; }
        else a = -1e9f;
        l[j] = a; mx = fmaxf(mx, a);
    }
    float sum = 0.f;
    for (int j = 0; j < 8; ++j) { l[j] = __expf(l[j] - mx); sum += l[j]; }
    float r = 1.f / sum;
    float acc[DH] = {};
    for (int j = 0; j < 8; ++j) {
        int p = s - 2 * j; if (p < 0) p = 0;
        for (int d = 0; d < DH; ++d) acc[d] += l[j] * r * bf2f(qkv[(size_t)p * 3 * D + 2 * D + h * DH + d]);
    }
    for (int d = 0; d < DH; ++d) o[(size_t)s * D + h * DH + d] = f2bf(acc[d]);
}

extern "C" void kernel_launch(void* const* d_in, const int* in_sizes, int n_in,
                              void* d_out, int out_size, void* d_ws, size_t ws_size,
                              hipStream_t stream) {
    const u16* x = (const u16*)d_in[0];
    const u16* wqkv = (const u16*)d_in[1];
    const u16* wo = (const u16*)d_in[2];
    const u16* w1 = (const u16*)d_in[4];
    const u16* w2 = (const u16*)d_in[5];
    u16* out = (u16*)d_out;
    u16* ln = (u16*)d_ws;
    u16* qkv = ln + S * D;
    u16* o = qkv + S * 3 * D;
    u16* h = o + S * D;
    u16* h1 = h + S * D;
    ln_kernel<<<S / 4, 256, 0, stream>>>(x, ln);
    gemm_kernel<0><<<dim3(3 * D / 64, S / 64), 256, 0, stream>>>(ln, wqkv, qkv, nullptr, 3 * D, D);
    attn_kernel<<<S * H / 256, 256, 0, stream>>>(qkv, o);
    gemm_kernel<2><<<dim3(D / 64, S / 64), 256, 0, stream>>>(o, wo, h, x, D, D);
    ln_kernel<<<S / 4, 256, 0, stream>>>(h, ln);
    gemm_kernel<1><<<dim3(F / 64, S / 64), 256, 0, stream>>>(ln, w1, h1, nullptr, F, D);
    gemm_kernel<2><<<dim3(D / 64, S / 64), 256, 0, stream>>>(h1, w2, out, h, D, F);
}
```

```cpp
#include <hip/hip_runtime.h>

__global__ __launch_bounds__(256, 1) void fused_block_kernel(const void* x, const void* wqkv, const void* wo, const void* w1,
                                                             const void* w2, void* out, void* ws) {
  asm volatile(
      "s_load_dwordx8 s[4:11], %0, 0x0\n"
      "s_load_dwordx4 s[12:15], %0, 0x20\n"
      "s_load_dwordx2 s[16:17], %0, 0x30\n"
      "v_lshrrev_b32 v2, 6, %2\n"
      "v_and_b32 v1, 63, %2\n"
      "v_readfirstlane_b32 s64, v2\n"
      "s_mov_b32 s68, %1\n"
      "s_and_b32 s69, s68, 1\n"
      "s_lshl_b32 s69, s69, 2\n"
      "s_bfe_u32 s70, s68, 131075\n"
      "s_add_u32 s65, s69, s70\n"
      "s_lshr_b32 s67, s68, 5\n"
      "s_bfe_u32 s69, s68, 131073\n"
      "s_lshl_b32 s69, s69, 3\n"
      "s_add_u32 s66, s69, s67\n"
      "s_mov_b32 s87, 1048576\n"
      "s_getpc_b64 s[28:29]\n"
      "PC0_%=:\n"
      "s_mov_b32 s30, KEND_%=-PC0_%=\n"
      "s_mov_b32 s31, 0x20000\n"
      "s_lshr_b32 s69, s68, 3\n"
      "s_lshl2_add_u32 s69, s69, s64\n"
      "s_and_b32 s69, s69, 31\n"
      "s_lshl_b32 s69, s69, 11\n"
      "v_lshlrev_b32 v239, 7, v1\n"
      "s_mov_b64 exec, 0xffff\n"
      "buffer_load_dword v239, v239, s[28:31], s69 offen\n"
      "s_mov_b64 exec, -1\n"
      "s_and_b32 s71, s64, 1\n"
      "s_lshr_b32 s72, s64, 1\n"
      "s_lshl_b32 s69, s66, 6\n"
      "s_lshl_b32 s70, s71, 5\n"
      "s_add_u32 s70, s70, s69\n"
      "s_add_u32 s70, s70, s72\n"
      "s_and_b32 s74, s65, 1\n"
      "s_lshl4_add_u32 s70, s74, s70\n"
      "s_mul_i32 s20, s70, 1152\n"
      "s_xor_b32 s70, s70, 16\n"
      "s_mul_i32 s21, s70, 1152\n"
      "v_lshrrev_b32 v2, 3, v1\n"
      "v_and_b32 v3, 7, v1\n"
      "v_mul_u32_u24 v4, 2304, v2\n"
      "v_lshl_add_u32 v235, v3, 4, v4\n"
      "s_lshl_b32 s69, s66, 10\n"
      "s_add_u32 s69, s69, 32768\n"
      "s_lshl3_add_u32 s70, s65, s69\n"
      "v_lshl_add_u32 v252, v1, 7, s70\n"
      "s_lshl_b32 s70, s65, 7\n"
      "s_add_u32 s70, s70, s69\n"
      "v_lshl_add_u32 v253, v1, 3, s70\n"
      "v_lshlrev_b32 v254, 2, v1\n"
      "v_cmp_gt_u32 vcc, 8, v1\n"
      "v_mov_b32 v249, 2147418112\n"
      "s_nop 0\n"
      "v_cndmask_b32 v252, v249, v252, vcc\n"
      "v_cndmask_b32 v253, v249, v253, vcc\n"
      "s_lshl_b32 s69, s65, 13\n"
      "s_add_u32 s69, s69, 319488\n"
      "s_lshl3_add_u32 s70, s66, s69\n"
      "v_lshl_add_u32 v248, v1, 8, s70\n"
      "s_lshl_b32 s70, s66, 8\n"
      "s_add_u32 s70, s70, s69\n"
      "s_add_u32 s70, s70, 7936\n"
      "v_lshl_add_u32 v249, v1, 3, s70\n"
      "v_cmp_gt_u32 vcc, 32, v1\n"
      "s_lshl3_add_u32 s23, s66, s65\n"
      "s_lshl_b32 s23, s23, 7\n"
      "v_cndmask_b32 v252, v248, v252, vcc\n"
      "v_cndmask_b32 v253, v249, v253, vcc\n"
      "s_cmp_eq_u32 s64, 3\n"
      "s_cselect_b32 s96, 0, 2147418112\n"
      "s_cselect_b32 s97, s23, 2147418112\n"
      "v_mul_u32_u24 v2, 7282, v1\n"
      "v_lshrrev_b32 v2, 16, v2\n"
      "v_lshlrev_b32 v234, 4, v1\n"
      "s_mul_i32 s73, s65, 144\n"
      "s_bfe_u32 s69, s66, 65539\n"
      "s_and_b32 s70, s66, 7\n"
      "s_lshl1_add_u32 s70, s70, s69\n"
      "s_and_b32 s69, s66, 16\n"
      "s_or_b32 s76, s69, s70\n"
      "s_lshr_b32 s69, s76, 4\n"
      "s_and_b32 s70, s76, 15\n"
      "s_mul_i32 s94, s69, 9\n"
      "s_add_u32 s94, s94, s70\n"
      "s_add_u32 s24, s94, 14\n"
      "s_sub_u32 s75, s76, 9\n"
      "s_mul_i32 s95, s69, 9\n"
      "s_sub_u32 s75, s75, s95\n"
      "s_lshl_b32 s95, s69, 1\n"
      "s_sub_u32 s95, s75, s95\n"
      "s_cmp_lt_u32 s70, 14\n"
      "s_cselect_b32 s95, s95, 9\n"
      "s_cmp_lt_u32 s70, 9\n"
      "s_cselect_b32 s95, 9, s95\n"
      "s_cselect_b32 s94, s94, 18\n"
      "s_cselect_b32 s24, s24, s75\n"
      "s_waitcnt lgkmcnt(0)\n"
      "s_mov_b64 s[60:61], s[16:17]\n"
      "s_mov_b32 s62, 37363712\n"
      "s_mov_b32 s63, 0x20000\n"
      "buffer_load_dword v250, v252, s[60:63], s96 offen offset:4 sc1\n"
      "s_mov_b64 s[36:37], s[4:5]\n"
      "s_mov_b32 s38, 2359296\n"
      "s_mov_b32 s39, 0x20000\n"
      "s_mov_b64 s[40:41], s[6:7]\n"
      "s_mov_b32 s42, 1990656\n"
      "s_mov_b32 s43, 0x20000\n"
      "s_mov_b64 s[44:45], s[8:9]\n"
      "s_mov_b32 s46, 663552\n"
      "s_mov_b32 s47, 0x20000\n"
      "s_cmp_lt_u32 s94, 18\n"
      "s_cbranch_scc0 JO1_%=\n"
      "v_mul_u32_u24 v3, 1008, v2\n"
      "v_add_u32 v3, v234, v3\n"
      "v_add_u32 v4, 10368, v3\n"
      "v_cmp_lt_u32 vcc, 26, v1\n"
      "s_mul_i32 s81, s64, 3456\n"
      "s_nop 0\n"
      "v_cndmask_b32 v176, v3, v4, vcc\n"
      "v_add_u32 v177, 2592, v176\n"
      "v_add_u32 v178, 5184, v176\n"
      "v_add_u32 v179, 7776, v176\n"
      "s_lshl3_add_u32 s80, s81, s73\n"
      "s_mul_i32 s75, s94, 110592\n"
      "s_add_u32 s80, s80, s75\n"
      "s_bfm_b64 s[88:89], 54, 0\n"
      "s_add_u32 m0, s81, 129024\n"
      "s_mov_b64 exec, s[88:89]\n"
      "buffer_load_dwordx4 v176, s[40:43], s80 offen offset:0 lds\n"
      "buffer_load_dwordx4 v177, s[40:43], s80 offen offset:864 lds\n"
      "buffer_load_dwordx4 v178, s[40:43], s80 offen offset:1728 lds\n"
      "buffer_load_dwordx4 v179, s[40:43], s80 offen offset:2592 lds\n"
      "s_mov_b64 exec, -1\n"
      "s_branch JE2_%=\n"
      "JO1_%=:\n"
      "v_mul_u32_u24 v3, 144, v2\n"
      "v_sub_u32 v4, v234, v3\n"
      "v_lshrrev_b32 v3, 1, v2\n"
      "v_mul_u32_u24 v3, 1152, v3\n"
      "v_add_u32 v4, v4, v3\n"
      "v_and_b32 v2, 1, v2\n"
      "s_lshl_b32 s75, s95, 1\n"
      "s_lshl1_add_u32 s69, s64, s72\n"
      "s_mul_i32 s70, s75, 9\n"
      "s_add_i32 s69, s69, s70\n"
      "s_lshl2_add_u32 s70, s75, s64\n"
      "s_mul_i32 s70, s70, 18\n"
      "s_add_i32 s70, s70, -1148\n"
      "s_cmp_lt_u32 s95, 8\n"
      "s_cselect_b32 s69, s69, s70\n"
      "s_cselect_b32 s79, 1, 8\n"
      "s_cselect_b32 s70, 1, 9\n"
      "s_mul_i32 s70, s70, 4608\n"
      "v_mul_u32_u24 v3, s70, v2\n"
      "v_add_u32 v228, v4, v3\n"
      "v_add_u32 v229, 1728, v228\n"
      "s_mul_i32 s69, s69, 4608\n"
      "s_add_u32 s69, s69, s73\n"
      "s_cmp_ge_u32 s95, 9\n"
      "s_cselect_b32 s82, 2147418112, s69\n"
      "s_mul_i32 s79, s79, 41472\n"
      "s_add_u32 s79, s79, s82\n"
      "s_mul_i32 s83, s64, 1152\n"
      "s_bfm_b64 s[90:91], 36, 0\n"
      "s_add_u32 m0, s83, 142848\n"
      "s_mov_b64 exec, s[90:91]\n"
      "buffer_load_dwordx4 v228, s[44:47], s82 offen offset:0 lds\n"
      "buffer_load_dwordx4 v229, s[44:47], s82 offen offset:576 lds\n"
      "s_add_u32 m0, s83, 147456\n"
      "s_nop 0\n"
      "buffer_load_dwordx4 v228, s[44:47], s79 offen offset:0 lds\n"
      "buffer_load_dwordx4 v229, s[44:47], s79 offen offset:576 lds\n"
      "s_mov_b64 exec, -1\n"
      "JE2_%=:\n"
      "buffer_load_dwordx4 v[8:11], v235, s[36:39], s20 offen offset:0\n"
      "buffer_load_dwordx4 v[12:15], v235, s[36:39], s20 offen offset:128\n"
      "buffer_load_dwordx4 v[16:19], v235, s[36:39], s20 offen offset:256\n"
      "buffer_load_dwordx4 v[20:23], v235, s[36:39], s20 offen offset:384\n"
      "s_mov_b64 s[48:49], s[10:11]\n"
      "s_mov_b32 s50, 5308416\n"
      "s_mov_b32 s51, 0x20000\n"
      "s_mov_b64 s[52:53], s[12:13]\n"
      "s_mov_b32 s54, 5308416\n"
      "s_mov_b32 s55, 0x20000\n"
      "s_mov_b64 s[56:57], s[14:15]\n"
      "s_mov_b32 s58, 2359296\n"
      "s_mov_b32 s59, 0x20000\n"
      "s_and_b32 s69, s68, 3\n"
      "s_lshl_b32 s69, s69, 1\n"
      "s_bfe_u32 s70, s68, 65539\n"
      "s_add_u32 s34, s69, s70\n"
      "s_lshr_b32 s67, s68, 4\n"
      "s_bfe_u32 s69, s68, 65538\n"
      "s_lshl_b32 s69, s69, 4\n"
      "s_add_u32 s35, s69, s67\n"
      "s_lshl3_add_u32 s18, s35, s34\n"
      "s_lshl_b32 s18, s18, 7\n"
      "s_add_u32 s18, s18, 393216\n"
      "s_add_u32 s18, s18, s96\n"
      "s_lshl_b32 s69, s35, 13\n"
      "s_lshl_b32 s70, s64, 7\n"
      "s_add_u32 s69, s69, s70\n"
      "s_lshl_b32 s70, s34, 10\n"
      "s_add_u32 s69, s69, s70\n"
      "s_add_u32 s23, s69, 65536\n"
      "v_mov_b32 v249, 0\n"
      "s_lshl_b32 s69, s66, 6\n"
      "s_sub_i32 s69, s69, 16\n"
      "s_max_i32 s69, s69, 0\n"
      "s_lshl3_add_u32 s69, s71, s69\n"
      "s_add_u32 s69, s69, s72\n"
      "s_mul_i32 s22, s69, 1152\n"
      "v_lshrrev_b32 v5, 4, v1\n"
      "v_and_b32 v6, 15, v1\n"
      "v_mul_u32_u24 v7, 2304, v5\n"
      "v_lshl_add_u32 v236, v6, 4, v7\n"
      "v_bfe_u32 v7, v1, 3, 1\n"
      "v_lshl_add_u32 v237, v7, 30, v236\n"
      "v_mov_b32 v188, 0x3f803f80\n"
      "s_mov_b32 s78, 0xffff0000\n"
      "v_mov_b32 v190, 0\n"
      "v_mov_b32 v191, 0\n"
      "v_mov_b32 v192, 0\n"
      "v_mov_b32 v193, 0\n"
      "v_mov_b32 v194, 0\n"
      "v_mov_b32 v195, 0\n"
      "s_lshl2_add_u32 s26, s67, s64\n"
      "v_lshrrev_b32 v2, 1, v1\n"
      "v_and_b32 v3, 1, v1\n"
      "v_mul_u32_u24 v4, 9216, v2\n"
      "v_lshlrev_b32 v5, 7, v3\n"
      "s_mul_i32 s69, s34, 1152\n"
      "v_add3_u32 v243, v4, v5, s69\n"
      "s_lshr_b32 s69, s35, 4\n"
      "s_and_b32 s70, s35, 15\n"
      "s_mul_i32 s75, s69, 9\n"
      "s_add_u32 s24, s75, s70\n"
      "s_add_u32 s24, s24, 14\n"
      "s_sub_u32 s75, s35, s75\n"
      "s_sub_u32 s75, s75, 9\n"
      "s_cmp_lt_u32 s70, 9\n"
      "s_cselect_b32 s24, s24, s75\n"
      "s_lshl_b32 s75, s95, 1\n"
      "v_lshrrev_b32 v2, 3, v1\n"
      "v_and_b32 v3, 7, v1\n"
      "v_lshrrev_b32 v5, 4, v1\n"
      "v_and_b32 v6, 15, v1\n"
      "v_xor_b32 v4, v2, v3\n"
      "v_lshlrev_b32 v4, 4, v4\n"
      "v_and_b32 v7, 3, v3\n"
      "v_lshl_add_u32 v4, v7, 8, v4\n"
      "v_lshrrev_b32 v2, 2, v3\n"
      "v_mul_u32_u24 v2, 5120, v2\n"
      "s_lshl_b32 s25, s64, 10\n"
      "s_lshl_b32 s69, s74, 7\n"
      "s_add_u32 s69, s69, s25\n"
      "s_add_u32 s69, s69, 0\n"
      "v_add3_u32 v184, v4, v2, s69\n"
      "v_xor_b32 v185, 0x80, v184\n"
      "v_add_u32 v240, 61440, v184\n"
      "v_add_u32 v241, 61440, v185\n"
      "s_lshl_b32 s69, s64, 2\n"
      "v_add_u32 v4, s69, v5\n"
      "v_xor_b32 v4, v4, v3\n"
      "v_lshlrev_b32 v4, 4, v4\n"
      "v_lshl_add_u32 v4, v7, 8, v4\n"
      "v_lshrrev_b32 v2, 2, v6\n"
      "v_mul_u32_u24 v2, 5120, v2\n"
      "v_add_u32 v2, 4096, v2\n"
      "v_add_u32 v186, v4, v2\n"
      "v_add_u32 v187, 81920, v186\n"
      "v_xor_b32 v4, v6, v5\n"
      "v_lshlrev_b32 v4, 4, v4\n"
      "v_lshl_add_u32 v4, v5, 8, v4\n"
      "v_add_u32 v182, 0, v4\n"
      "v_xor_b32 v183, 64, v182\n"
      "v_lshrrev_b32 v2, 2, v1\n"
      "v_mul_u32_u24 v2, 864, v2\n"
      "v_and_b32 v3, 3, v1\n"
      "s_waitcnt vmcnt(8)\n"
      "v_add_u32 v250, 1, v250\n"
      "buffer_store_dword v250, v253, s[60:63], s96 offen sc1\n"
      "v_lshl_add_u32 v4, v3, 3, v2\n"
      "s_mul_i32 s69, s64, 36\n"
      "s_lshl2_add_u32 s69, s71, s69\n"
      "s_add_u32 s69, s69, 129024\n"
      "v_add_u32 v180, s69, v4\n"
      "v_min_u32 v3, 2, v3\n"
      "v_mul_u32_u24 v3, 144, v3\n"
      "s_mul_i32 s69, s72, 72\n"
      "s_add_u32 s69, s69, 129056\n"
      "v_add3_u32 v181, v2, v3, s69\n"
      "v_lshrrev_b32 v2, 2, v1\n"
      "v_mul_u32_u24 v2, 288, v2\n"
      "v_and_b32 v3, 3, v1\n"
      "v_lshl_add_u32 v2, v3, 3, v2\n"
      "s_mul_i32 s69, s71, 4608\n"
      "s_add_u32 s69, s69, 142976\n"
      "v_add_u32 v231, s69, v2\n"
      "s_lshl_b32 s69, s64, 5\n"
      "s_add_u32 s69, s69, 142848\n"
      "v_add_u32 v230, s69, v2\n"
      "v_and_b32 v2, 7, v1\n"
      "v_lshl_add_u32 v2, v5, 3, v2\n"
      "v_lshlrev_b32 v242, 4, v2\n"
      "v_bfe_u32 v2, v1, 3, 1\n"
      "v_lshl_add_u32 v189, v2, 30, v242\n"
      "s_lshl2_add_u32 s69, s65, s64\n"
      "s_mul_i32 s69, s69, 73728\n"
      "s_add_u32 s31, s69, 34340864\n"
      "s_lshl_b32 s69, s94, 12\n"
      "s_add_u32 s69, s69, s31\n"
      "s_cmp_lt_u32 s94, 18\n"
      "s_cselect_b32 s28, s69, 2147418112\n"
      "s_mul_i32 s69, s65, 82944\n"
      "s_add_u32 s69, s69, 36700160\n"
      "s_add_u32 s84, s69, s25\n"
      "s_mul_i32 s69, s75, 4608\n"
      "s_add_u32 s69, s69, s84\n"
      "s_cmp_ge_u32 s95, 9\n"
      "s_cselect_b32 s29, 2147418112, s69\n"
      "s_mul_i32 s69, s64, 3584\n"
      "s_add_u32 s69, s69, s29\n"
      "s_add_u32 s69, s69, 4096\n"
      "s_cmp_lt_u32 s64, 2\n"
      "s_cselect_b32 s30, s69, 2147418112\n"
      "s_lshr_b32 s69, s24, 1\n"
      "s_and_b32 s70, s24, 1\n"
      "s_cmp_lt_u32 s69, 7\n"
      "s_cselect_b64 s[12:13], s[48:49], s[52:53]\n"
      "s_movk_i32 s72, 1152\n"
      "s_cselect_b32 s71, 9216, s72\n"
      "s_cselect_b32 s72, s72, 663552\n"
      "s_cselect_b32 s73, 2, -7\n"
      "s_mov_b32 s14, 5308416\n"
      "s_mov_b32 s15, 0x20000\n"
      "s_add_i32 s73, s73, s69\n"
      "s_lshl_b32 s73, s73, 7\n"
      "s_mul_i32 s72, s72, s34\n"
      "s_add_u32 s72, s72, s73\n"
      "s_mul_i32 s73, s70, 36\n"
      "s_add_u32 s73, s73, s64\n"
      "s_mul_i32 s73, s73, s71\n"
      "s_lshl3_add_u32 s85, s73, s72\n"
      "s_lshl_b32 s77, s71, 5\n"
      "s_lshl4_add_u32 s69, s34, s69\n"
      "s_lshl2_add_u32 s69, s69, s64\n"
      "s_mul_i32 s69, s69, 19456\n"
      "s_add_u32 s69, s69, 24379392\n"
      "s_lshl_b32 s19, s70, 9\n"
      "s_add_u32 s19, s19, s69\n"
      "s_add_u32 s19, s19, 18432\n"
      "s_mul_i32 s73, s70, 9216\n"
      "s_add_u32 s86, s69, s73\n"
      "s_cmp_lt_u32 s24, 14\n"
      "s_cselect_b32 s19, s19, 2147418112\n"
      "s_and_b32 s70, s64, 1\n"
      "s_lshl_b32 s70, s70, 2\n"
      "v_bfe_u32 v3, v1, 4, 1\n"
      "v_lshlrev_b32 v3, 1, v3\n"
      "v_and_b32 v4, 7, v1\n"
      "v_xor_b32 v4, v4, v3\n"
      "v_xor_b32 v4, s70, v4\n"
      "v_lshrrev_b32 v2, 3, v1\n"
      "v_mul_u32_u24 v3, s71, v2\n"
      "v_lshl_add_u32 v232, v4, 4, v3\n"
      "v_lshrrev_b32 v2, 4, v1\n"
      "v_bfe_u32 v3, v1, 2, 2\n"
      "v_lshlrev_b32 v2, 10, v2\n"
      "v_lshl_add_u32 v2, v3, 7, v2\n"
      "v_bfe_u32 v3, v1, 3, 2\n"
      "v_xor_b32 v3, s64, v3\n"
      "v_lshl_add_u32 v2, v3, 5, v2\n"
      "v_and_b32 v3, 3, v1\n"
      "v_lshl_add_u32 v233, v3, 3, v2\n"
      "v_add_u32 v233, 92160, v233\n"
      "v_mov_b32 v2, 0x3f003f00\n"
      "v_accvgpr_write_b32 a116, v2\n"
      "v_accvgpr_write_b32 a117, v2\n"
      "v_accvgpr_write_b32 a118, v2\n"
      "v_accvgpr_write_b32 a119, v2\n"
      "v_and_b32 v2, 15, v1\n"
      "v_lshrrev_b32 v3, 4, v1\n"
      "v_lshlrev_b32 v4, 2, v3\n"
      "v_sub_u32 v4, v4, v2\n"
      "v_mov_b32 v5, 0xf149f2ca\n"
      "s_cmp_eq_u32 s66, 0\n"
      "s_cselect_b32 s69, 0xf149f2ca, 0\n"
      "v_cmp_gt_u32 vcc, 32, v1\n"
      "s_nop 1\n"
      "v_cndmask_b32 v7, -16, v4, vcc\n"
      "v_cmp_lt_i32 vcc, 8, v4\n"
      "v_mov_b32 v6, 0xffff\n"
      "s_nop 0\n"
      "v_cndmask_b32 v204, v5, 0, vcc\n"
      "v_cndmask_b32 v214, 0, v6, vcc\n"
      "v_cmp_lt_i32 vcc, 0, v7\n"
      "v_mov_b32 v6, 0xffff\n"
      "s_nop 0\n"
      "v_cndmask_b32 v196, v5, 0, vcc\n"
      "v_cndmask_b32 v212, 0, v6, vcc\n"
      "v_add_u32 v6, 7, v4\n"
      "v_cmp_gt_u32 vcc, 8, v6\n"
      "v_min_f32 v200, s69, v204\n"
      "v_min_f32 v196, s69, v196\n"
      "v_cndmask_b32 v208, v5, 0, vcc\n"
      "v_cmp_lt_i32 vcc, 7, v4\n"
      "v_mov_b32 v6, 0xffff0000\n"
      "s_nop 0\n"
      "v_cndmask_b32 v205, v5, 0, vcc\n"
      "v_cndmask_b32 v6, 0, v6, vcc\n"
      "v_or_b32 v214, v214, v6\n"
      "v_cmp_lt_i32 vcc, -1, v7\n"
      "v_mov_b32 v6, 0xffff0000\n"
      "s_nop 0\n"
      "v_cndmask_b32 v197, v5, 0, vcc\n"
      "v_cndmask_b32 v6, 0, v6, vcc\n"
      "v_or_b32 v212, v212, v6\n"
      "v_add_u32 v6, 8, v4\n"
      "v_cmp_gt_u32 vcc, 8, v6\n"
      "v_min_f32 v201, s69, v205\n"
      "v_min_f32 v197, s69, v197\n"
      "v_cndmask_b32 v209, v5, 0, vcc\n"
      "v_cmp_lt_i32 vcc, 6, v4\n"
      "v_mov_b32 v6, 0xffff\n"
      "s_nop 0\n"
      "v_cndmask_b32 v206, v5, 0, vcc\n"
      "v_cndmask_b32 v215, 0, v6, vcc\n"
      "v_cmp_lt_i32 vcc, -2, v7\n"
      "v_mov_b32 v6, 0xffff\n"
      "s_nop 0\n"
      "v_cndmask_b32 v198, v5, 0, vcc\n"
      "v_cndmask_b32 v213, 0, v6, vcc\n"
      "v_add_u32 v6, 9, v4\n"
      "v_cmp_gt_u32 vcc, 8, v6\n"
      "v_min_f32 v202, s69, v206\n"
      "v_min_f32 v198, s69, v198\n"
      "v_cndmask_b32 v210, v5, 0, vcc\n"
      "v_cmp_lt_i32 vcc, 5, v4\n"
      "v_mov_b32 v6, 0xffff0000\n"
      "s_nop 0\n"
      "v_cndmask_b32 v207, v5, 0, vcc\n"
      "v_cndmask_b32 v6, 0, v6, vcc\n"
      "v_or_b32 v215, v215, v6\n"
      "v_cmp_lt_i32 vcc, -3, v7\n"
      "v_mov_b32 v6, 0xffff0000\n"
      "s_nop 0\n"
      "v_cndmask_b32 v199, v5, 0, vcc\n"
      "v_cndmask_b32 v6, 0, v6, vcc\n"
      "v_or_b32 v213, v213, v6\n"
      "v_add_u32 v6, 10, v4\n"
      "v_cmp_gt_u32 vcc, 8, v6\n"
      "v_min_f32 v203, s69, v207\n"
      "v_min_f32 v199, s69, v199\n"
      "v_cndmask_b32 v211, v5, 0, vcc\n"
      "s_and_b32 s69, s64, 1\n"
      "s_sub_u32 s70, s69, 1\n"
      "s_not_b32 s71, s70\n"
      "v_cmp_eq_u32 vcc, 1, v3\n"
      "v_mov_b32 v220, 0\n"
      "v_mov_b32 v221, 0\n"
      "v_cndmask_b32 v6, 0, -1, vcc\n"
      "v_and_b32 v216, s70, v6\n"
      "v_and_b32 v217, s71, v6\n"
      "v_mov_b32 v6, 0\n"
      "s_mov_b32 s69, 0x3f80\n"
      "v_writelane_b32 v6, s69, 32\n"
      "s_mov_b32 s69, 0x3f800000\n"
      "v_writelane_b32 v6, s69, 33\n"
      "v_and_b32 v7, 3, v1\n"
      "v_cmp_eq_u32 vcc, 2, v7\n"
      "v_and_b32 v222, s70, v6\n"
      "v_and_b32 v223, s71, v6\n"
      "v_cndmask_b32 v224, 0, 1.0, vcc\n"
      "v_cndmask_b32 v225, 0, 1.0, vcc\n"
      "v_cndmask_b32 v226, 0, 1.0, vcc\n"
      "v_cndmask_b32 v227, 0, 1.0, vcc\n"
      "v_mov_b32 v218, v214\n"
      "v_mov_b32 v219, v215\n"
      "s_bitcmp0_b32 s64, 0\n"
      "s_cbranch_scc1 EV3_%=\n"
      "v_swap_b32 v196, v200\n"
      "v_swap_b32 v197, v201\n"
      "v_swap_b32 v198, v202\n"
      "v_swap_b32 v199, v203\n"
      "v_swap_b32 v212, v218\n"
      "v_swap_b32 v213, v219\n"
      "EV3_%=:\n"
      "buffer_load_dwordx4 v[24:27], v235, s[36:39], s20 offen offset:512\n"
      "buffer_load_dwordx4 v[28:31], v235, s[36:39], s20 offen offset:640\n"
      "s_waitcnt vmcnt(7)\n"
      "s_barrier\n"
      "s_mov_b32 exec_hi, 0\n"
      "buffer_store_dword v249, v254, s[60:63], s23 offen sc1\n"
      "buffer_store_dword v249, v254, s[60:63], s97 offen sc1\n"
      "buffer_store_dword v249, v254, s[60:63], s18 offen sc1\n"
      "s_mov_b64 exec, -1\n"
      "s_cmp_lt_u32 s94, 18\n"
      "s_cbranch_scc0 JO4_%=\n"
      "ds_read_b64_tr_b16 a[120:121], v180 offset:0\n"
      "ds_read_b64_tr_b16 a[122:123], v180 offset:432\n"
      "ds_read_b64_tr_b16 a[124:125], v180 offset:144\n"
      "ds_read_b64_tr_b16 a[126:127], v180 offset:576\n"
      "ds_read_b64_tr_b16 a[128:129], v181 offset:0\n"
      "ds_read_b64_tr_b16 a[130:131], v181 offset:432\n"
      "ds_read_b64_tr_b16 a[132:133], v180 offset:288\n"
      "ds_read_b64_tr_b16 a[134:135], v180 offset:720\n"
      "s_waitcnt lgkmcnt(6)\n"
      "buffer_store_dwordx4 a[120:123], v234, s[60:63], s28 offen offset:0 sc1\n"
      "s_waitcnt lgkmcnt(4)\n"
      "buffer_store_dwordx4 a[124:127], v234, s[60:63], s28 offen offset:1024 sc1\n"
      "s_waitcnt lgkmcnt(2)\n"
      "buffer_store_dwordx4 a[128:131], v234, s[60:63], s28 offen offset:2048 sc1\n"
      "s_waitcnt lgkmcnt(0)\n"
      "buffer_store_dwordx4 a[132:135], v234, s[60:63], s28 offen offset:3072 sc1\n"
      "s_branch JE5_%=\n"
      "JO4_%=:\n"
      "ds_read_b64_tr_b16 a[136:137], v230 offset:0\n"
      "ds_read_b64_tr_b16 a[138:139], v230 offset:144\n"
      "ds_read_b64_tr_b16 a[140:141], v231 offset:0\n"
      "ds_read_b64_tr_b16 a[142:143], v231 offset:144\n"
      "ds_read_b64_tr_b16 a[144:145], v230 offset:4608\n"
      "ds_read_b64_tr_b16 a[146:147], v230 offset:4752\n"
      "s_waitcnt lgkmcnt(4)\n"
      "buffer_store_dwordx4 a[136:139], v234, s[60:63], s29 offen sc1\n"
      "s_waitcnt lgkmcnt(2)\n"
      "buffer_store_dwordx4 a[140:143], v189, s[60:63], s30 offen sc1\n"
      "s_add_u32 s29, s29, 4608\n"
      "s_waitcnt lgkmcnt(0)\n"
      "buffer_store_dwordx4 a[144:147], v234, s[60:63], s29 offen sc1\n"
      "JE5_%=:\n"
      "s_and_b32 s69, s64, 1\n"
      "s_lshl_b32 s70, s69, 11\n"
      "v_add_u32 v176, s70, v182\n"
      "v_add_u32 v177, s70, v183\n"
      "s_xor_b32 s70, s70, 2048\n"
      "v_add_u32 v178, s70, v182\n"
      "v_add_u32 v179, s70, v183\n"
      "s_add_u32 s70, s25, 157696\n"
      "v_lshl_add_u32 v180, v1, 3, s70\n"
      "v_xor_b32 v181, 1024, v180\n"
      "buffer_load_dwordx4 v[32:35], v235, s[36:39], s20 offen offset:768\n"
      "buffer_load_dwordx4 v[36:39], v235, s[36:39], s20 offen offset:896\n"
      "buffer_load_dwordx4 v[40:43], v235, s[36:39], s20 offen offset:1024\n"
      "buffer_load_dwordx4 v[44:47], v235, s[36:39], s21 offen offset:0\n"
      "buffer_load_dwordx4 v[48:51], v235, s[36:39], s21 offen offset:128\n"
      "buffer_load_dwordx4 v[52:55], v235, s[36:39], s21 offen offset:256\n"
      "buffer_load_dwordx4 v[56:59], v235, s[36:39], s21 offen offset:384\n"
      "s_waitcnt vmcnt(19)\n"
      "v_dot2c_f32_bf16 v191, v8, v188\n"
      "v_dot2c_f32_bf16 v190, v8, v8\n"
      "v_dot2c_f32_bf16 v191, v9, v188\n"
      "v_dot2c_f32_bf16 v190, v9, v9\n"
      "v_dot2c_f32_bf16 v191, v10, v188\n"
      "v_dot2c_f32_bf16 v190, v10, v10\n"
      "v_dot2c_f32_bf16 v191, v11, v188\n"
      "v_dot2c_f32_bf16 v190, v11, v11\n"
      "buffer_load_dwordx4 v[60:63], v235, s[36:39], s21 offen offset:512\n"
      "s_waitcnt vmcnt(19)\n"
      "v_dot2c_f32_bf16 v191, v12, v188\n"
      "v_dot2c_f32_bf16 v190, v12, v12\n"
      "v_dot2c_f32_bf16 v191, v13, v188\n"
      "v_dot2c_f32_bf16 v190, v13, v13\n"
      "v_dot2c_f32_bf16 v191, v14, v188\n"
      "v_dot2c_f32_bf16 v190, v14, v14\n"
      "v_dot2c_f32_bf16 v191, v15, v188\n"
      "v_dot2c_f32_bf16 v190, v15, v15\n"
      "buffer_load_dwordx4 v[64:67], v235, s[36:39], s21 offen offset:640\n"
      "s_waitcnt vmcnt(19)\n"
      "v_dot2c_f32_bf16 v191, v16, v188\n"
      "v_dot2c_f32_bf16 v190, v16, v16\n"
      "v_dot2c_f32_bf16 v191, v17, v188\n"
      "v_dot2c_f32_bf16 v190, v17, v17\n"
      "v_dot2c_f32_bf16 v191, v18, v188\n"
      "v_dot2c_f32_bf16 v190, v18, v18\n"
      "v_dot2c_f32_bf16 v191, v19, v188\n"
      "v_dot2c_f32_bf16 v190, v19, v19\n"
      "buffer_load_dwordx4 v[68:71], v235, s[36:39], s21 offen offset:768\n"
      "s_waitcnt vmcnt(19)\n"
      "v_dot2c_f32_bf16 v191, v20, v188\n"
      "v_dot2c_f32_bf16 v190, v20, v20\n"
      "v_dot2c_f32_bf16 v191, v21, v188\n"
      "v_dot2c_f32_bf16 v190, v21, v21\n"
      "v_dot2c_f32_bf16 v191, v22, v188\n"
      "v_dot2c_f32_bf16 v190, v22, v22\n"
      "v_dot2c_f32_bf16 v191, v23, v188\n"
      "v_dot2c_f32_bf16 v190, v23, v23\n"
      "buffer_load_dwordx4 v[72:75], v235, s[36:39], s21 offen offset:896\n"
      "s_waitcnt vmcnt(18)\n"
      "v_dot2c_f32_bf16 v191, v24, v188\n"
      "v_dot2c_f32_bf16 v190, v24, v24\n"
      "v_dot2c_f32_bf16 v191, v25, v188\n"
      "v_dot2c_f32_bf16 v190, v25, v25\n"
      "v_dot2c_f32_bf16 v191, v26, v188\n"
      "v_dot2c_f32_bf16 v190, v26, v26\n"
      "v_dot2c_f32_bf16 v191, v27, v188\n"
      "v_dot2c_f32_bf16 v190, v27, v27\n"
      "buffer_load_dwordx4 v[76:79], v235, s[36:39], s21 offen offset:1024\n"
      "s_waitcnt vmcnt(18)\n"
      "v_dot2c_f32_bf16 v191, v28, v188\n"
      "v_dot2c_f32_bf16 v190, v28, v28\n"
      "v_dot2c_f32_bf16 v191, v29, v188\n"
      "v_dot2c_f32_bf16 v190, v29, v29\n"
      "v_dot2c_f32_bf16 v191, v30, v188\n"
      "v_dot2c_f32_bf16 v190, v30, v30\n"
      "v_dot2c_f32_bf16 v191, v31, v188\n"
      "v_dot2c_f32_bf16 v190, v31, v31\n"
      "buffer_load_dword v251, v252, s[60:63], s96 offen sc1\n"
      "s_waitcnt vmcnt(12)\n"
      "v_dot2c_f32_bf16 v191, v32, v188\n"
      "v_dot2c_f32_bf16 v190, v32, v32\n"
      "v_dot2c_f32_bf16 v191, v33, v188\n"
      "v_dot2c_f32_bf16 v190, v33, v33\n"
      "v_dot2c_f32_bf16 v191, v34, v188\n"
      "v_dot2c_f32_bf16 v190, v34, v34\n"
      "v_dot2c_f32_bf16 v191, v35, v188\n"
      "v_dot2c_f32_bf16 v190, v35, v35\n"
      "buffer_load_dwordx4 v[80:83], v236, s[36:39], s22 offen offset:0\n"
      "s_waitcnt vmcnt(12)\n"
      "v_dot2c_f32_bf16 v191, v36, v188\n"
      "v_dot2c_f32_bf16 v190, v36, v36\n"
      "v_dot2c_f32_bf16 v191, v37, v188\n"
      "v_dot2c_f32_bf16 v190, v37, v37\n"
      "v_dot2c_f32_bf16 v191, v38, v188\n"
      "v_dot2c_f32_bf16 v190, v38, v38\n"
      "v_dot2c_f32_bf16 v191, v39, v188\n"
      "v_dot2c_f32_bf16 v190, v39, v39\n"
      "buffer_load_dwordx4 v[84:87], v236, s[36:39], s22 offen offset:256\n"
      "s_waitcnt vmcnt(12)\n"
      "v_dot2c_f32_bf16 v191, v40, v188\n"
      "v_dot2c_f32_bf16 v190, v40, v40\n"
      "v_dot2c_f32_bf16 v191, v41, v188\n"
      "v_dot2c_f32_bf16 v190, v41, v41\n"
      "v_dot2c_f32_bf16 v191, v42, v188\n"
      "v_dot2c_f32_bf16 v190, v42, v42\n"
      "v_dot2c_f32_bf16 v191, v43, v188\n"
      "v_dot2c_f32_bf16 v190, v43, v43\n"
      "buffer_load_dwordx4 v[88:91], v236, s[36:39], s22 offen offset:512\n"
      "s_nop 1\n"
      "v_add_f32_dpp v190, v190, v190 quad_perm:[1,0,3,2] row_mask:0xf bank_mask:0xf\n"
      "v_add_f32_dpp v191, v191, v191 quad_perm:[1,0,3,2] row_mask:0xf bank_mask:0xf\n"
      "s_nop 0\n"
      "v_add_f32_dpp v190, v190, v190 quad_perm:[2,3,0,1] row_mask:0xf bank_mask:0xf\n"
      "v_add_f32_dpp v191, v191, v191 quad_perm:[2,3,0,1] row_mask:0xf bank_mask:0xf\n"
      "s_nop 0\n"
      "v_add_f32_dpp v190, v190, v190 row_half_mirror row_mask:0xf bank_mask:0xf\n"
      "v_add_f32_dpp v191, v191, v191 row_half_mirror row_mask:0xf bank_mask:0xf\n"
      "v_mul_f32 v191, 0x3ae38e39, v191\n"
      "v_mul_f32 v190, 0x3ae38e39, v190\n"
      "v_fma_f32 v190, -v191, v191, v190\n"
      "v_max_f32 v190, 0, v190\n"
      "v_add_f32 v190, 0x3727c5ac, v190\n"
      "v_rsq_f32 v190, v190\n"
      "s_nop 0\n"
      "v_mul_f32 v191, v191, v190\n"
      "v_lshlrev_b32 v2, 16, v8\n"
      "s_waitcnt vmcnt(12)\n"
      "v_dot2c_f32_bf16 v193, v44, v188\n"
      "v_dot2c_f32_bf16 v192, v44, v44\n"
      "v_dot2c_f32_bf16 v193, v45, v188\n"
      "v_dot2c_f32_bf16 v192, v45, v45\n"
      "v_dot2c_f32_bf16 v193, v46, v188\n"
      "v_dot2c_f32_bf16 v192, v46, v46\n"
      "v_dot2c_f32_bf16 v193, v47, v188\n"
      "v_dot2c_f32_bf16 v192, v47, v47\n"
      "buffer_load_dwordx4 v[92:95], v236, s[36:39], s22 offen offset:768\n"
      "v_and_b32 v3, s78, v8\n"
      "v_lshlrev_b32 v4, 16, v9\n"
      "v_and_b32 v5, s78, v9\n"
      "v_pk_fma_f32 v[2:3], v[2:3], v[190:191], v[190:191] op_sel:[0,0,1] op_sel_hi:[1,0,1] neg_lo:[0,0,1] neg_hi:[0,0,1]\n"
      "v_pk_fma_f32 v[4:5], v[4:5], v[190:191], v[190:191] op_sel:[0,0,1] op_sel_hi:[1,0,1] neg_lo:[0,0,1] neg_hi:[0,0,1]\n"
      "v_cvt_pk_bf16_f32 v8, v2, v3\n"
      "v_cvt_pk_bf16_f32 v9, v4, v5\n"
      "v_lshlrev_b32 v2, 16, v10\n"
      "v_and_b32 v3, s78, v10\n"
      "v_lshlrev_b32 v4, 16, v11\n"
      "v_and_b32 v5, s78, v11\n"
      "v_pk_fma_f32 v[2:3], v[2:3], v[190:191], v[190:191] op_sel:[0,0,1] op_sel_hi:[1,0,1] neg_lo:[0,0,1] neg_hi:[0,0,1]\n"
      "v_pk_fma_f32 v[4:5], v[4:5], v[190:191], v[190:191] op_sel:[0,0,1] op_sel_hi:[1,0,1] neg_lo:[0,0,1] neg_hi:[0,0,1]\n"
      "v_cvt_pk_bf16_f32 v10, v2, v3\n"
      "s_waitcnt vmcnt(12)\n"
      "v_dot2c_f32_bf16 v193, v48, v188\n"
      "v_dot2c_f32_bf16 v192, v48, v48\n"
      "v_dot2c_f32_bf16 v193, v49, v188\n"
      "v_dot2c_f32_bf16 v192, v49, v49\n"
      "v_dot2c_f32_bf16 v193, v50, v188\n"
      "v_dot2c_f32_bf16 v192, v50, v50\n"
      "v_dot2c_f32_bf16 v193, v51, v188\n"
      "v_dot2c_f32_bf16 v192, v51, v51\n"
      "buffer_load_dwordx4 v[96:99], v237, s[36:39], s22 offen offset:1024\n"
      "v_cvt_pk_bf16_f32 v11, v4, v5\n"
      "ds_write_b128 v184, v[8:11] offset:0\n"
      "v_lshlrev_b32 v2, 16, v12\n"
      "v_and_b32 v3, s78, v12\n"
      "v_lshlrev_b32 v4, 16, v13\n"
      "v_and_b32 v5, s78, v13\n"
      "v_pk_fma_f32 v[2:3], v[2:3], v[190:191], v[190:191] op_sel:[0,0,1] op_sel_hi:[1,0,1] neg_lo:[0,0,1] neg_hi:[0,0,1]\n"
      "v_pk_fma_f32 v[4:5], v[4:5], v[190:191], v[190:191] op_sel:[0,0,1] op_sel_hi:[1,0,1] neg_lo:[0,0,1] neg_hi:[0,0,1]\n"
      "v_cvt_pk_bf16_f32 v12, v2, v3\n"
      "v_cvt_pk_bf16_f32 v13, v4, v5\n"
      "v_lshlrev_b32 v2, 16, v14\n"
      "v_and_b32 v3, s78, v14\n"
      "v_lshlrev_b32 v4, 16, v15\n"
      "v_and_b32 v5, s78, v15\n"
      "s_waitcnt vmcnt(12)\n"
      "v_dot2c_f32_bf16 v193, v52, v188\n"
      "v_dot2c_f32_bf16 v192, v52, v52\n"
      "v_dot2c_f32_bf16 v193, v53, v188\n"
      "v_dot2c_f32_bf16 v192, v53, v53\n"
      "v_dot2c_f32_bf16 v193, v54, v188\n"
      "v_dot2c_f32_bf16 v192, v54, v54\n"
      "v_dot2c_f32_bf16 v193, v55, v188\n"
      "v_dot2c_f32_bf16 v192, v55, v55\n"
      "s_add_u32 m0, s25, 92160\n"
      "s_nop 0\n"
      "buffer_load_dwordx4 v232, s[12:15], s85 offen lds\n"
      "v_pk_fma_f32 v[2:3], v[2:3], v[190:191], v[190:191] op_sel:[0,0,1] op_sel_hi:[1,0,1] neg_lo:[0,0,1] neg_hi:[0,0,1]\n"
      "v_pk_fma_f32 v[4:5], v[4:5], v[190:191], v[190:191] op_sel:[0,0,1] op_sel_hi:[1,0,1] neg_lo:[0,0,1] neg_hi:[0,0,1]\n"
      "v_cvt_pk_bf16_f32 v14, v2, v3\n"
      "v_cvt_pk_bf16_f32 v15, v4, v5\n"
      "ds_write_b128 v184, v[12:15] offset:10240\n"
      "v_lshlrev_b32 v2, 16, v16\n"
      "v_and_b32 v3, s78, v16\n"
      "v_lshlrev_b32 v4, 16, v17\n"
      "v_and_b32 v5, s78, v17\n"
      "v_pk_fma_f32 v[2:3], v[2:3], v[190:191], v[190:191] op_sel:[0,0,1] op_sel_hi:[1,0,1] neg_lo:[0,0,1] neg_hi:[0,0,1]\n"
      "v_pk_fma_f32 v[4:5], v[4:5], v[190:191], v[190:191] op_sel:[0,0,1] op_sel_hi:[1,0,1] neg_lo:[0,0,1] neg_hi:[0,0,1]\n"
      "v_cvt_pk_bf16_f32 v16, v2, v3\n"
      "v_cvt_pk_bf16_f32 v17, v4, v5\n"
      "v_lshlrev_b32 v2, 16, v18\n"
      "s_waitcnt vmcnt(12)\n"
      "v_dot2c_f32_bf16 v193, v56, v188\n"
      "v_dot2c_f32_bf16 v192, v56, v56\n"
      "v_dot2c_f32_bf16 v193, v57, v188\n"
      "v_dot2c_f32_bf16 v192, v57, v57\n"
      "v_dot2c_f32_bf16 v193, v58, v188\n"
      "v_dot2c_f32_bf16 v192, v58, v58\n"
      "v_dot2c_f32_bf16 v193, v59, v188\n"
      "v_dot2c_f32_bf16 v192, v59, v59\n"
      "s_add_u32 m0, s25, 96256\n"
      "s_add_u32 s85, s85, s77\n"
      "buffer_load_dwordx4 v232, s[12:15], s85 offen lds\n"
      "v_and_b32 v3, s78, v18\n"
      "v_lshlrev_b32 v4, 16, v19\n"
      "v_and_b32 v5, s78, v19\n"
      "v_pk_fma_f32 v[2:3], v[2:3], v[190:191], v[190:191] op_sel:[0,0,1] op_sel_hi:[1,0,1] neg_lo:[0,0,1] neg_hi:[0,0,1]\n"
      "v_pk_fma_f32 v[4:5], v[4:5], v[190:191], v[190:191] op_sel:[0,0,1] op_sel_hi:[1,0,1] neg_lo:[0,0,1] neg_hi:[0,0,1]\n"
      "v_cvt_pk_bf16_f32 v18, v2, v3\n"
      "v_cvt_pk_bf16_f32 v19, v4, v5\n"
      "ds_write_b128 v184, v[16:19] offset:20480\n"
      "v_lshlrev_b32 v2, 16, v20\n"
      "v_and_b32 v3, s78, v20\n"
      "v_lshlrev_b32 v4, 16, v21\n"
      "v_and_b32 v5, s78, v21\n"
      "v_pk_fma_f32 v[2:3], v[2:3], v[190:191], v[190:191] op_sel:[0,0,1] op_sel_hi:[1,0,1] neg_lo:[0,0,1] neg_hi:[0,0,1]\n"
      "v_pk_fma_f32 v[4:5], v[4:5], v[190:191], v[190:191] op_sel:[0,0,1] op_sel_hi:[1,0,1] neg_lo:[0,0,1] neg_hi:[0,0,1]\n"
      "s_waitcnt vmcnt(12)\n"
      "v_dot2c_f32_bf16 v193, v60, v188\n"
      "v_dot2c_f32_bf16 v192, v60, v60\n"
      "v_dot2c_f32_bf16 v193, v61, v188\n"
      "v_dot2c_f32_bf16 v192, v61, v61\n"
      "v_dot2c_f32_bf16 v193, v62, v188\n"
      "v_dot2c_f32_bf16 v192, v62, v62\n"
      "v_dot2c_f32_bf16 v193, v63, v188\n"
      "v_dot2c_f32_bf16 v192, v63, v63\n"
      "s_add_u32 m0, s25, 100352\n"
      "s_add_u32 s85, s85, s77\n"
      "buffer_load_dwordx4 v232, s[12:15], s85 offen lds\n"
      "v_cvt_pk_bf16_f32 v20, v2, v3\n"
      "v_cvt_pk_bf16_f32 v21, v4, v5\n"
      "v_lshlrev_b32 v2, 16, v22\n"
      "v_and_b32 v3, s78, v22\n"
      "v_lshlrev_b32 v4, 16, v23\n"
      "v_and_b32 v5, s78, v23\n"
      "v_pk_fma_f32 v[2:3], v[2:3], v[190:191], v[190:191] op_sel:[0,0,1] op_sel_hi:[1,0,1] neg_lo:[0,0,1] neg_hi:[0,0,1]\n"
      "v_pk_fma_f32 v[4:5], v[4:5], v[190:191], v[190:191] op_sel:[0,0,1] op_sel_hi:[1,0,1] neg_lo:[0,0,1] neg_hi:[0,0,1]\n"
      "v_cvt_pk_bf16_f32 v22, v2, v3\n"
      "v_cvt_pk_bf16_f32 v23, v4, v5\n"
      "ds_write_b128 v184, v[20:23] offset:30720\n"
      "v_lshlrev_b32 v2, 16, v24\n"
      "v_and_b32 v3, s78, v24\n"
      "v_lshlrev_b32 v4, 16, v25\n"
      "s_waitcnt vmcnt(12)\n"
      "v_dot2c_f32_bf16 v193, v64, v188\n"
      "v_dot2c_f32_bf16 v192, v64, v64\n"
      "v_dot2c_f32_bf16 v193, v65, v188\n"
      "v_dot2c_f32_bf16 v192, v65, v65\n"
      "v_dot2c_f32_bf16 v193, v66, v188\n"
      "v_dot2c_f32_bf16 v192, v66, v66\n"
      "v_dot2c_f32_bf16 v193, v67, v188\n"
      "v_dot2c_f32_bf16 v192, v67, v67\n"
      "s_barrier\n"
      "s_mov_b32 exec_lo, 0\n"
      "s_waitcnt vmcnt(8)\n"
      "buffer_store_dwordx2 v[250:251], v253, s[60:63], s96 offen sc1\n"
      "s_mov_b64 exec, -1\n"
      "s_add_u32 m0, s25, 104448\n"
      "s_add_u32 s85, s85, s77\n"
      "buffer_load_dwordx4 v232, s[12:15], s85 offen lds\n"
      "v_and_b32 v5, s78, v25\n"
      "v_pk_fma_f32 v[2:3], v[2:3], v[190:191], v[190:191] op_sel:[0,0,1] op_sel_hi:[1,0,1] neg_lo:[0,0,1] neg_hi:[0,0,1]\n"
      "v_pk_fma_f32 v[4:5], v[4:5], v[190:191], v[190:191] op_sel:[0,0,1] op_sel_hi:[1,0,1] neg_lo:[0,0,1] neg_hi:[0,0,1]\n"
      "v_cvt_pk_bf16_f32 v24, v2, v3\n"
      "v_cvt_pk_bf16_f32 v25, v4, v5\n"
      "v_lshlrev_b32 v2, 16, v26\n"
      "v_and_b32 v3, s78, v26\n"
      "v_lshlrev_b32 v4, 16, v27\n"
      "v_and_b32 v5, s78, v27\n"
      "v_pk_fma_f32 v[2:3], v[2:3], v[190:191], v[190:191] op_sel:[0,0,1] op_sel_hi:[1,0,1] neg_lo:[0,0,1] neg_hi:[0,0,1]\n"
      "v_pk_fma_f32 v[4:5], v[4:5], v[190:191], v[190:191] op_sel:[0,0,1] op_sel_hi:[1,0,1] neg_lo:[0,0,1] neg_hi:[0,0,1]\n"
      "v_cvt_pk_bf16_f32 v26, v2, v3\n"
      "v_cvt_pk_bf16_f32 v27, v4, v5\n"
      "ds_write_b128 v184, v[24:27] offset:40960\n"
      "v_dot2c_f32_bf16 v193, v68, v188\n"
      "v_dot2c_f32_bf16 v192, v68, v68\n"
      "v_dot2c_f32_bf16 v193, v69, v188\n"
      "v_dot2c_f32_bf16 v192, v69, v69\n"
      "v_dot2c_f32_bf16 v193, v70, v188\n"
      "v_dot2c_f32_bf16 v192, v70, v70\n"
      "v_dot2c_f32_bf16 v193, v71, v188\n"
      "v_dot2c_f32_bf16 v192, v71, v71\n"
      "s_add_u32 m0, s25, 108544\n"
      "s_add_u32 s85, s85, s77\n"
      "buffer_load_dwordx4 v232, s[12:15], s85 offen lds\n"
      "v_lshlrev_b32 v2, 16, v28\n"
      "v_and_b32 v3, s78, v28\n"
      "v_lshlrev_b32 v4, 16, v29\n"
      "v_and_b32 v5, s78, v29\n"
      "v_pk_fma_f32 v[2:3], v[2:3], v[190:191], v[190:191] op_sel:[0,0,1] op_sel_hi:[1,0,1] neg_lo:[0,0,1] neg_hi:[0,0,1]\n"
      "v_pk_fma_f32 v[4:5], v[4:5], v[190:191], v[190:191] op_sel:[0,0,1] op_sel_hi:[1,0,1] neg_lo:[0,0,1] neg_hi:[0,0,1]\n"
      "v_cvt_pk_bf16_f32 v28, v2, v3\n"
      "v_cvt_pk_bf16_f32 v29, v4, v5\n"
      "v_lshlrev_b32 v2, 16, v30\n"
      "v_and_b32 v3, s78, v30\n"
      "v_lshlrev_b32 v4, 16, v31\n"
      "v_and_b32 v5, s78, v31\n"
      "v_pk_fma_f32 v[2:3], v[2:3], v[190:191], v[190:191] op_sel:[0,0,1] op_sel_hi:[1,0,1] neg_lo:[0,0,1] neg_hi:[0,0,1]\n"
      "v_pk_fma_f32 v[4:5], v[4:5], v[190:191], v[190:191] op_sel:[0,0,1] op_sel_hi:[1,0,1] neg_lo:[0,0,1] neg_hi:[0,0,1]\n"
      "v_dot2c_f32_bf16 v193, v72, v188\n"
      "v_dot2c_f32_bf16 v192, v72, v72\n"
      "v_dot2c_f32_bf16 v193, v73, v188\n"
      "v_dot2c_f32_bf16 v192, v73, v73\n"
      "v_dot2c_f32_bf16 v193, v74, v188\n"
      "v_dot2c_f32_bf16 v192, v74, v74\n"
      "v_dot2c_f32_bf16 v193, v75, v188\n"
      "v_dot2c_f32_bf16 v192, v75, v75\n"
      "s_add_u32 m0, s25, 112640\n"
      "s_add_u32 s85, s85, s77\n"
      "buffer_load_dwordx4 v232, s[12:15], s85 offen lds\n"
      "v_cvt_pk_bf16_f32 v30, v2, v3\n"
      "v_cvt_pk_bf16_f32 v31, v4, v5\n"
      "ds_write_b128 v184, v[28:31] offset:51200\n"
      "v_lshlrev_b32 v2, 16, v32\n"
      "v_and_b32 v3, s78, v32\n"
      "v_lshlrev_b32 v4, 16, v33\n"
      "v_and_b32 v5, s78, v33\n"
      "v_pk_fma_f32 v[2:3], v[2:3], v[190:191], v[190:191] op_sel:[0,0,1] op_sel_hi:[1,0,1] neg_lo:[0,0,1] neg_hi:[0,0,1]\n"
      "v_pk_fma_f32 v[4:5], v[4:5], v[190:191], v[190:191] op_sel:[0,0,1] op_sel_hi:[1,0,1] neg_lo:[0,0,1] neg_hi:[0,0,1]\n"
      "v_cvt_pk_bf16_f32 v32, v2, v3\n"
      "v_cvt_pk_bf16_f32 v33, v4, v5\n"
      "v_lshlrev_b32 v2, 16, v34\n"
      "v_and_b32 v3, s78, v34\n"
      "v_lshlrev_b32 v4, 16, v35\n"
      "v_dot2c_f32_bf16 v193, v76, v188\n"
      "v_dot2c_f32_bf16 v192, v76, v76\n"
      "v_dot2c_f32_bf16 v193, v77, v188\n"
      "v_dot2c_f32_bf16 v192, v77, v77\n"
      "v_dot2c_f32_bf16 v193, v78, v188\n"
      "v_dot2c_f32_bf16 v192, v78, v78\n"
      "v_dot2c_f32_bf16 v193, v79, v188\n"
      "v_dot2c_f32_bf16 v192, v79, v79\n"
      "s_add_u32 m0, s25, 116736\n"
      "s_add_u32 s85, s85, s77\n"
      "buffer_load_dwordx4 v232, s[12:15], s85 offen lds\n"
      "v_and_b32 v5, s78, v35\n"
      "v_pk_fma_f32 v[2:3], v[2:3], v[190:191], v[190:191] op_sel:[0,0,1] op_sel_hi:[1,0,1] neg_lo:[0,0,1] neg_hi:[0,0,1]\n"
      "v_pk_fma_f32 v[4:5], v[4:5], v[190:191], v[190:191] op_sel:[0,0,1] op_sel_hi:[1,0,1] neg_lo:[0,0,1] neg_hi:[0,0,1]\n"
      "v_cvt_pk_bf16_f32 v34, v2, v3\n"
      "v_cvt_pk_bf16_f32 v35, v4, v5\n"
      "ds_write_b128 v240, v[32:35] offset:0\n"
      "v_lshlrev_b32 v2, 16, v36\n"
      "v_and_b32 v3, s78, v36\n"
      "v_lshlrev_b32 v4, 16, v37\n"
      "v_and_b32 v5, s78, v37\n"
      "v_pk_fma_f32 v[2:3], v[2:3], v[190:191], v[190:191] op_sel:[0,0,1] op_sel_hi:[1,0,1] neg_lo:[0,0,1] neg_hi:[0,0,1]\n"
      "v_pk_fma_f32 v[4:5], v[4:5], v[190:191], v[190:191] op_sel:[0,0,1] op_sel_hi:[1,0,1] neg_lo:[0,0,1] neg_hi:[0,0,1]\n"
      "v_cvt_pk_bf16_f32 v36, v2, v3\n"
      "v_cvt_pk_bf16_f32 v37, v4, v5\n"
      "s_waitcnt vmcnt(12)\n"
      "v_dot2c_f32_bf16 v195, v80, v188\n"
      "v_dot2c_f32_bf16 v194, v80, v80\n"
      "v_dot2c_f32_bf16 v195, v81, v188\n"
      "v_dot2c_f32_bf16 v194, v81, v81\n"
      "v_dot2c_f32_bf16 v195, v82, v188\n"
      "v_dot2c_f32_bf16 v194, v82, v82\n"
      "v_dot2c_f32_bf16 v195, v83, v188\n"
      "v_dot2c_f32_bf16 v194, v83, v83\n"
      "v_lshlrev_b32 v2, 16, v38\n"
      "v_and_b32 v3, s78, v38\n"
      "v_lshlrev_b32 v4, 16, v39\n"
      "v_and_b32 v5, s78, v39\n"
      "v_pk_fma_f32 v[2:3], v[2:3], v[190:191], v[190:191] op_sel:[0,0,1] op_sel_hi:[1,0,1] neg_lo:[0,0,1] neg_hi:[0,0,1]\n"
      "v_pk_fma_f32 v[4:5], v[4:5], v[190:191], v[190:191] op_sel:[0,0,1] op_sel_hi:[1,0,1] neg_lo:[0,0,1] neg_hi:[0,0,1]\n"
      "v_cvt_pk_bf16_f32 v38, v2, v3\n"
      "v_cvt_pk_bf16_f32 v39, v4, v5\n"
      "ds_write_b128 v240, v[36:39] offset:10240\n"
      "v_lshlrev_b32 v2, 16, v40\n"
      "v_and_b32 v3, s78, v40\n"
      "v_lshlrev_b32 v4, 16, v41\n"
      "v_and_b32 v5, s78, v41\n"
      "v_pk_fma_f32 v[2:3], v[2:3], v[190:191], v[190:191] op_sel:[0,0,1] op_sel_hi:[1,0,1] neg_lo:[0,0,1] neg_hi:[0,0,1]\n"
      "s_waitcnt vmcnt(11)\n"
      "v_dot2c_f32_bf16 v195, v84, v188\n"
      "v_dot2c_f32_bf16 v194, v84, v84\n"
      "v_dot2c_f32_bf16 v195, v85, v188\n"
      "v_dot2c_f32_bf16 v194, v85, v85\n"
      "v_dot2c_f32_bf16 v195, v86, v188\n"
      "v_dot2c_f32_bf16 v194, v86, v86\n"
      "v_dot2c_f32_bf16 v195, v87, v188\n"
      "v_dot2c_f32_bf16 v194, v87, v87\n"
      "v_pk_fma_f32 v[4:5], v[4:5], v[190:191], v[190:191] op_sel:[0,0,1] op_sel_hi:[1,0,1] neg_lo:[0,0,1] neg_hi:[0,0,1]\n"
      "v_cvt_pk_bf16_f32 v40, v2, v3\n"
      "v_cvt_pk_bf16_f32 v41, v4, v5\n"
      "v_lshlrev_b32 v2, 16, v42\n"
      "v_and_b32 v3, s78, v42\n"
      "v_lshlrev_b32 v4, 16, v43\n"
      "v_and_b32 v5, s78, v43\n"
      "v_pk_fma_f32 v[2:3], v[2:3], v[190:191], v[190:191] op_sel:[0,0,1] op_sel_hi:[1,0,1] neg_lo:[0,0,1] neg_hi:[0,0,1]\n"
      "v_pk_fma_f32 v[4:5], v[4:5], v[190:191], v[190:191] op_sel:[0,0,1] op_sel_hi:[1,0,1] neg_lo:[0,0,1] neg_hi:[0,0,1]\n"
      "v_cvt_pk_bf16_f32 v42, v2, v3\n"
      "v_cvt_pk_bf16_f32 v43, v4, v5\n"
      "ds_write_b128 v240, v[40:43] offset:20480\n"
      "s_waitcnt vmcnt(10)\n"
      "v_dot2c_f32_bf16 v195, v88, v188\n"
      "v_dot2c_f32_bf16 v194, v88, v88\n"
      "v_dot2c_f32_bf16 v195, v89, v188\n"
      "v_dot2c_f32_bf16 v194, v89, v89\n"
      "v_dot2c_f32_bf16 v195, v90, v188\n"
      "v_dot2c_f32_bf16 v194, v90, v90\n"
      "v_dot2c_f32_bf16 v195, v91, v188\n"
      "v_dot2c_f32_bf16 v194, v91, v91\n"
      "s_waitcnt vmcnt(9)\n"
      "v_dot2c_f32_bf16 v195, v92, v188\n"
      "v_dot2c_f32_bf16 v194, v92, v92\n"
      "v_dot2c_f32_bf16 v195, v93, v188\n"
      "v_dot2c_f32_bf16 v194, v93, v93\n"
      "v_dot2c_f32_bf16 v195, v94, v188\n"
      "v_dot2c_f32_bf16 v194, v94, v94\n"
      "v_dot2c_f32_bf16 v195, v95, v188\n"
      "v_dot2c_f32_bf16 v194, v95, v95\n"
      "s_waitcnt vmcnt(8)\n"
      "v_dot2c_f32_bf16 v195, v96, v188\n"
      "v_dot2c_f32_bf16 v194, v96, v96\n"
      "v_dot2c_f32_bf16 v195, v97, v188\n"
      "v_dot2c_f32_bf16 v194, v97, v97\n"
      "v_dot2c_f32_bf16 v195, v98, v188\n"
      "v_dot2c_f32_bf16 v194, v98, v98\n"
      "v_dot2c_f32_bf16 v195, v99, v188\n"
      "v_dot2c_f32_bf16 v194, v99, v99\n"
      "v_add_f32_dpp v192, v192, v192 quad_perm:[1,0,3,2] row_mask:0xf bank_mask:0xf\n"
      "v_add_f32_dpp v193, v193, v193 quad_perm:[1,0,3,2] row_mask:0xf bank_mask:0xf\n"
      "s_nop 0\n"
      "v_add_f32_dpp v194, v194, v194 quad_perm:[1,0,3,2] row_mask:0xf bank_mask:0xf\n"
      "v_add_f32_dpp v195, v195, v195 quad_perm:[1,0,3,2] row_mask:0xf bank_mask:0xf\n"
      "v_add_f32_dpp v192, v192, v192 quad_perm:[2,3,0,1] row_mask:0xf bank_mask:0xf\n"
      "v_add_f32_dpp v193, v193, v193 quad_perm:[2,3,0,1] row_mask:0xf bank_mask:0xf\n"
      "v_add_f32_dpp v194, v194, v194 quad_perm:[2,3,0,1] row_mask:0xf bank_mask:0xf\n"
      "v_add_f32_dpp v195, v195, v195 quad_perm:[2,3,0,1] row_mask:0xf bank_mask:0xf\n"
      "v_add_f32_dpp v192, v192, v192 row_half_mirror row_mask:0xf bank_mask:0xf\n"
      "v_add_f32_dpp v193, v193, v193 row_half_mirror row_mask:0xf bank_mask:0xf\n"
      "v_add_f32_dpp v194, v194, v194 row_half_mirror row_mask:0xf bank_mask:0xf\n"
      "v_add_f32_dpp v195, v195, v195 row_half_mirror row_mask:0xf bank_mask:0xf\n"
      "s_nop 0\n"
      "v_add_f32_dpp v194, v194, v194 row_mirror row_mask:0xf bank_mask:0xf\n"
      "v_add_f32_dpp v195, v195, v195 row_mirror row_mask:0xf bank_mask:0xf\n"
      "v_mul_f32 v193, 0x3ae38e39, v193\n"
      "v_mul_f32 v195, 0x3ae38e39, v195\n"
      "v_mul_f32 v192, 0x3ae38e39, v192\n"
      "v_mul_f32 v194, 0x3ae38e39, v194\n"
      "v_fma_f32 v192, -v193, v193, v192\n"
      "v_fma_f32 v194, -v195, v195, v194\n"
      "v_max_f32 v192, 0, v192\n"
      "v_max_f32 v194, 0, v194\n"
      "v_add_f32 v192, 0x3727c5ac, v192\n"
      "v_add_f32 v194, 0x3727c5ac, v194\n"
      "v_rsq_f32 v192, v192\n"
      "v_rsq_f32 v194, v194\n"
      "v_mul_f32 v193, v193, v192\n"
      "v_mul_f32 v195, v195, v194\n"
      "v_lshlrev_b32 v2, 16, v44\n"
      "v_and_b32 v3, s78, v44\n"
      "v_lshlrev_b32 v4, 16, v45\n"
      "v_and_b32 v5, s78, v45\n"
      "v_pk_fma_f32 v[2:3], v[2:3], v[192:193], v[192:193] op_sel:[0,0,1] op_sel_hi:[1,0,1] neg_lo:[0,0,1] neg_hi:[0,0,1]\n"
      "v_pk_fma_f32 v[4:5], v[4:5], v[192:193], v[192:193] op_sel:[0,0,1] op_sel_hi:[1,0,1] neg_lo:[0,0,1] neg_hi:[0,0,1]\n"
      "v_cvt_pk_bf16_f32 v44, v2, v3\n"
      "v_cvt_pk_bf16_f32 v45, v4, v5\n"
      "v_lshlrev_b32 v2, 16, v46\n"
      "v_and_b32 v3, s78, v46\n"
      "v_lshlrev_b32 v4, 16, v47\n"
      "v_and_b32 v5, s78, v47\n"
      "v_pk_fma_f32 v[2:3], v[2:3], v[192:193], v[192:193] op_sel:[0,0,1] op_sel_hi:[1,0,1] neg_lo:[0,0,1] neg_hi:[0,0,1]\n"
      "v_pk_fma_f32 v[4:5], v[4:5], v[192:193], v[192:193] op_sel:[0,0,1] op_sel_hi:[1,0,1] neg_lo:[0,0,1] neg_hi:[0,0,1]\n"
      "v_cvt_pk_bf16_f32 v46, v2, v3\n"
      "v_cvt_pk_bf16_f32 v47, v4, v5\n"
      "ds_write_b128 v185, v[44:47] offset:0\n"
      "v_lshlrev_b32 v2, 16, v80\n"
      "v_and_b32 v3, s78, v80\n"
      "v_lshlrev_b32 v4, 16, v81\n"
      "v_and_b32 v5, s78, v81\n"
      "v_pk_fma_f32 v[2:3], v[2:3], v[194:195], v[194:195] op_sel:[0,0,1] op_sel_hi:[1,0,1] neg_lo:[0,0,1] neg_hi:[0,0,1]\n"
      "v_pk_fma_f32 v[4:5], v[4:5], v[194:195], v[194:195] op_sel:[0,0,1] op_sel_hi:[1,0,1] neg_lo:[0,0,1] neg_hi:[0,0,1]\n"
      "v_cvt_pk_bf16_f32 v80, v2, v3\n"
      "v_cvt_pk_bf16_f32 v81, v4, v5\n"
      "v_lshlrev_b32 v2, 16, v82\n"
      "v_and_b32 v3, s78, v82\n"
      "v_lshlrev_b32 v4, 16, v83\n"
      "v_and_b32 v5, s78, v83\n"
      "v_pk_fma_f32 v[2:3], v[2:3], v[194:195], v[194:195] op_sel:[0,0,1] op_sel_hi:[1,0,1] neg_lo:[0,0,1] neg_hi:[0,0,1]\n"
      "v_pk_fma_f32 v[4:5], v[4:5], v[194:195], v[194:195] op_sel:[0,0,1] op_sel_hi:[1,0,1] neg_lo:[0,0,1] neg_hi:[0,0,1]\n"
      "v_cvt_pk_bf16_f32 v82, v2, v3\n"
      "v_cvt_pk_bf16_f32 v83, v4, v5\n"
      "ds_write_b128 v186, v[80:83] offset:0\n"
      "v_lshlrev_b32 v2, 16, v48\n"
      "v_and_b32 v3, s78, v48\n"
      "v_lshlrev_b32 v4, 16, v49\n"
      "v_and_b32 v5, s78, v49\n"
      "v_pk_fma_f32 v[2:3], v[2:3], v[192:193], v[192:193] op_sel:[0,0,1] op_sel_hi:[1,0,1] neg_lo:[0,0,1] neg_hi:[0,0,1]\n"
      "v_pk_fma_f32 v[4:5], v[4:5], v[192:193], v[192:193] op_sel:[0,0,1] op_sel_hi:[1,0,1] neg_lo:[0,0,1] neg_hi:[0,0,1]\n"
      "v_cvt_pk_bf16_f32 v48, v2, v3\n"
      "v_cvt_pk_bf16_f32 v49, v4, v5\n"
      "v_lshlrev_b32 v2, 16, v50\n"
      "v_and_b32 v3, s78, v50\n"
      "v_lshlrev_b32 v4, 16, v51\n"
      "v_and_b32 v5, s78, v51\n"
      "v_pk_fma_f32 v[2:3], v[2:3], v[192:193], v[192:193] op_sel:[0,0,1] op_sel_hi:[1,0,1] neg_lo:[0,0,1] neg_hi:[0,0,1]\n"
      "v_pk_fma_f32 v[4:5], v[4:5], v[192:193], v[192:193] op_sel:[0,0,1] op_sel_hi:[1,0,1] neg_lo:[0,0,1] neg_hi:[0,0,1]\n"
      "v_cvt_pk_bf16_f32 v50, v2, v3\n"
      "v_cvt_pk_bf16_f32 v51, v4, v5\n"
      "ds_write_b128 v185, v[48:51] offset:10240\n"
      "v_lshlrev_b32 v2, 16, v52\n"
      "v_and_b32 v3, s78, v52\n"
      "v_lshlrev_b32 v4, 16, v53\n"
      "v_and_b32 v5, s78, v53\n"
      "v_pk_fma_f32 v[2:3], v[2:3], v[192:193], v[192:193] op_sel:[0,0,1] op_sel_hi:[1,0,1] neg_lo:[0,0,1] neg_hi:[0,0,1]\n"
      "v_pk_fma_f32 v[4:5], v[4:5], v[192:193], v[192:193] op_sel:[0,0,1] op_sel_hi:[1,0,1] neg_lo:[0,0,1] neg_hi:[0,0,1]\n"
      "v_cvt_pk_bf16_f32 v52, v2, v3\n"
      "v_cvt_pk_bf16_f32 v53, v4, v5\n"
      "v_lshlrev_b32 v2, 16, v54\n"
      "v_and_b32 v3, s78, v54\n"
      "v_lshlrev_b32 v4, 16, v55\n"
      "v_and_b32 v5, s78, v55\n"
      "v_pk_fma_f32 v[2:3], v[2:3], v[192:193], v[192:193] op_sel:[0,0,1] op_sel_hi:[1,0,1] neg_lo:[0,0,1] neg_hi:[0,0,1]\n"
      "v_pk_fma_f32 v[4:5], v[4:5], v[192:193], v[192:193] op_sel:[0,0,1] op_sel_hi:[1,0,1] neg_lo:[0,0,1] neg_hi:[0,0,1]\n"
      "v_cvt_pk_bf16_f32 v54, v2, v3\n"
      "v_cvt_pk_bf16_f32 v55, v4, v5\n"
      "ds_write_b128 v185, v[52:55] offset:20480\n"
      "v_lshlrev_b32 v2, 16, v84\n"
      "v_and_b32 v3, s78, v84\n"
      "v_lshlrev_b32 v4, 16, v85\n"
      "v_and_b32 v5, s78, v85\n"
      "v_pk_fma_f32 v[2:3], v[2:3], v[194:195], v[194:195] op_sel:[0,0,1] op_sel_hi:[1,0,1] neg_lo:[0,0,1] neg_hi:[0,0,1]\n"
      "v_pk_fma_f32 v[4:5], v[4:5], v[194:195], v[194:195] op_sel:[0,0,1] op_sel_hi:[1,0,1] neg_lo:[0,0,1] neg_hi:[0,0,1]\n"
      "v_cvt_pk_bf16_f32 v84, v2, v3\n"
      "v_cvt_pk_bf16_f32 v85, v4, v5\n"
      "v_lshlrev_b32 v2, 16, v86\n"
      "v_and_b32 v3, s78, v86\n"
      "v_lshlrev_b32 v4, 16, v87\n"
      "v_and_b32 v5, s78, v87\n"
      "v_pk_fma_f32 v[2:3], v[2:3], v[194:195], v[194:195] op_sel:[0,0,1] op_sel_hi:[1,0,1] neg_lo:[0,0,1] neg_hi:[0,0,1]\n"
      "v_pk_fma_f32 v[4:5], v[4:5], v[194:195], v[194:195] op_sel:[0,0,1] op_sel_hi:[1,0,1] neg_lo:[0,0,1] neg_hi:[0,0,1]\n"
      "v_cvt_pk_bf16_f32 v86, v2, v3\n"
      "v_cvt_pk_bf16_f32 v87, v4, v5\n"
      "ds_write_b128 v186, v[84:87] offset:20480\n"
      "v_lshlrev_b32 v2, 16, v56\n"
      "v_and_b32 v3, s78, v56\n"
      "v_lshlrev_b32 v4, 16, v57\n"
      "v_and_b32 v5, s78, v57\n"
      "v_pk_fma_f32 v[2:3], v[2:3], v[192:193], v[192:193] op_sel:[0,0,1] op_sel_hi:[1,0,1] neg_lo:[0,0,1] neg_hi:[0,0,1]\n"
      "v_pk_fma_f32 v[4:5], v[4:5], v[192:193], v[192:193] op_sel:[0,0,1] op_sel_hi:[1,0,1] neg_lo:[0,0,1] neg_hi:[0,0,1]\n"
      "v_cvt_pk_bf16_f32 v56, v2, v3\n"
      "v_cvt_pk_bf16_f32 v57, v4, v5\n"
      "v_lshlrev_b32 v2, 16, v58\n"
      "v_and_b32 v3, s78, v58\n"
      "v_lshlrev_b32 v4, 16, v59\n"
      "v_and_b32 v5, s78, v59\n"
      "v_pk_fma_f32 v[2:3], v[2:3], v[192:193], v[192:193] op_sel:[0,0,1] op_sel_hi:[1,0,1] neg_lo:[0,0,1] neg_hi:[0,0,1]\n"
      "v_pk_fma_f32 v[4:5], v[4:5], v[192:193], v[192:193] op_sel:[0,0,1] op_sel_hi:[1,0,1] neg_lo:[0,0,1] neg_hi:[0,0,1]\n"
      "v_cvt_pk_bf16_f32 v58, v2, v3\n"
      "v_cvt_pk_bf16_f32 v59, v4, v5\n"
      "ds_write_b128 v185, v[56:59] offset:30720\n"
      "v_lshlrev_b32 v2, 16, v60\n"
      "v_and_b32 v3, s78, v60\n"
      "v_lshlrev_b32 v4, 16, v61\n"
      "v_and_b32 v5, s78, v61\n"
      "v_pk_fma_f32 v[2:3], v[2:3], v[192:193], v[192:193] op_sel:[0,0,1] op_sel_hi:[1,0,1] neg_lo:[0,0,1] neg_hi:[0,0,1]\n"
      "v_pk_fma_f32 v[4:5], v[4:5], v[192:193], v[192:193] op_sel:[0,0,1] op_sel_hi:[1,0,1] neg_lo:[0,0,1] neg_hi:[0,0,1]\n"
      "v_cvt_pk_bf16_f32 v60, v2, v3\n"
      "v_cvt_pk_bf16_f32 v61, v4, v5\n"
      "v_lshlrev_b32 v2, 16, v62\n"
      "v_and_b32 v3, s78, v62\n"
      "v_lshlrev_b32 v4, 16, v63\n"
      "v_and_b32 v5, s78, v63\n"
      "v_pk_fma_f32 v[2:3], v[2:3], v[192:193], v[192:193] op_sel:[0,0,1] op_sel_hi:[1,0,1] neg_lo:[0,0,1] neg_hi:[0,0,1]\n"
      "v_pk_fma_f32 v[4:5], v[4:5], v[192:193], v[192:193] op_sel:[0,0,1] op_sel_hi:[1,0,1] neg_lo:[0,0,1] neg_hi:[0,0,1]\n"
      "v_cvt_pk_bf16_f32 v62, v2, v3\n"
      "v_cvt_pk_bf16_f32 v63, v4, v5\n"
      "ds_write_b128 v185, v[60:63] offset:40960\n"
      "v_lshlrev_b32 v2, 16, v88\n"
      "v_and_b32 v3, s78, v88\n"
      "v_lshlrev_b32 v4, 16, v89\n"
      "v_and_b32 v5, s78, v89\n"
      "v_pk_fma_f32 v[2:3], v[2:3], v[194:195], v[194:195] op_sel:[0,0,1] op_sel_hi:[1,0,1] neg_lo:[0,0,1] neg_hi:[0,0,1]\n"
      "v_pk_fma_f32 v[4:5], v[4:5], v[194:195], v[194:195] op_sel:[0,0,1] op_sel_hi:[1,0,1] neg_lo:[0,0,1] neg_hi:[0,0,1]\n"
      "v_cvt_pk_bf16_f32 v88, v2, v3\n"
      "v_cvt_pk_bf16_f32 v89, v4, v5\n"
      "v_lshlrev_b32 v2, 16, v90\n"
      "v_and_b32 v3, s78, v90\n"
      "v_lshlrev_b32 v4, 16, v91\n"
      "v_and_b32 v5, s78, v91\n"
      "v_pk_fma_f32 v[2:3], v[2:3], v[194:195], v[194:195] op_sel:[0,0,1] op_sel_hi:[1,0,1] neg_lo:[0,0,1] neg_hi:[0,0,1]\n"
      "v_pk_fma_f32 v[4:5], v[4:5], v[194:195], v[194:195] op_sel:[0,0,1] op_sel_hi:[1,0,1] neg_lo:[0,0,1] neg_hi:[0,0,1]\n"
      "v_cvt_pk_bf16_f32 v90, v2, v3\n"
      "v_cvt_pk_bf16_f32 v91, v4, v5\n"
      "ds_write_b128 v186, v[88:91] offset:40960\n"
      "v_lshlrev_b32 v2, 16, v64\n"
      "v_and_b32 v3, s78, v64\n"
      "v_lshlrev_b32 v4, 16, v65\n"
      "v_and_b32 v5, s78, v65\n"
      "v_pk_fma_f32 v[2:3], v[2:3], v[192:193], v[192:193] op_sel:[0,0,1] op_sel_hi:[1,0,1] neg_lo:[0,0,1] neg_hi:[0,0,1]\n"
      "v_pk_fma_f32 v[4:5], v[4:5], v[192:193], v[192:193] op_sel:[0,0,1] op_sel_hi:[1,0,1] neg_lo:[0,0,1] neg_hi:[0,0,1]\n"
      "v_cvt_pk_bf16_f32 v64, v2, v3\n"
      "v_cvt_pk_bf16_f32 v65, v4, v5\n"
      "v_lshlrev_b32 v2, 16, v66\n"
      "v_and_b32 v3, s78, v66\n"
      "v_lshlrev_b32 v4, 16, v67\n"
      "v_and_b32 v5, s78, v67\n"
      "v_pk_fma_f32 v[2:3], v[2:3], v[192:193], v[192:193] op_sel:[0,0,1] op_sel_hi:[1,0,1] neg_lo:[0,0,1] neg_hi:[0,0,1]\n"
      "v_pk_fma_f32 v[4:5], v[4:5], v[192:193], v[192:193] op_sel:[0,0,1] op_sel_hi:[1,0,1] neg_lo:[0,0,1] neg_hi:[0,0,1]\n"
      "v_cvt_pk_bf16_f32 v66, v2, v3\n"
      "v_cvt_pk_bf16_f32 v67, v4, v5\n"
      "ds_write_b128 v185, v[64:67] offset:51200\n"
      "v_lshlrev_b32 v2, 16, v68\n"
      "v_and_b32 v3, s78, v68\n"
      "v_lshlrev_b32 v4, 16, v69\n"
      "v_and_b32 v5, s78, v69\n"
      "v_pk_fma_f32 v[2:3], v[2:3], v[192:193], v[192:193] op_sel:[0,0,1] op_sel_hi:[1,0,1] neg_lo:[0,0,1] neg_hi:[0,0,1]\n"
      "v_pk_fma_f32 v[4:5], v[4:5], v[192:193], v[192:193] op_sel:[0,0,1] op_sel_hi:[1,0,1] neg_lo:[0,0,1] neg_hi:[0,0,1]\n"
      "v_cvt_pk_bf16_f32 v68, v2, v3\n"
      "v_cvt_pk_bf16_f32 v69, v4, v5\n"
      "v_lshlrev_b32 v2, 16, v70\n"
      "v_and_b32 v3, s78, v70\n"
      "v_lshlrev_b32 v4, 16, v71\n"
      "v_and_b32 v5, s78, v71\n"
      "v_pk_fma_f32 v[2:3], v[2:3], v[192:193], v[192:193] op_sel:[0,0,1] op_sel_hi:[1,0,1] neg_lo:[0,0,1] neg_hi:[0,0,1]\n"
      "v_pk_fma_f32 v[4:5], v[4:5], v[192:193], v[192:193] op_sel:[0,0,1] op_sel_hi:[1,0,1] neg_lo:[0,0,1] neg_hi:[0,0,1]\n"
      "v_cvt_pk_bf16_f32 v70, v2, v3\n"
      "v_cvt_pk_bf16_f32 v71, v4, v5\n"
      "ds_write_b128 v241, v[68:71] offset:0\n"
      "v_lshlrev_b32 v2, 16, v92\n"
      "v_and_b32 v3, s78, v92\n"
      "v_lshlrev_b32 v4, 16, v93\n"
      "v_and_b32 v5, s78, v93\n"
      "v_pk_fma_f32 v[2:3], v[2:3], v[194:195], v[194:195] op_sel:[0,0,1] op_sel_hi:[1,0,1] neg_lo:[0,0,1] neg_hi:[0,0,1]\n"
      "v_pk_fma_f32 v[4:5], v[4:5], v[194:195], v[194:195] op_sel:[0,0,1] op_sel_hi:[1,0,1] neg_lo:[0,0,1] neg_hi:[0,0,1]\n"
      "v_cvt_pk_bf16_f32 v92, v2, v3\n"
      "v_cvt_pk_bf16_f32 v93, v4, v5\n"
      "v_lshlrev_b32 v2, 16, v94\n"
      "v_and_b32 v3, s78, v94\n"
      "v_lshlrev_b32 v4, 16, v95\n"
      "v_and_b32 v5, s78, v95\n"
      "v_pk_fma_f32 v[2:3], v[2:3], v[194:195], v[194:195] op_sel:[0,0,1] op_sel_hi:[1,0,1] neg_lo:[0,0,1] neg_hi:[0,0,1]\n"
      "v_pk_fma_f32 v[4:5], v[4:5], v[194:195], v[194:195] op_sel:[0,0,1] op_sel_hi:[1,0,1] neg_lo:[0,0,1] neg_hi:[0,0,1]\n"
      "s_mov_b32 exec_lo, 0\n"
      "buffer_load_dwordx2 v[246:247], v252, s[60:63], s96 offen sc1\n"
      "s_mov_b64 exec, -1\n"
      "s_add_u32 m0, s25, 120832\n"
      "s_add_u32 s85, s85, s77\n"
      "buffer_load_dwordx4 v232, s[12:15], s85 offen lds\n"
      "s_add_u32 m0, s25, 124928\n"
      "s_add_u32 s85, s85, s77\n"
      "buffer_load_dwordx4 v232, s[12:15], s85 offen lds\n"
      "s_mul_i32 s27, s26, 82944\n"
      "s_add_u32 s27, s27, 0\n"
      "s_bfm_b64 exec, 18, 0\n"
      "buffer_load_dword v245, v243, s[48:51], s27 offen\n"
      "s_mov_b64 exec, -1\n"
      "v_cvt_pk_bf16_f32 v94, v2, v3\n"
      "v_cvt_pk_bf16_f32 v95, v4, v5\n"
      "ds_write_b128 v186, v[92:95] offset:61440\n"
      "v_lshlrev_b32 v2, 16, v72\n"
      "v_and_b32 v3, s78, v72\n"
      "v_lshlrev_b32 v4, 16, v73\n"
      "v_and_b32 v5, s78, v73\n"
      "v_pk_fma_f32 v[2:3], v[2:3], v[192:193], v[192:193] op_sel:[0,0,1] op_sel_hi:[1,0,1] neg_lo:[0,0,1] neg_hi:[0,0,1]\n"
      "v_pk_fma_f32 v[4:5], v[4:5], v[192:193], v[192:193] op_sel:[0,0,1] op_sel_hi:[1,0,1] neg_lo:[0,0,1] neg_hi:[0,0,1]\n"
      "v_cvt_pk_bf16_f32 v72, v2, v3\n"
      "v_cvt_pk_bf16_f32 v73, v4, v5\n"
      "v_lshlrev_b32 v2, 16, v74\n"
      "v_and_b32 v3, s78, v74\n"
      "v_lshlrev_b32 v4, 16, v75\n"
      "v_and_b32 v5, s78, v75\n"
      "v_pk_fma_f32 v[2:3], v[2:3], v[192:193], v[192:193] op_sel:[0,0,1] op_sel_hi:[1,0,1] neg_lo:[0,0,1] neg_hi:[0,0,1]\n"
      "v_pk_fma_f32 v[4:5], v[4:5], v[192:193], v[192:193] op_sel:[0,0,1] op_sel_hi:[1,0,1] neg_lo:[0,0,1] neg_hi:[0,0,1]\n"
      "v_cvt_pk_bf16_f32 v74, v2, v3\n"
      "v_cvt_pk_bf16_f32 v75, v4, v5\n"
      "ds_write_b128 v241, v[72:75] offset:10240\n"
      "v_lshlrev_b32 v2, 16, v76\n"
      "v_and_b32 v3, s78, v76\n"
      "v_lshlrev_b32 v4, 16, v77\n"
      "v_and_b32 v5, s78, v77\n"
      "v_pk_fma_f32 v[2:3], v[2:3], v[192:193], v[192:193] op_sel:[0,0,1] op_sel_hi:[1,0,1] neg_lo:[0,0,1] neg_hi:[0,0,1]\n"
      "v_pk_fma_f32 v[4:5], v[4:5], v[192:193], v[192:193] op_sel:[0,0,1] op_sel_hi:[1,0,1] neg_lo:[0,0,1] neg_hi:[0,0,1]\n"
      "v_cvt_pk_bf16_f32 v76, v2, v3\n"
      "v_cvt_pk_bf16_f32 v77, v4, v5\n"
      "v_lshlrev_b32 v2, 16, v78\n"
      "v_and_b32 v3, s78, v78\n"
      "v_lshlrev_b32 v4, 16, v79\n"
      "v_and_b32 v5, s78, v79\n"
      "v_pk_fma_f32 v[2:3], v[2:3], v[192:193], v[192:193] op_sel:[0,0,1] op_sel_hi:[1,0,1] neg_lo:[0,0,1] neg_hi:[0,0,1]\n"
      "v_pk_fma_f32 v[4:5], v[4:5], v[192:193], v[192:193] op_sel:[0,0,1] op_sel_hi:[1,0,1] neg_lo:[0,0,1] neg_hi:[0,0,1]\n"
      "v_cvt_pk_bf16_f32 v78, v2, v3\n"
      "v_cvt_pk_bf16_f32 v79, v4, v5\n"
      "ds_write_b128 v241, v[76:79] offset:20480\n"
      "v_lshlrev_b32 v2, 16, v96\n"
      "v_and_b32 v3, s78, v96\n"
      "v_lshlrev_b32 v4, 16, v97\n"
      "v_and_b32 v5, s78, v97\n"
      "v_pk_fma_f32 v[2:3], v[2:3], v[194:195], v[194:195] op_sel:[0,0,1] op_sel_hi:[1,0,1] neg_lo:[0,0,1] neg_hi:[0,0,1]\n"
      "v_pk_fma_f32 v[4:5], v[4:5], v[194:195], v[194:195] op_sel:[0,0,1] op_sel_hi:[1,0,1] neg_lo:[0,0,1] neg_hi:[0,0,1]\n"
      "v_cvt_pk_bf16_f32 v96, v2, v3\n"
      "v_cvt_pk_bf16_f32 v97, v4, v5\n"
      "v_lshlrev_b32 v2, 16, v98\n"
      "v_and_b32 v3, s78, v98\n"
      "v_lshlrev_b32 v4, 16, v99\n"
      "v_and_b32 v5, s78, v99\n"
      "v_pk_fma_f32 v[2:3], v[2:3], v[194:195], v[194:195] op_sel:[0,0,1] op_sel_hi:[1,0,1] neg_lo:[0,0,1] neg_hi:[0,0,1]\n"
      "v_pk_fma_f32 v[4:5], v[4:5], v[194:195], v[194:195] op_sel:[0,0,1] op_sel_hi:[1,0,1] neg_lo:[0,0,1] neg_hi:[0,0,1]\n"
      "v_cvt_pk_bf16_f32 v98, v2, v3\n"
      "v_cvt_pk_bf16_f32 v99, v4, v5\n"
      "s_mov_b32 exec_lo, 0x00ff00ff\n"
      "s_mov_b32 exec_hi, 0x00ff00ff\n"
      "ds_write_b128 v187, v[96:99]\n"
      "s_mov_b64 exec, -1\n"
      "s_cmp_eq_u32 s64, 3\n"
      "s_cbranch_scc0 NP6_%=\n"
      "s_mov_b32 exec_lo, 0\n"
      "s_waitcnt vmcnt(3)\n"
      "s_branch PC9_%=\n"
      "PL7_%=:\n"
      "buffer_load_dwordx2 v[246:247], v252, s[60:63], s96 offen sc1\n"
      "s_waitcnt vmcnt(0)\n"
      "PC9_%=:\n"
      "v_cmp_ne_u32 vcc, v246, v251\n"
      "s_nop 1\n"
      "s_cbranch_vccz NE8_%=\n"
      "v_mov_b32 v251, v246\n"
      "buffer_store_dwordx2 v[250:251], v253, s[60:63], s96 offen sc1\n"
      "NE8_%=:\n"
      "v_cmp_ne_u32 vcc, v247, v250\n"
      "s_nop 1\n"
      "s_and_b32 vcc_hi, vcc_hi, 3871\n"
      "s_cbranch_scc0 NP6_%=\n"
      "s_cmp_eq_u32 s87, 0\n"
      "s_cbranch_scc1 NP6_%=\n"
      "s_sub_u32 s87, s87, 1\n"
      "s_branch PL7_%=\n"
      "NP6_%=:\n"
      "s_mov_b64 exec, -1\n"
      "s_waitcnt lgkmcnt(0)\n"
      "s_barrier\n"
      "s_add_u32 s76, s31, 0\n"
      "buffer_load_dwordx4 a[128:131], v234, s[60:63], s76 offen offset:2048 sc1\n"
      "s_add_u32 s76, s31, 4096\n"
      "buffer_load_dwordx4 a[144:147], v234, s[60:63], s76 offen offset:2048 sc1\n"
      "s_add_u32 s76, s31, 0\n"
      "buffer_load_dwordx4 a[120:123], v234, s[60:63], s76 offen offset:0 sc1\n"
      "s_add_u32 s76, s31, 4096\n"
      "buffer_load_dwordx4 a[136:139], v234, s[60:63], s76 offen offset:0 sc1\n"
      "s_add_u32 s76, s31, 0\n"
      "buffer_load_dwordx4 a[124:127], v234, s[60:63], s76 offen offset:1024 sc1\n"
      "s_add_u32 s76, s31, 4096\n"
      "buffer_load_dwordx4 a[140:143], v234, s[60:63], s76 offen offset:1024 sc1\n"
      "s_add_u32 s76, s31, 0\n"
      "buffer_load_dwordx4 a[132:135], v234, s[60:63], s76 offen offset:3072 sc1\n"
      "s_add_u32 s76, s31, 4096\n"
      "buffer_load_dwordx4 a[148:151], v234, s[60:63], s76 offen offset:3072 sc1\n"
      "s_add_u32 s76, s31, 8192\n"
      "buffer_load_dwordx4 a[160:163], v234, s[60:63], s76 offen offset:2048 sc1\n"
      "s_add_u32 s76, s31, 12288\n"
      "buffer_load_dwordx4 a[176:179], v234, s[60:63], s76 offen offset:2048 sc1\n"
      "s_add_u32 s76, s31, 8192\n"
      "buffer_load_dwordx4 a[152:155], v234, s[60:63], s76 offen offset:0 sc1\n"
      "ds_read_b128 a[0:3], v176 offset:0\n"
      "ds_read_b128 a[20:23], v177 offset:5120\n"
      "ds_read_b128 a[4:7], v176 offset:1024\n"
      "ds_read_b128 a[24:27], v177 offset:6144\n"
      "ds_read_b128 a[16:19], v182 offset:4096\n"
      "ds_read_b128 a[36:39], v183 offset:9216\n"
      "ds_read_b128 a[8:11], v178 offset:0\n"
      "ds_read_b128 a[28:31], v179 offset:5120\n"
      "ds_read_b128 a[12:15], v178 offset:1024\n"
      "ds_read_b128 a[32:35], v179 offset:6144\n"
      "s_waitcnt vmcnt(10)\n"
      "s_waitcnt lgkmcnt(9)\n"
      "v_mfma_f32_16x16x32_bf16 v[140:143], a[128:131], a[0:3], 0\n"
      "s_add_u32 s76, s31, 12288\n"
      "s_waitcnt vmcnt(9)\n"
      "s_waitcnt lgkmcnt(8)\n"
      "v_mfma_f32_16x16x32_bf16 v[140:143], a[144:147], a[20:23], v[140:143]\n"
      "buffer_load_dwordx4 a[168:171], v234, s[60:63], s76 offen offset:0 sc1\n"
      "s_waitcnt lgkmcnt(7)\n"
      "v_mfma_f32_16x16x32_bf16 v[144:147], a[128:131], a[4:7], 0\n"
      "ds_read_b128 a[40:43], v176 offset:10240\n"
      "s_waitcnt lgkmcnt(7)\n"
      "v_mfma_f32_16x16x32_bf16 v[144:147], a[144:147], a[24:27], v[144:147]\n"
      "s_waitcnt vmcnt(8)\n"
      "ds_read_b128 a[60:63], v177 offset:15360\n"
      "s_waitcnt lgkmcnt(7)\n"
      "v_mfma_f32_16x16x32_bf16 v[136:139], a[128:131], a[16:19], 0\n"
      "s_add_u32 s76, s31, 8192\n"
      "s_waitcnt lgkmcnt(6)\n"
      "v_mfma_f32_16x16x32_bf16 v[136:139], a[144:147], a[36:39], v[136:139]\n"
      "buffer_load_dwordx4 a[156:159], v234, s[60:63], s76 offen offset:1024 sc1\n"
      "v_mfma_f32_16x16x32_bf16 v[100:103], a[120:123], a[0:3], 0\n"
      "ds_read_b128 a[44:47], v176 offset:11264\n"
      "v_mfma_f32_16x16x32_bf16 v[100:103], a[136:139], a[20:23], v[100:103]\n"
      "ds_read_b128 a[64:67], v177 offset:16384\n"
      "v_mfma_f32_16x16x32_bf16 v[104:107], a[120:123], a[4:7], 0\n"
      "s_add_u32 s76, s31, 12288\n"
      "v_mfma_f32_16x16x32_bf16 v[104:107], a[136:139], a[24:27], v[104:107]\n"
      "buffer_load_dwordx4 a[172:175], v234, s[60:63], s76 offen offset:1024 sc1\n"
      "s_waitcnt lgkmcnt(7)\n"
      "v_mfma_f32_16x16x32_bf16 v[108:111], a[120:123], a[8:11], 0\n"
      "ds_read_b128 a[56:59], v182 offset:14336\n"
      "s_waitcnt lgkmcnt(7)\n"
      "v_mfma_f32_16x16x32_bf16 v[108:111], a[136:139], a[28:31], v[108:111]\n"
      "s_waitcnt vmcnt(8)\n"
      "ds_read_b128 a[76:79], v183 offset:19456\n"
      "s_waitcnt lgkmcnt(7)\n"
      "v_mfma_f32_16x16x32_bf16 v[112:115], a[120:123], a[12:15], 0\n"
      "s_add_u32 s76, s31, 8192\n"
      "s_waitcnt lgkmcnt(6)\n"
      "v_mfma_f32_16x16x32_bf16 v[112:115], a[136:139], a[32:35], v[112:115]\n"
      "buffer_load_dwordx4 a[164:167], v234, s[60:63], s76 offen offset:3072 sc1\n"
      "v_mfma_f32_16x16x32_bf16 v[120:123], a[124:127], a[0:3], 0\n"
      "ds_read_b128 a[48:51], v178 offset:10240\n"
      "v_mfma_f32_16x16x32_bf16 v[120:123], a[140:143], a[20:23], v[120:123]\n"
      "v_mfma_f32_16x16x32_bf16 v[124:127], a[124:127], a[4:7], 0\n"
      "s_add_u32 s76, s31, 12288\n"
      "ds_read_b128 a[68:71], v179 offset:15360\n"
      "v_mfma_f32_16x16x32_bf16 v[124:127], a[140:143], a[24:27], v[124:127]\n"
      "buffer_load_dwordx4 a[180:183], v234, s[60:63], s76 offen offset:3072 sc1\n"
      "v_mfma_f32_16x16x32_bf16 v[128:131], a[124:127], a[8:11], 0\n"
      "ds_read_b128 a[52:55], v178 offset:11264\n"
      "v_mfma_f32_16x16x32_bf16 v[128:131], a[140:143], a[28:31], v[128:131]\n"
      "v_mfma_f32_16x16x32_bf16 v[132:135], a[124:127], a[12:15], 0\n"
      "s_add_u32 s76, s31, 16384\n"
      "ds_read_b128 a[72:75], v179 offset:16384\n"
      "v_mfma_f32_16x16x32_bf16 v[132:135], a[140:143], a[32:35], v[132:135]\n"
      "buffer_load_dwordx4 a[192:195], v234, s[60:63], s76 offen offset:2048 sc1\n"
      "v_mfma_f32_16x16x32_bf16 v[116:119], a[124:127], a[16:19], 0\n"
      "v_mfma_f32_16x16x32_bf16 v[116:119], a[140:143], a[36:39], v[116:119]\n"
      "s_waitcnt vmcnt(9)\n"
      "v_mfma_f32_16x16x32_bf16 v[160:163], a[0:3], a[132:135], 0\n"
      "s_add_u32 s76, s31, 20480\n"
      "v_mfma_f32_16x16x32_bf16 v[160:163], a[20:23], a[148:151], v[160:163]\n"
      "buffer_load_dwordx4 a[208:211], v234, s[60:63], s76 offen offset:2048 sc1\n"
      "v_mfma_f32_16x16x32_bf16 v[164:167], a[4:7], a[132:135], 0\n"
      "s_mov_b32 exec_lo, 0\n"
      "buffer_load_dwordx2 v[246:247], v252, s[60:63], s96 offen sc1\n"
      "s_mov_b64 exec, -1\n"
      "v_mfma_f32_16x16x32_bf16 v[164:167], a[24:27], a[148:151], v[164:167]\n"
      "v_mfma_f32_16x16x32_bf16 v[168:171], a[8:11], a[132:135], 0\n"
      "s_add_u32 s76, s31, 16384\n"
      "v_mfma_f32_16x16x32_bf16 v[168:171], a[28:31], a[148:151], v[168:171]\n"
      "buffer_load_dwordx4 a[184:187], v234, s[60:63], s76 offen offset:0 sc1\n"
      "v_mfma_f32_16x16x32_bf16 v[172:175], a[12:15], a[132:135], 0\n"
      "s_waitcnt lgkmcnt(0)\n"
      "v_mfma_f32_16x16x32_bf16 v[172:175], a[32:35], a[148:151], v[172:175]\n"
      "s_waitcnt vmcnt(10)\n"
      "v_mfma_f32_16x16x32_bf16 v[156:159], a[16:19], a[132:135], 0\n"
      "s_add_u32 s76, s31, 20480\n"
      "v_mfma_f32_16x16x32_bf16 v[156:159], a[36:39], a[148:151], v[156:159]\n"
      "buffer_load_dwordx4 a[200:203], v234, s[60:63], s76 offen offset:0 sc1\n"
      "v_mfma_f32_16x16x32_bf16 v[140:143], a[160:163], a[40:43], v[140:143]\n"
      "v_mfma_f32_16x16x32_bf16 v[140:143], a[176:179], a[60:63], v[140:143]\n"
      "v_mfma_f32_16x16x32_bf16 v[144:147], a[160:163], a[44:47], v[144:147]\n"
      "s_add_u32 s76, s31, 16384\n"
      "ds_read_b128 a[0:3], v176 offset:20480\n"
      "v_mfma_f32_16x16x32_bf16 v[144:147], a[176:179], a[64:67], v[144:147]\n"
      "buffer_load_dwordx4 a[188:191], v234, s[60:63], s76 offen offset:1024 sc1\n"
      "v_mfma_f32_16x16x32_bf16 v[136:139], a[160:163], a[56:59], v[136:139]\n"
      "ds_read_b128 a[20:23], v177 offset:25600\n"
      "v_mfma_f32_16x16x32_bf16 v[136:139], a[176:179], a[76:79], v[136:139]\n"
      "s_waitcnt vmcnt(10)\n"
      "ds_read_b128 a[4:7], v176 offset:21504\n"
      "v_mfma_f32_16x16x32_bf16 v[100:103], a[152:155], a[40:43], v[100:103]\n"
      "s_add_u32 s76, s31, 20480\n"
      "v_mfma_f32_16x16x32_bf16 v[100:103], a[168:171], a[60:63], v[100:103]\n"
      "buffer_load_dwordx4 a[204:207], v234, s[60:63], s76 offen offset:1024 sc1\n"
      "v_mfma_f32_16x16x32_bf16 v[104:107], a[152:155], a[44:47], v[104:107]\n"
      "ds_read_b128 a[24:27], v177 offset:26624\n"
      "v_mfma_f32_16x16x32_bf16 v[104:107], a[168:171], a[64:67], v[104:107]\n"
      "ds_read_b128 a[16:19], v182 offset:24576\n"
      "v_mfma_f32_16x16x32_bf16 v[108:111], a[152:155], a[48:51], v[108:111]\n"
      "s_add_u32 s76, s31, 16384\n"
      "v_mfma_f32_16x16x32_bf16 v[108:111], a[168:171], a[68:71], v[108:111]\n"
      "buffer_load_dwordx4 a[196:199], v234, s[60:63], s76 offen offset:3072 sc1\n"
      "v_mfma_f32_16x16x32_bf16 v[112:115], a[152:155], a[52:55], v[112:115]\n"
      "ds_read_b128 a[36:39], v183 offset:29696\n"
      "v_mfma_f32_16x16x32_bf16 v[112:115], a[168:171], a[72:75], v[112:115]\n"
      "s_waitcnt vmcnt(10)\n"
      "v_mfma_f32_16x16x32_bf16 v[120:123], a[156:159], a[40:43], v[120:123]\n"
      "s_add_u32 s76, s31, 20480\n"
      "ds_read_b128 a[8:11], v178 offset:20480\n"
      "v_mfma_f32_16x16x32_bf16 v[120:123], a[172:175], a[60:63], v[120:123]\n"
      "buffer_load_dwordx4 a[212:215], v234, s[60:63], s76 offen offset:3072 sc1\n"
      "v_mfma_f32_16x16x32_bf16 v[124:127], a[156:159], a[44:47], v[124:127]\n"
      "ds_read_b128 a[28:31], v179 offset:25600\n"
      "v_mfma_f32_16x16x32_bf16 v[124:127], a[172:175], a[64:67], v[124:127]\n"
      "v_mfma_f32_16x16x32_bf16 v[128:131], a[156:159], a[48:51], v[128:131]\n"
      "s_add_u32 s76, s31, 24576\n"
      "ds_read_b128 a[12:15], v178 offset:21504\n"
      "v_mfma_f32_16x16x32_bf16 v[128:131], a[172:175], a[68:71], v[128:131]\n"
      "buffer_load_dwordx4 a[224:227], v234, s[60:63], s76 offen offset:2048 sc1\n"
      "v_mfma_f32_16x16x32_bf16 v[132:135], a[156:159], a[52:55], v[132:135]\n"
      "ds_read_b128 a[32:35], v179 offset:26624\n"
      "v_mfma_f32_16x16x32_bf16 v[132:135], a[172:175], a[72:75], v[132:135]\n"
      "s_waitcnt vmcnt(10)\n"
      "v_mfma_f32_16x16x32_bf16 v[116:119], a[156:159], a[56:59], v[116:119]\n"
      "s_add_u32 s76, s31, 28672\n"
      "v_mfma_f32_16x16x32_bf16 v[116:119], a[172:175], a[76:79], v[116:119]\n"
      "buffer_load_dwordx4 a[240:243], v234, s[60:63], s76 offen offset:2048 sc1\n"
      "v_mfma_f32_16x16x32_bf16 v[160:163], a[40:43], a[164:167], v[160:163]\n"
      "v_mfma_f32_16x16x32_bf16 v[160:163], a[60:63], a[180:183], v[160:163]\n"
      "v_mfma_f32_16x16x32_bf16 v[164:167], a[44:47], a[164:167], v[164:167]\n"
      "s_add_u32 s76, s31, 24576\n"
      "v_mfma_f32_16x16x32_bf16 v[164:167], a[64:67], a[180:183], v[164:167]\n"
      "buffer_load_dwordx4 a[216:219], v234, s[60:63], s76 offen offset:0 sc1\n"
      "v_mfma_f32_16x16x32_bf16 v[168:171], a[48:51], a[164:167], v[168:171]\n"
      "v_mfma_f32_16x16x32_bf16 v[168:171], a[68:71], a[180:183], v[168:171]\n"
      "s_waitcnt lgkmcnt(0)\n"
      "v_mfma_f32_16x16x32_bf16 v[172:175], a[52:55], a[164:167], v[172:175]\n"
      "s_add_u32 s76, s31, 28672\n"
      "v_mfma_f32_16x16x32_bf16 v[172:175], a[72:75], a[180:183], v[172:175]\n"
      "buffer_load_dwordx4 a[232:235], v234, s[60:63], s76 offen offset:0 sc1\n"
      "v_mfma_f32_16x16x32_bf16 v[156:159], a[56:59], a[164:167], v[156:159]\n"
      "v_mfma_f32_16x16x32_bf16 v[156:159], a[76:79], a[180:183], v[156:159]\n"
      "s_waitcnt vmcnt(11)\n"
      "v_mfma_f32_16x16x32_bf16 v[140:143], a[192:195], a[0:3], v[140:143]\n"
      "s_add_u32 s76, s31, 24576\n"
      "s_cmp_eq_u32 s64, 3\n"
      "s_cbranch_scc0 NP10_%=\n"
      "s_mov_b32 exec_lo, 0\n"
      "s_waitcnt vmcnt(10)\n"
      "s_branch PC13_%=\n"
      "PL11_%=:\n"
      "buffer_load_dwordx2 v[246:247], v252, s[60:63], s96 offen sc1\n"
      "s_waitcnt vmcnt(0)\n"
      "PC13_%=:\n"
      "v_cmp_ne_u32 vcc, v246, v251\n"
      "s_nop 1\n"
      "s_cbranch_vccz NE12_%=\n"
      "v_mov_b32 v251, v246\n"
      "buffer_store_dwordx2 v[250:251], v253, s[60:63], s96 offen sc1\n"
      "NE12_%=:\n"
      "v_cmp_ne_u32 vcc, v247, v250\n"
      "s_nop 1\n"
      "s_cbranch_vccz NP10_%=\n"
      "s_cmp_eq_u32 s87, 0\n"
      "s_cbranch_scc1 NP10_%=\n"
      "s_sub_u32 s87, s87, 1\n"
      "s_branch PL11_%=\n"
      "NP10_%=:\n"
      "s_mov_b64 exec, -1\n"
      "s_barrier\n"
      "v_mfma_f32_16x16x32_bf16 v[140:143], a[208:211], a[20:23], v[140:143]\n"
      "buffer_load_dwordx4 a[220:223], v234, s[60:63], s76 offen offset:1024 sc1\n"
      "v_mfma_f32_16x16x32_bf16 v[144:147], a[192:195], a[4:7], v[144:147]\n"
      "ds_read_b128 a[40:43], v176 offset:30720\n"
      "ds_read_b64_tr_b16 a[80:81], v233 offset:0\n"
      "v_mfma_f32_16x16x32_bf16 v[144:147], a[208:211], a[24:27], v[144:147]\n"
      "s_waitcnt vmcnt(9)\n"
      "ds_read_b128 a[60:63], v177 offset:35840\n"
      "ds_read_b64_tr_b16 a[82:83], v233 offset:512\n"
      "v_mfma_f32_16x16x32_bf16 v[136:139], a[192:195], a[16:19], v[136:139]\n"
      "s_add_u32 s76, s31, 28672\n"
      "v_mfma_f32_16x16x32_bf16 v[136:139], a[208:211], a[36:39], v[136:139]\n"
      "buffer_load_dwordx4 a[236:239], v234, s[60:63], s76 offen offset:1024 sc1\n"
      "v_mfma_f32_16x16x32_bf16 v[100:103], a[184:187], a[0:3], v[100:103]\n"
      "ds_read_b128 a[44:47], v176 offset:31744\n"
      "ds_read_b64_tr_b16 a[84:85], v233 offset:4096\n"
      "v_mfma_f32_16x16x32_bf16 v[100:103], a[200:203], a[20:23], v[100:103]\n"
      "ds_read_b128 a[64:67], v177 offset:36864\n"
      "v_mfma_f32_16x16x32_bf16 v[104:107], a[184:187], a[4:7], v[104:107]\n"
      "s_add_u32 s76, s31, 24576\n"
      "v_mfma_f32_16x16x32_bf16 v[104:107], a[200:203], a[24:27], v[104:107]\n"
      "buffer_load_dwordx4 a[228:231], v234, s[60:63], s76 offen offset:3072 sc1\n"
      "v_mfma_f32_16x16x32_bf16 v[108:111], a[184:187], a[8:11], v[108:111]\n"
      "ds_read_b128 a[56:59], v182 offset:34816\n"
      "ds_read_b64_tr_b16 a[86:87], v233 offset:4608\n"
      "v_mfma_f32_16x16x32_bf16 v[108:111], a[200:203], a[28:31], v[108:111]\n"
      "s_waitcnt vmcnt(9)\n"
      "ds_read_b128 a[76:79], v183 offset:39936\n"
      "v_mfma_f32_16x16x32_bf16 v[112:115], a[184:187], a[12:15], v[112:115]\n"
      "s_add_u32 s76, s31, 28672\n"
      "ds_read_b64_tr_b16 a[88:89], v233 offset:8192\n"
      "v_mfma_f32_16x16x32_bf16 v[112:115], a[200:203], a[32:35], v[112:115]\n"
      "buffer_load_dwordx4 a[244:247], v234, s[60:63], s76 offen offset:3072 sc1\n"
      "v_mfma_f32_16x16x32_bf16 v[120:123], a[188:191], a[0:3], v[120:123]\n"
      "ds_read_b128 a[48:51], v178 offset:30720\n"
      "v_mfma_f32_16x16x32_bf16 v[120:123], a[204:207], a[20:23], v[120:123]\n"
      "ds_read_b64_tr_b16 a[90:91], v233 offset:8704\n"
      "v_mfma_f32_16x16x32_bf16 v[124:127], a[188:191], a[4:7], v[124:127]\n"
      "s_add_u32 s76, s31, 32768\n"
      "ds_read_b128 a[68:71], v179 offset:35840\n"
      "v_mfma_f32_16x16x32_bf16 v[124:127], a[204:207], a[24:27], v[124:127]\n"
      "buffer_load_dwordx4 a[128:131], v234, s[60:63], s76 offen offset:2048 sc1\n"
      "v_mfma_f32_16x16x32_bf16 v[128:131], a[188:191], a[8:11], v[128:131]\n"
      "ds_read_b128 a[52:55], v178 offset:31744\n"
      "ds_read_b64_tr_b16 a[92:93], v233 offset:12288\n"
      "v_mfma_f32_16x16x32_bf16 v[128:131], a[204:207], a[28:31], v[128:131]\n"
      "v_mfma_f32_16x16x32_bf16 v[132:135], a[188:191], a[12:15], v[132:135]\n"
      "s_add_u32 s76, s31, 36864\n"
      "ds_read_b128 a[72:75], v179 offset:36864\n"
      "v_mfma_f32_16x16x32_bf16 v[132:135], a[204:207], a[32:35], v[132:135]\n"
      "buffer_load_dwordx4 a[144:147], v234, s[60:63], s76 offen offset:2048 sc1\n"
      "v_mfma_f32_16x16x32_bf16 v[116:119], a[188:191], a[16:19], v[116:119]\n"
      "ds_read_b64_tr_b16 a[94:95], v233 offset:12800\n"
      "v_mfma_f32_16x16x32_bf16 v[116:119], a[204:207], a[36:39], v[116:119]\n"
      "s_waitcnt vmcnt(10)\n"
      "v_mfma_f32_16x16x32_bf16 v[160:163], a[0:3], a[196:199], v[160:163]\n"
      "s_add_u32 s76, s31, 32768\n"
      "ds_read_b64_tr_b16 a[96:97], v233 offset:16384\n"
      "v_mfma_f32_16x16x32_bf16 v[160:163], a[20:23], a[212:215], v[160:163]\n"
      "buffer_load_dwordx4 a[120:123], v234, s[60:63], s76 offen offset:0 sc1\n"
      "v_mfma_f32_16x16x32_bf16 v[164:167], a[4:7], a[196:199], v[164:167]\n"
      "v_mfma_f32_16x16x32_bf16 v[164:167], a[24:27], a[212:215], v[164:167]\n"
      "ds_read_b64_tr_b16 a[98:99], v233 offset:16896\n"
      "v_mfma_f32_16x16x32_bf16 v[168:171], a[8:11], a[196:199], v[168:171]\n"
      "s_add_u32 s76, s31, 36864\n"
      "v_mfma_f32_16x16x32_bf16 v[168:171], a[28:31], a[212:215], v[168:171]\n"
      "buffer_load_dwordx4 a[136:139], v234, s[60:63], s76 offen offset:0 sc1\n"
      "v_mfma_f32_16x16x32_bf16 v[172:175], a[12:15], a[196:199], v[172:175]\n"
      "s_waitcnt lgkmcnt(3)\n"
      "v_mfma_f32_16x16x32_bf16 v[172:175], a[32:35], a[212:215], v[172:175]\n"
      "s_waitcnt vmcnt(10)\n"
      "ds_read_b64_tr_b16 a[100:101], v233 offset:20480\n"
      "v_mfma_f32_16x16x32_bf16 v[156:159], a[16:19], a[196:199], v[156:159]\n"
      "s_add_u32 s76, s31, 32768\n"
      "v_mfma_f32_16x16x32_bf16 v[156:159], a[36:39], a[212:215], v[156:159]\n"
      "buffer_load_dwordx4 a[124:127], v234, s[60:63], s76 offen offset:1024 sc1\n"
      "v_mfma_f32_16x16x32_bf16 v[140:143], a[224:227], a[40:43], v[140:143]\n"
      "ds_read_b64_tr_b16 a[102:103], v233 offset:20992\n"
      "v_mfma_f32_16x16x32_bf16 v[140:143], a[240:243], a[60:63], v[140:143]\n"
      "v_mfma_f32_16x16x32_bf16 v[144:147], a[224:227], a[44:47], v[144:147]\n"
      "s_add_u32 s76, s31, 36864\n"
      "ds_read_b128 a[0:3], v176 offset:40960\n"
      "v_mfma_f32_16x16x32_bf16 v[144:147], a[240:243], a[64:67], v[144:147]\n"
      "buffer_load_dwordx4 a[140:143], v234, s[60:63], s76 offen offset:1024 sc1\n"
      "v_mfma_f32_16x16x32_bf16 v[136:139], a[224:227], a[56:59], v[136:139]\n"
      "ds_read_b128 a[20:23], v177 offset:46080\n"
      "ds_read_b64_tr_b16 a[104:105], v233 offset:24576\n"
      "v_mfma_f32_16x16x32_bf16 v[136:139], a[240:243], a[76:79], v[136:139]\n"
      "s_waitcnt vmcnt(10)\n"
      "ds_read_b128 a[4:7], v176 offset:41984\n"
      "v_mfma_f32_16x16x32_bf16 v[100:103], a[216:219], a[40:43], v[100:103]\n"
      "s_add_u32 s76, s31, 32768\n"
      "ds_read_b64_tr_b16 a[106:107], v233 offset:25088\n"
      "v_mfma_f32_16x16x32_bf16 v[100:103], a[232:235], a[60:63], v[100:103]\n"
      "buffer_load_dwordx4 a[132:135], v234, s[60:63], s76 offen offset:3072 sc1\n"
      "v_mfma_f32_16x16x32_bf16 v[104:107], a[216:219], a[44:47], v[104:107]\n"
      "ds_read_b128 a[24:27], v177 offset:47104\n"
      "v_mfma_f32_16x16x32_bf16 v[104:107], a[232:235], a[64:67], v[104:107]\n"
      "ds_read_b128 a[16:19], v182 offset:45056\n"
      "ds_read_b64_tr_b16 a[108:109], v233 offset:28672\n"
      "v_mfma_f32_16x16x32_bf16 v[108:111], a[216:219], a[48:51], v[108:111]\n"
      "s_add_u32 s76, s31, 36864\n"
      "v_mfma_f32_16x16x32_bf16 v[108:111], a[232:235], a[68:71], v[108:111]\n"
      "buffer_load_dwordx4 a[148:151], v234, s[60:63], s76 offen offset:3072 sc1\n"
      "v_mfma_f32_16x16x32_bf16 v[112:115], a[216:219], a[52:55], v[112:115]\n"
      "ds_read_b128 a[36:39], v183 offset:50176\n"
      "ds_read_b64_tr_b16 a[110:111], v233 offset:29184\n"
      "v_mfma_f32_16x16x32_bf16 v[112:115], a[232:235], a[72:75], v[112:115]\n"
      "s_waitcnt vmcnt(10)\n"
      "v_mfma_f32_16x16x32_bf16 v[120:123], a[220:223], a[40:43], v[120:123]\n"
      "s_add_u32 s76, s31, 40960\n"
      "ds_read_b128 a[8:11], v178 offset:40960\n"
      "v_mfma_f32_16x16x32_bf16 v[120:123], a[236:239], a[60:63], v[120:123]\n"
      "buffer_load_dwordx4 a[160:163], v234, s[60:63], s76 offen offset:2048 sc1\n"
      "v_mfma_f32_16x16x32_bf16 v[124:127], a[220:223], a[44:47], v[124:127]\n"
      "ds_read_b128 a[28:31], v179 offset:46080\n"
      "ds_read_b64_tr_b16 a[112:113], v233 offset:32768\n"
      "v_mfma_f32_16x16x32_bf16 v[124:127], a[236:239], a[64:67], v[124:127]\n"
      "v_mfma_f32_16x16x32_bf16 v[128:131], a[220:223], a[48:51], v[128:131]\n"
      "s_add_u32 s76, s31, 45056\n"
      "ds_read_b128 a[12:15], v178 offset:41984\n"
      "ds_read_b64_tr_b16 a[114:115], v233 offset:33280\n"
      "v_mfma_f32_16x16x32_bf16 v[128:131], a[236:239], a[68:71], v[128:131]\n"
      "buffer_load_dwordx4 a[176:179], v234, s[60:63], s76 offen offset:2048 sc1\n"
      "v_mfma_f32_16x16x32_bf16 v[132:135], a[220:223], a[52:55], v[132:135]\n"
      "ds_read_b128 a[32:35], v179 offset:47104\n"
      "v_mfma_f32_16x16x32_bf16 v[132:135], a[236:239], a[72:75], v[132:135]\n"
      "s_waitcnt vmcnt(10)\n"
      "v_mfma_f32_16x16x32_bf16 v[116:119], a[220:223], a[56:59], v[116:119]\n"
      "s_add_u32 s76, s31, 40960\n"
      "v_mfma_f32_16x16x32_bf16 v[116:119], a[236:239], a[76:79], v[116:119]\n"
      "buffer_load_dwordx4 a[152:155], v234, s[60:63], s76 offen offset:0 sc1\n"
      "v_mfma_f32_16x16x32_bf16 v[160:163], a[40:43], a[228:231], v[160:163]\n"
      "v_mfma_f32_16x16x32_bf16 v[160:163], a[60:63], a[244:247], v[160:163]\n"
      "v_mfma_f32_16x16x32_bf16 v[164:167], a[44:47], a[228:231], v[164:167]\n"
      "s_add_u32 s76, s31, 45056\n"
      "v_mfma_f32_16x16x32_bf16 v[164:167], a[64:67], a[244:247], v[164:167]\n"
      "buffer_load_dwordx4 a[168:171], v234, s[60:63], s76 offen offset:0 sc1\n"
      "v_mfma_f32_16x16x32_bf16 v[168:171], a[48:51], a[228:231], v[168:171]\n"
      "v_mfma_f32_16x16x32_bf16 v[168:171], a[68:71], a[244:247], v[168:171]\n"
      "v_mfma_f32_16x16x32_bf16 v[172:175], a[52:55], a[228:231], v[172:175]\n"
      "s_add_u32 s76, s31, 40960\n"
      "v_mfma_f32_16x16x32_bf16 v[172:175], a[72:75], a[244:247], v[172:175]\n"
      "buffer_load_dwordx4 a[156:159], v234, s[60:63], s76 offen offset:1024 sc1\n"
      "v_mfma_f32_16x16x32_bf16 v[156:159], a[56:59], a[228:231], v[156:159]\n"
      "v_mfma_f32_16x16x32_bf16 v[156:159], a[76:79], a[244:247], v[156:159]\n"
      "v_mfma_f32_16x16x32_bf16 v[236:239], a[116:119], a[80:83], 0\n"
      "s_add_u32 s76, s31, 45056\n"
      "v_mfma_f32_16x16x32_bf16 v[236:239], a[116:119], a[84:87], v[236:239]\n"
      "buffer_load_dwordx4 a[172:175], v234, s[60:63], s76 offen offset:1024 sc1\n"
      "v_mfma_f32_16x16x32_bf16 v[236:239], a[116:119], a[88:91], v[236:239]\n"
      "s_waitcnt lgkmcnt(15)\n"
      "v_mfma_f32_16x16x32_bf16 v[236:239], a[116:119], a[92:95], v[236:239]\n"
      "s_waitcnt lgkmcnt(0)\n"
      "v_mfma_f32_16x16x32_bf16 v[236:239], a[116:119], a[96:99], v[236:239]\n"
      "s_add_u32 s76, s31, 40960\n"
      "v_mfma_f32_16x16x32_bf16 v[236:239], a[116:119], a[100:103], v[236:239]\n"
      "buffer_load_dwordx4 a[164:167], v234, s[60:63], s76 offen offset:3072 sc1\n"
      "v_mfma_f32_16x16x32_bf16 v[236:239], a[116:119], a[104:107], v[236:239]\n"
      "v_mfma_f32_16x16x32_bf16 v[236:239], a[116:119], a[108:111], v[236:239]\n"
      "s_waitcnt vmcnt(13)\n"
      "v_mfma_f32_16x16x32_bf16 v[236:239], a[116:119], a[112:115], v[236:239]\n"
      "s_add_u32 s76, s31, 45056\n"
      "v_mfma_f32_16x16x32_bf16 v[140:143], a[128:131], a[0:3], v[140:143]\n"
      "buffer_load_dwordx4 a[180:183], v234, s[60:63], s76 offen offset:3072 sc1\n"
      "v_mfma_f32_16x16x32_bf16 v[140:143], a[144:147], a[20:23], v[140:143]\n"
      "v_mfma_f32_16x16x32_bf16 v[144:147], a[128:131], a[4:7], v[144:147]\n"
      "ds_read_b128 a[40:43], v176 offset:51200\n"
      "v_and_b32 v2, 15, v1\n"
      "v_mfma_f32_16x16x32_bf16 v[144:147], a[144:147], a[24:27], v[144:147]\n"
      "s_add_u32 s76, s31, 49152\n"
      "ds_read_b128 a[60:63], v177 offset:56320\n"
      "v_bfe_u32 v3, v1, 4, 1\n"
      "v_mfma_f32_16x16x32_bf16 v[136:139], a[128:131], a[16:19], v[136:139]\n"
      "buffer_load_dwordx4 a[192:195], v234, s[60:63], s76 offen offset:2048 sc1\n"
      "v_mfma_f32_16x16x32_bf16 v[136:139], a[144:147], a[36:39], v[136:139]\n"
      "s_waitcnt vmcnt(13)\n"
      "ds_read_b128 a[44:47], v176 offset:52224\n"
      "v_lshrrev_b32 v4, 5, v1\n"
      "v_mfma_f32_16x16x32_bf16 v[100:103], a[120:123], a[0:3], v[100:103]\n"
      "v_cvt_pk_bf16_f32 v236, v238, v238\n"
      "v_lshlrev_b32 v2, 4, v2\n"
      "v_mfma_f32_16x16x32_bf16 v[100:103], a[136:139], a[20:23], v[100:103]\n"
      "s_add_u32 s76, s31, 53248\n"
      "ds_read_b128 a[64:67], v177 offset:57344\n"
      "v_lshl_add_u32 v2, v4, 8, v2\n"
      "v_mfma_f32_16x16x32_bf16 v[104:107], a[120:123], a[4:7], v[104:107]\n"
      "buffer_load_dwordx4 a[208:211], v234, s[60:63], s76 offen offset:2048 sc1\n"
      "v_mfma_f32_16x16x32_bf16 v[104:107], a[136:139], a[24:27], v[104:107]\n"
      "ds_read_b128 a[56:59], v182 offset:55296\n"
      "v_and_b32 v239, 0xffff0000, v236\n"
      "v_mfma_f32_16x16x32_bf16 v[108:111], a[120:123], a[8:11], v[108:111]\n"
      "s_waitcnt vmcnt(12)\n"
      "v_lshl_add_u32 v96, v3, 14, v2\n"
      "v_and_b32 v2, 31, v1\n"
      "v_mfma_f32_16x16x32_bf16 v[108:111], a[136:139], a[28:31], v[108:111]\n"
      "s_add_u32 s76, s31, 49152\n"
      "ds_read_b128 a[76:79], v183 offset:60416\n"
      "v_lshlrev_b32 v2, 2, v2\n"
      "v_mfma_f32_16x16x32_bf16 v[112:115], a[120:123], a[12:15], v[112:115]\n"
      "buffer_load_dwordx4 a[184:187], v234, s[60:63], s76 offen offset:0 sc1\n"
      "v_mfma_f32_16x16x32_bf16 v[112:115], a[136:139], a[32:35], v[112:115]\n"
      "s_and_b32 s72, s64, 1\n"
      "v_sub_f32 v239, v238, v239\n"
      "v_xor_b32 v4, s72, v4\n"
      "v_mfma_f32_16x16x32_bf16 v[120:123], a[124:127], a[0:3], v[120:123]\n"
      "ds_read_b128 a[48:51], v178 offset:51200\n"
      "v_lshl_add_u32 v97, v4, 12, v2\n"
      "v_mfma_f32_16x16x32_bf16 v[120:123], a[140:143], a[20:23], v[120:123]\n"
      "s_add_u32 s76, s31, 53248\n"
      "v_cvt_pk_bf16_f32 v236, v238, v239\n"
      "s_lshl2_add_u32 s69, s65, s64\n"
      "v_mfma_f32_16x16x32_bf16 v[124:127], a[124:127], a[4:7], v[124:127]\n"
      "buffer_load_dwordx4 a[200:203], v234, s[60:63], s76 offen offset:0 sc1\n"
      "v_mfma_f32_16x16x32_bf16 v[124:127], a[140:143], a[24:27], v[124:127]\n"
      "s_lshl_b32 s70, s69, 9\n"
      "ds_read_b128 a[68:71], v179 offset:56320\n"
      "s_lshl_b32 s71, s66, 16\n"
      "v_mfma_f32_16x16x32_bf16 v[128:131], a[124:127], a[8:11], v[128:131]\n"
      "s_add_u32 s70, s70, s71\n"
      "ds_read_b128 a[52:55], v178 offset:52224\n"
      "s_lshl_b32 s72, s72, 15\n"
      "v_mfma_f32_16x16x32_bf16 v[128:131], a[140:143], a[28:31], v[128:131]\n"
      "s_add_u32 s76, s31, 49152\n"
      "v_mov_b32 v237, v236\n"
      "s_add_u32 s70, s70, s72\n"
      "v_mfma_f32_16x16x32_bf16 v[132:135], a[124:127], a[12:15], v[132:135]\n"
      "buffer_load_dwordx4 a[188:191], v234, s[60:63], s76 offen offset:1024 sc1\n"
      "v_mfma_f32_16x16x32_bf16 v[132:135], a[140:143], a[32:35], v[132:135]\n"
      "s_add_u32 s20, s70, 524288\n"
      "ds_read_b128 a[72:75], v179 offset:57344\n"
      "s_xor_b32 s21, s20, 32768\n"
      "v_mfma_f32_16x16x32_bf16 v[116:119], a[124:127], a[16:19], v[116:119]\n"
      "s_lshl_b32 s70, s69, 7\n"
      "v_mov_b32 v238, 0\n"
      "s_lshl_b32 s71, s66, 13\n"
      "v_mfma_f32_16x16x32_bf16 v[116:119], a[140:143], a[36:39], v[116:119]\n"
      "s_add_u32 s76, s31, 53248\n"
      "s_add_u32 s70, s70, s71\n"
      "s_waitcnt vmcnt(14)\n"
      "v_mfma_f32_16x16x32_bf16 v[160:163], a[0:3], a[132:135], v[160:163]\n"
      "buffer_load_dwordx4 a[204:207], v234, s[60:63], s76 offen offset:1024 sc1\n"
      "s_waitcnt vmcnt(14)\n"
      "v_mfma_f32_16x16x32_bf16 v[160:163], a[20:23], a[148:151], v[160:163]\n"
      "s_add_u32 s22, s70, 2621440\n"
      "v_lshrrev_b32 v2, 3, v1\n"
      "v_mfma_f32_16x16x32_bf16 v[164:167], a[4:7], a[132:135], v[164:167]\n"
      "v_mov_b32 v239, 0\n"
      "v_bfe_u32 v3, v1, 4, 1\n"
      "v_mfma_f32_16x16x32_bf16 v[164:167], a[24:27], a[148:151], v[164:167]\n"
      "s_add_u32 s76, s31, 49152\n"
      "v_lshlrev_b32 v3, 1, v3\n"
      "v_mfma_f32_16x16x32_bf16 v[168:171], a[8:11], a[132:135], v[168:171]\n"
      "buffer_load_dwordx4 a[196:199], v234, s[60:63], s76 offen offset:3072 sc1\n"
      "v_mfma_f32_16x16x32_bf16 v[168:171], a[28:31], a[148:151], v[168:171]\n"
      "v_and_b32 v4, 7, v1\n"
      "v_xor_b32 v4, v4, v3\n"
      "v_mfma_f32_16x16x32_bf16 v[172:175], a[12:15], a[132:135], v[172:175]\n"
      "s_waitcnt lgkmcnt(0)\n"
      "v_lshlrev_b32 v4, 4, v4\n"
      "v_mfma_f32_16x16x32_bf16 v[172:175], a[32:35], a[148:151], v[172:175]\n"
      "s_add_u32 s76, s31, 53248\n"
      "v_mul_u32_u24 v3, 9216, v2\n"
      "v_mfma_f32_16x16x32_bf16 v[156:159], a[16:19], a[132:135], v[156:159]\n"
      "buffer_load_dwordx4 a[212:215], v234, s[60:63], s76 offen offset:3072 sc1\n"
      "v_mfma_f32_16x16x32_bf16 v[156:159], a[36:39], a[148:151], v[156:159]\n"
      "s_waitcnt vmcnt(14)\n"
      "v_add_u32 v240, v3, v4\n"
      "v_xor_b32 v4, 64, v4\n"
      "v_mfma_f32_16x16x32_bf16 v[140:143], a[160:163], a[40:43], v[140:143]\n"
      "v_add_u32 v3, 72704, v3\n"
      "v_mfma_f32_16x16x32_bf16 v[140:143], a[176:179], a[60:63], v[140:143]\n"
      "s_add_u32 s76, s31, 57344\n"
      "v_add_u32 v241, v3, v4\n"
      "v_mfma_f32_16x16x32_bf16 v[144:147], a[160:163], a[44:47], v[144:147]\n"
      "buffer_load_dwordx4 a[224:227], v234, s[60:63], s76 offen offset:2048 sc1\n"
      "v_mfma_f32_16x16x32_bf16 v[144:147], a[176:179], a[64:67], v[144:147]\n"
      "s_mul_i32 s69, s64, 147456\n"
      "v_add_u32 v182, 61440, v182\n"
      "v_add_u32 v183, 61440, v183\n"
      "v_mfma_f32_16x16x32_bf16 v[136:139], a[160:163], a[56:59], v[136:139]\n"
      "s_mul_i32 s70, s34, 1152\n"
      "v_add_u32 v176, 61440, v176\n"
      "v_mfma_f32_16x16x32_bf16 v[136:139], a[176:179], a[76:79], v[136:139]\n"
      "s_add_u32 s76, s31, 61440\n"
      "v_add_u32 v177, 61440, v177\n"
      "s_add_u32 s94, s69, s70\n"
      "s_waitcnt vmcnt(14)\n"
      "v_mfma_f32_16x16x32_bf16 v[100:103], a[152:155], a[40:43], v[100:103]\n"
      "buffer_load_dwordx4 a[240:243], v234, s[60:63], s76 offen offset:2048 sc1\n"
      "s_waitcnt vmcnt(14)\n"
      "v_mfma_f32_16x16x32_bf16 v[100:103], a[168:171], a[60:63], v[100:103]\n"
      "s_lshl_b32 s95, s64, 11\n"
      "v_add_u32 v178, 61440, v178\n"
      "v_and_b32 v2, 15, v1\n"
      "v_mfma_f32_16x16x32_bf16 v[104:107], a[152:155], a[44:47], v[104:107]\n"
      "v_add_u32 v179, 61440, v179\n"
      "v_mul_u32_u24 v5, 2304, v2\n"
      "v_mfma_f32_16x16x32_bf16 v[104:107], a[168:171], a[64:67], v[104:107]\n"
      "s_add_u32 s76, s31, 57344\n"
      "ds_read_b128 a[0:3], v176 offset:0\n"
      "v_bfe_u32 v4, v1, 4, 1\n"
      "v_mfma_f32_16x16x32_bf16 v[108:111], a[152:155], a[48:51], v[108:111]\n"
      "buffer_load_dwordx4 a[216:219], v234, s[60:63], s76 offen offset:0 sc1\n"
      "v_mfma_f32_16x16x32_bf16 v[108:111], a[168:171], a[68:71], v[108:111]\n"
      "ds_read_b128 a[20:23], v177 offset:5120\n"
      "v_lshl_add_u32 v4, v4, 5, v5\n"
      "v_mfma_f32_16x16x32_bf16 v[112:115], a[152:155], a[52:55], v[112:115]\n"
      "s_waitcnt vmcnt(13)\n"
      "ds_read_b128 a[4:7], v176 offset:1024\n"
      "v_lshrrev_b32 v2, 5, v1\n"
      "v_mfma_f32_16x16x32_bf16 v[112:115], a[168:171], a[72:75], v[112:115]\n"
      "s_add_u32 s76, s31, 61440\n"
      "ds_read_b128 a[24:27], v177 offset:6144\n"
      "v_lshl_add_u32 v4, v2, 4, v4\n"
      "v_mfma_f32_16x16x32_bf16 v[120:123], a[156:159], a[40:43], v[120:123]\n"
      "buffer_load_dwordx4 a[232:235], v234, s[60:63], s76 offen offset:0 sc1\n"
      "v_mfma_f32_16x16x32_bf16 v[120:123], a[172:175], a[60:63], v[120:123]\n"
      "s_and_b32 s69, s64, 1\n"
      "ds_read_b128 a[16:19], v182 offset:4096\n"
      "s_lshl1_add_u32 s69, s66, s69\n"
      "v_mfma_f32_16x16x32_bf16 v[124:127], a[156:159], a[44:47], v[124:127]\n"
      "s_lshl_b32 s69, s69, 5\n"
      "ds_read_b128 a[36:39], v183 offset:9216\n"
      "s_lshr_b32 s70, s64, 1\n"
      "v_mfma_f32_16x16x32_bf16 v[124:127], a[172:175], a[64:67], v[124:127]\n"
      "s_add_u32 s76, s31, 57344\n"
      "ds_read_b128 a[8:11], v178 offset:0\n"
      "s_add_u32 s69, s69, s70\n"
      "v_mfma_f32_16x16x32_bf16 v[128:131], a[156:159], a[48:51], v[128:131]\n"
      "buffer_load_dwordx4 a[220:223], v234, s[60:63], s76 offen offset:1024 sc1\n"
      "v_mfma_f32_16x16x32_bf16 v[128:131], a[172:175], a[68:71], v[128:131]\n"
      "s_mul_i32 s69, s69, 1152\n"
      "ds_read_b128 a[28:31], v179 offset:5120\n"
      "s_mul_i32 s70, s65, 144\n"
      "v_mfma_f32_16x16x32_bf16 v[132:135], a[156:159], a[52:55], v[132:135]\n"
      "s_add_u32 s23, s69, s70\n"
      "ds_read_b128 a[12:15], v178 offset:1024\n"
      "v_mfma_f32_16x16x32_bf16 v[132:135], a[172:175], a[72:75], v[132:135]\n"
      "s_add_u32 s76, s31, 61440\n"
      "v_mfma_f32_16x16x32_bf16 v[116:119], a[156:159], a[56:59], v[116:119]\n"
      "buffer_load_dwordx4 a[236:239], v234, s[60:63], s76 offen offset:1024 sc1\n"
      "v_mfma_f32_16x16x32_bf16 v[116:119], a[172:175], a[76:79], v[116:119]\n"
      "s_waitcnt vmcnt(14)\n"
      "ds_read_b128 a[32:35], v179 offset:6144\n"
      "v_mfma_f32_16x16x32_bf16 v[160:163], a[40:43], a[164:167], v[160:163]\n"
      "v_mfma_f32_16x16x32_bf16 v[160:163], a[60:63], a[180:183], v[160:163]\n"
      "s_add_u32 s76, s31, 57344\n"
      "v_mfma_f32_16x16x32_bf16 v[164:167], a[44:47], a[164:167], v[164:167]\n"
      "buffer_load_dwordx4 a[228:231], v234, s[60:63], s76 offen offset:3072 sc1\n"
      "v_mfma_f32_16x16x32_bf16 v[164:167], a[64:67], a[180:183], v[164:167]\n"
      "v_mfma_f32_16x16x32_bf16 v[168:171], a[48:51], a[164:167], v[168:171]\n"
      "s_waitcnt lgkmcnt(0)\n"
      "v_mfma_f32_16x16x32_bf16 v[168:171], a[68:71], a[180:183], v[168:171]\n"
      "s_add_u32 s76, s31, 61440\n"
      "v_mfma_f32_16x16x32_bf16 v[172:175], a[52:55], a[164:167], v[172:175]\n"
      "buffer_load_dwordx4 a[244:247], v234, s[60:63], s76 offen offset:3072 sc1\n"
      "v_mfma_f32_16x16x32_bf16 v[172:175], a[72:75], a[180:183], v[172:175]\n"
      "v_mfma_f32_16x16x32_bf16 v[156:159], a[56:59], a[164:167], v[156:159]\n"
      "s_waitcnt vmcnt(14)\n"
      "v_mfma_f32_16x16x32_bf16 v[156:159], a[76:79], a[180:183], v[156:159]\n"
      "s_add_u32 s76, s31, 65536\n"
      "v_mfma_f32_16x16x32_bf16 v[140:143], a[192:195], a[0:3], v[140:143]\n"
      "buffer_load_dwordx4 a[128:131], v234, s[60:63], s76 offen offset:2048 sc1\n"
      "v_mfma_f32_16x16x32_bf16 v[140:143], a[208:211], a[20:23], v[140:143]\n"
      "v_mfma_f32_16x16x32_bf16 v[144:147], a[192:195], a[4:7], v[144:147]\n"
      "ds_read_b128 a[40:43], v176 offset:10240\n"
      "v_mfma_f32_16x16x32_bf16 v[144:147], a[208:211], a[24:27], v[144:147]\n"
      "s_add_u32 s76, s31, 69632\n"
      "ds_read_b128 a[60:63], v177 offset:15360\n"
      "v_mfma_f32_16x16x32_bf16 v[136:139], a[192:195], a[16:19], v[136:139]\n"
      "buffer_load_dwordx4 a[144:147], v234, s[60:63], s76 offen offset:2048 sc1\n"
      "v_mfma_f32_16x16x32_bf16 v[136:139], a[208:211], a[36:39], v[136:139]\n"
      "s_waitcnt vmcnt(14)\n"
      "ds_read_b128 a[44:47], v176 offset:11264\n"
      "v_mfma_f32_16x16x32_bf16 v[100:103], a[184:187], a[0:3], v[100:103]\n"
      "v_mfma_f32_16x16x32_bf16 v[100:103], a[200:203], a[20:23], v[100:103]\n"
      "s_add_u32 s76, s31, 65536\n"
      "ds_read_b128 a[64:67], v177 offset:16384\n"
      "v_mfma_f32_16x16x32_bf16 v[104:107], a[184:187], a[4:7], v[104:107]\n"
      "buffer_load_dwordx4 a[120:123], v234, s[60:63], s76 offen offset:0 sc1\n"
      "v_mfma_f32_16x16x32_bf16 v[104:107], a[200:203], a[24:27], v[104:107]\n"
      "ds_read_b128 a[56:59], v182 offset:14336\n"
      "v_mfma_f32_16x16x32_bf16 v[108:111], a[184:187], a[8:11], v[108:111]\n"
      "v_mfma_f32_16x16x32_bf16 v[108:111], a[200:203], a[28:31], v[108:111]\n"
      "s_add_u32 s76, s31, 69632\n"
      "ds_read_b128 a[76:79], v183 offset:19456\n"
      "v_mfma_f32_16x16x32_bf16 v[112:115], a[184:187], a[12:15], v[112:115]\n"
      "buffer_load_dwordx4 a[136:139], v234, s[60:63], s76 offen offset:0 sc1\n"
      "v_mfma_f32_16x16x32_bf16 v[112:115], a[200:203], a[32:35], v[112:115]\n"
      "s_waitcnt vmcnt(14)\n"
      "v_mfma_f32_16x16x32_bf16 v[120:123], a[188:191], a[0:3], v[120:123]\n"
      "ds_read_b128 a[48:51], v178 offset:10240\n"
      "v_mfma_f32_16x16x32_bf16 v[120:123], a[204:207], a[20:23], v[120:123]\n"
      "s_add_u32 s76, s31, 65536\n"
      "v_mfma_f32_16x16x32_bf16 v[124:127], a[188:191], a[4:7], v[124:127]\n"
      "buffer_load_dwordx4 a[124:127], v234, s[60:63], s76 offen offset:1024 sc1\n"
      "v_mfma_f32_16x16x32_bf16 v[124:127], a[204:207], a[24:27], v[124:127]\n"
      "ds_read_b128 a[68:71], v179 offset:15360\n"
      "v_mfma_f32_16x16x32_bf16 v[128:131], a[188:191], a[8:11], v[128:131]\n"
      "ds_read_b128 a[52:55], v178 offset:11264\n"
      "v_mfma_f32_16x16x32_bf16 v[128:131], a[204:207], a[28:31], v[128:131]\n"
      "s_add_u32 s76, s31, 69632\n"
      "v_mfma_f32_16x16x32_bf16 v[132:135], a[188:191], a[12:15], v[132:135]\n"
      "buffer_load_dwordx4 a[140:143], v234, s[60:63], s76 offen offset:1024 sc1\n"
      "v_mfma_f32_16x16x32_bf16 v[132:135], a[204:207], a[32:35], v[132:135]\n"
      "ds_read_b128 a[72:75], v179 offset:16384\n"
      "v_mfma_f32_16x16x32_bf16 v[116:119], a[188:191], a[16:19], v[116:119]\n"
      "s_waitcnt vmcnt(14)\n"
      "v_mfma_f32_16x16x32_bf16 v[116:119], a[204:207], a[36:39], v[116:119]\n"
      "s_add_u32 s76, s31, 65536\n"
      "v_mfma_f32_16x16x32_bf16 v[160:163], a[0:3], a[196:199], v[160:163]\n"
      "buffer_load_dwordx4 a[132:135], v234, s[60:63], s76 offen offset:3072 sc1\n"
      "v_mfma_f32_16x16x32_bf16 v[160:163], a[20:23], a[212:215], v[160:163]\n"
      "v_mfma_f32_16x16x32_bf16 v[164:167], a[4:7], a[196:199], v[164:167]\n"
      "v_mfma_f32_16x16x32_bf16 v[164:167], a[24:27], a[212:215], v[164:167]\n"
      "s_add_u32 s76, s31, 69632\n"
      "v_mfma_f32_16x16x32_bf16 v[168:171], a[8:11], a[196:199], v[168:171]\n"
      "buffer_load_dwordx4 a[148:151], v234, s[60:63], s76 offen offset:3072 sc1\n"
      "v_mfma_f32_16x16x32_bf16 v[168:171], a[28:31], a[212:215], v[168:171]\n"
      "v_mfma_f32_16x16x32_bf16 v[172:175], a[12:15], a[196:199], v[172:175]\n"
      "s_waitcnt lgkmcnt(0)\n"
      "v_mfma_f32_16x16x32_bf16 v[172:175], a[32:35], a[212:215], v[172:175]\n"
      "v_mfma_f32_16x16x32_bf16 v[156:159], a[16:19], a[196:199], v[156:159]\n"
      "s_mov_b64 exec, 0xff\n"
      "buffer_load_dword v251, v252, s[60:63], s96 offen sc1\n"
      "s_mov_b64 exec, -1\n"
      "v_mfma_f32_16x16x32_bf16 v[156:159], a[36:39], a[212:215], v[156:159]\n"
      "s_waitcnt vmcnt(15)\n"
      "v_mfma_f32_16x16x32_bf16 v[140:143], a[224:227], a[40:43], v[140:143]\n"
      "v_mfma_f32_16x16x32_bf16 v[140:143], a[240:243], a[60:63], v[140:143]\n"
      "v_mfma_f32_16x16x32_bf16 v[144:147], a[224:227], a[44:47], v[144:147]\n"
      "ds_read_b128 a[0:3], v176 offset:20480\n"
      "v_mfma_f32_16x16x32_bf16 v[144:147], a[240:243], a[64:67], v[144:147]\n"
      "ds_read_b128 a[20:23], v177 offset:25600\n"
      "v_mfma_f32_16x16x32_bf16 v[136:139], a[224:227], a[56:59], v[136:139]\n"
      "v_mfma_f32_16x16x32_bf16 v[136:139], a[240:243], a[76:79], v[136:139]\n"
      "s_waitcnt vmcnt(13)\n"
      "ds_read_b128 a[4:7], v176 offset:21504\n"
      "v_mfma_f32_16x16x32_bf16 v[100:103], a[216:219], a[40:43], v[100:103]\n"
      "v_mfma_f32_16x16x32_bf16 v[100:103], a[232:235], a[60:63], v[100:103]\n"
      "ds_read_b128 a[24:27], v177 offset:26624\n"
      "v_mfma_f32_16x16x32_bf16 v[104:107], a[216:219], a[44:47], v[104:107]\n"
      "v_mfma_f32_16x16x32_bf16 v[104:107], a[232:235], a[64:67], v[104:107]\n"
      "ds_read_b128 a[16:19], v182 offset:24576\n"
      "v_mfma_f32_16x16x32_bf16 v[108:111], a[216:219], a[48:51], v[108:111]\n"
      "v_mfma_f32_16x16x32_bf16 v[108:111], a[232:235], a[68:71], v[108:111]\n"
      "ds_read_b128 a[36:39], v183 offset:29696\n"
      "v_mfma_f32_16x16x32_bf16 v[112:115], a[216:219], a[52:55], v[112:115]\n"
      "v_mfma_f32_16x16x32_bf16 v[112:115], a[232:235], a[72:75], v[112:115]\n"
      "s_waitcnt vmcnt(11)\n"
      "v_mfma_f32_16x16x32_bf16 v[120:123], a[220:223], a[40:43], v[120:123]\n"
      "ds_read_b128 a[8:11], v178 offset:20480\n"
      "v_mfma_f32_16x16x32_bf16 v[120:123], a[236:239], a[60:63], v[120:123]\n"
      "v_mfma_f32_16x16x32_bf16 v[124:127], a[220:223], a[44:47], v[124:127]\n"
      "ds_read_b128 a[28:31], v179 offset:25600\n"
      "v_mfma_f32_16x16x32_bf16 v[124:127], a[236:239], a[64:67], v[124:127]\n"
      "v_mfma_f32_16x16x32_bf16 v[128:131], a[220:223], a[48:51], v[128:131]\n"
      "ds_read_b128 a[12:15], v178 offset:21504\n"
      "v_mfma_f32_16x16x32_bf16 v[128:131], a[236:239], a[68:71], v[128:131]\n"
      "v_mfma_f32_16x16x32_bf16 v[132:135], a[220:223], a[52:55], v[132:135]\n"
      "ds_read_b128 a[32:35], v179 offset:26624\n"
      "v_mfma_f32_16x16x32_bf16 v[132:135], a[236:239], a[72:75], v[132:135]\n"
      "v_mfma_f32_16x16x32_bf16 v[116:119], a[220:223], a[56:59], v[116:119]\n"
      "v_mfma_f32_16x16x32_bf16 v[116:119], a[236:239], a[76:79], v[116:119]\n"
      "s_waitcnt vmcnt(9)\n"
      "v_mfma_f32_16x16x32_bf16 v[160:163], a[40:43], a[228:231], v[160:163]\n"
      "v_mfma_f32_16x16x32_bf16 v[160:163], a[60:63], a[244:247], v[160:163]\n"
      "v_mfma_f32_16x16x32_bf16 v[164:167], a[44:47], a[228:231], v[164:167]\n"
      "v_mfma_f32_16x16x32_bf16 v[164:167], a[64:67], a[244:247], v[164:167]\n"
      "v_mfma_f32_16x16x32_bf16 v[168:171], a[48:51], a[228:231], v[168:171]\n"
      "buffer_store_dwordx4 a[80:83], v234, s[60:63], s86 offen offset:0 sc1\n"
      "v_mfma_f32_16x16x32_bf16 v[168:171], a[68:71], a[244:247], v[168:171]\n"
      "s_waitcnt lgkmcnt(0)\n"
      "v_mfma_f32_16x16x32_bf16 v[172:175], a[52:55], a[228:231], v[172:175]\n"
      "buffer_store_dwordx4 a[84:87], v234, s[60:63], s86 offen offset:1024 sc1\n"
      "v_mfma_f32_16x16x32_bf16 v[172:175], a[72:75], a[244:247], v[172:175]\n"
      "v_mfma_f32_16x16x32_bf16 v[156:159], a[56:59], a[228:231], v[156:159]\n"
      "v_mfma_f32_16x16x32_bf16 v[156:159], a[76:79], a[244:247], v[156:159]\n"
      "s_waitcnt vmcnt(9)\n"
      "v_mfma_f32_16x16x32_bf16 v[140:143], a[128:131], a[0:3], v[140:143]\n"
      "buffer_store_dwordx4 a[88:91], v234, s[60:63], s86 offen offset:2048 sc1\n"
      "v_mfma_f32_16x16x32_bf16 v[140:143], a[144:147], a[20:23], v[140:143]\n"
      "v_mfma_f32_16x16x32_bf16 v[144:147], a[128:131], a[4:7], v[144:147]\n"
      "v_mfma_f32_16x16x32_bf16 v[144:147], a[144:147], a[24:27], v[144:147]\n"
      "buffer_store_dwordx4 a[92:95], v234, s[60:63], s86 offen offset:3072 sc1\n"
      "v_mfma_f32_16x16x32_bf16 v[136:139], a[128:131], a[16:19], v[136:139]\n"
      "v_mfma_f32_16x16x32_bf16 v[136:139], a[144:147], a[36:39], v[136:139]\n"
      "s_waitcnt vmcnt(9)\n"
      "v_mfma_f32_16x16x32_bf16 v[100:103], a[120:123], a[0:3], v[100:103]\n"
      "v_mfma_f32_16x16x32_bf16 v[100:103], a[136:139], a[20:23], v[100:103]\n"
      "s_add_u32 s86, s86, 4096\n"
      "v_cvt_pk_bf16_f32 v30, v140, v141\n"
      "v_cvt_pk_bf16_f32 v31, v142, v143\n"
      "v_mfma_f32_16x16x32_bf16 v[104:107], a[120:123], a[4:7], v[104:107]\n"
      "buffer_store_dwordx4 a[96:99], v234, s[60:63], s86 offen offset:0 sc1\n"
      "v_mfma_f32_16x16x32_bf16 v[104:107], a[136:139], a[24:27], v[104:107]\n"
      "v_cvt_pk_bf16_f32 v34, v144, v145\n"
      "v_cvt_pk_bf16_f32 v35, v146, v147\n"
      "v_mfma_f32_16x16x32_bf16 v[108:111], a[120:123], a[8:11], v[108:111]\n"
      "ds_write_b64 v180, v[30:31] offset:0\n"
      "v_mfma_f32_16x16x32_bf16 v[108:111], a[136:139], a[28:31], v[108:111]\n"
      "ds_write_b64 v180, v[34:35] offset:512\n"
      "v_mfma_f32_16x16x32_bf16 v[112:115], a[120:123], a[12:15], v[112:115]\n"
      "buffer_store_dwordx4 a[100:103], v234, s[60:63], s86 offen offset:1024 sc1\n"
      "v_mfma_f32_16x16x32_bf16 v[112:115], a[136:139], a[32:35], v[112:115]\n"
      "s_waitcnt vmcnt(9)\n"
      "v_cvt_pk_bf16_f32 v26, v136, v137\n"
      "v_cvt_pk_bf16_f32 v27, v138, v139\n"
      "v_mfma_f32_16x16x32_bf16 v[120:123], a[124:127], a[0:3], v[120:123]\n"
      "v_mfma_f32_16x16x32_bf16 v[120:123], a[140:143], a[20:23], v[120:123]\n"
      "buffer_store_dwordx4 a[104:107], v234, s[60:63], s86 offen offset:2048 sc1\n"
      "v_mfma_f32_16x16x32_bf16 v[124:127], a[124:127], a[4:7], v[124:127]\n"
      "v_mfma_f32_16x16x32_bf16 v[124:127], a[140:143], a[24:27], v[124:127]\n"
      "v_mfma_f32_16x16x32_bf16 v[128:131], a[124:127], a[8:11], v[128:131]\n"
      "v_cvt_pk_bf16_f32 v8, v100, v101\n"
      "v_cvt_pk_bf16_f32 v9, v102, v103\n"
      "v_mfma_f32_16x16x32_bf16 v[128:131], a[140:143], a[28:31], v[128:131]\n"
      "buffer_store_dwordx4 a[108:111], v234, s[60:63], s86 offen offset:3072 sc1\n"
      "v_mfma_f32_16x16x32_bf16 v[132:135], a[124:127], a[12:15], v[132:135]\n"
      "v_cvt_pk_bf16_f32 v12, v104, v105\n"
      "v_cvt_pk_bf16_f32 v13, v106, v107\n"
      "v_mfma_f32_16x16x32_bf16 v[132:135], a[140:143], a[32:35], v[132:135]\n"
      "v_cvt_pk_bf16_f32 v16, v108, v109\n"
      "v_cvt_pk_bf16_f32 v17, v110, v111\n"
      "s_waitcnt lgkmcnt(0)\n"
      "s_barrier\n"
      "v_mfma_f32_16x16x32_bf16 v[116:119], a[124:127], a[16:19], v[116:119]\n"
      "s_waitcnt vmcnt(9)\n"
      "ds_read_b64 v[38:39], v181 offset:0\n"
      "ds_read_b64 v[42:43], v181 offset:512\n"
      "v_mfma_f32_16x16x32_bf16 v[116:119], a[140:143], a[36:39], v[116:119]\n"
      "s_add_u32 s86, s86, 4096\n"
      "v_cvt_pk_bf16_f32 v20, v112, v113\n"
      "v_cvt_pk_bf16_f32 v21, v114, v115\n"
      "v_mfma_f32_16x16x32_bf16 v[160:163], a[0:3], a[132:135], v[160:163]\n"
      "buffer_store_dwordx4 a[112:115], v234, s[60:63], s86 offen offset:0 sc1\n"
      "v_mfma_f32_16x16x32_bf16 v[160:163], a[20:23], a[148:151], v[160:163]\n"
      "v_mfma_f32_16x16x32_bf16 v[164:167], a[4:7], a[132:135], v[164:167]\n"
      "v_mfma_f32_16x16x32_bf16 v[164:167], a[24:27], a[148:151], v[164:167]\n"
      "v_cvt_pk_bf16_f32 v28, v120, v121\n"
      "v_cvt_pk_bf16_f32 v29, v122, v123\n"
      "v_mfma_f32_16x16x32_bf16 v[168:171], a[8:11], a[132:135], v[168:171]\n"
      "v_cvt_pk_bf16_f32 v32, v124, v125\n"
      "v_cvt_pk_bf16_f32 v33, v126, v127\n"
      "v_mfma_f32_16x16x32_bf16 v[168:171], a[28:31], a[148:151], v[168:171]\n"
      "v_cvt_pk_bf16_f32 v36, v128, v129\n"
      "v_cvt_pk_bf16_f32 v37, v130, v131\n"
      "v_mfma_f32_16x16x32_bf16 v[172:175], a[12:15], a[132:135], v[172:175]\n"
      "s_mov_b32 exec_hi, 0\n"
      "buffer_store_dwordx4 v[236:239], v234, s[60:63], s19 offen sc1\n"
      "s_mov_b64 exec, -1\n"
      "v_mfma_f32_16x16x32_bf16 v[172:175], a[32:35], a[148:151], v[172:175]\n"
      "v_cvt_pk_bf16_f32 v40, v132, v133\n"
      "v_cvt_pk_bf16_f32 v41, v134, v135\n"
      "v_mfma_f32_16x16x32_bf16 v[156:159], a[16:19], a[132:135], v[156:159]\n"
      "v_cvt_pk_bf16_f32 v24, v116, v117\n"
      "v_cvt_pk_bf16_f32 v25, v118, v119\n"
      "v_mfma_f32_16x16x32_bf16 v[156:159], a[36:39], a[148:151], v[156:159]\n"
      "v_mov_b32 v88, v30\n"
      "v_mov_b32 v89, v31\n"
      "v_mov_b32 v90, v34\n"
      "v_mov_b32 v91, v35\n"
      "s_waitcnt lgkmcnt(1)\n"
      "v_mov_b32 v92, v38\n"
      "v_mov_b32 v93, v39\n"
      "s_waitcnt lgkmcnt(0)\n"
      "v_mov_b32 v94, v42\n"
      "v_mov_b32 v95, v43\n"
      "v_mfma_f32_16x16x32_bf16 v[68:71], v[24:27], v[220:223], v[224:227]\n"
      "v_permlane16_swap_b32 v10, v88\n"
      "v_mfma_f32_16x16x32_bf16 v[72:75], v[28:31], v[220:223], v[224:227]\n"
      "v_permlane16_swap_b32 v11, v89\n"
      "v_permlane16_swap_b32 v14, v90\n"
      "v_mfma_f32_16x16x32_bf16 v[76:79], v[32:35], v[220:223], v[224:227]\n"
      "v_permlane16_swap_b32 v15, v91\n"
      "v_mfma_f32_16x16x32_bf16 v[80:83], v[36:39], v[220:223], v[224:227]\n"
      "v_permlane16_swap_b32 v18, v92\n"
      "v_permlane16_swap_b32 v19, v93\n"
      "v_mfma_f32_16x16x32_bf16 v[84:87], v[40:43], v[220:223], v[224:227]\n"
      "v_permlane16_swap_b32 v22, v94\n"
      "v_permlane16_swap_b32 v23, v95\n"
      "v_and_b32 v10, v10, v216\n"
      "v_and_b32 v11, v11, v217\n"
      "v_and_b32 v14, v14, v216\n"
      "v_and_b32 v15, v15, v217\n"
      "v_and_b32 v18, v18, v216\n"
      "v_and_b32 v19, v19, v217\n"
      "v_and_b32 v22, v22, v216\n"
      "v_and_b32 v23, v23, v217\n"
      "v_cvt_pk_bf16_f32 v44, v156, v157\n"
      "v_cvt_pk_bf16_f32 v45, v158, v159\n"
      "v_mfma_f32_16x16x32_bf16 v[100:103], v[24:27], v[8:11], v[196:199]\n"
      "v_cvt_pk_bf16_f32 v46, v160, v161\n"
      "v_cvt_pk_bf16_f32 v47, v162, v163\n"
      "v_mfma_f32_16x16x32_bf16 v[104:107], v[28:31], v[8:11], v[208:211]\n"
      "v_cvt_pk_bf16_f32 v48, v164, v165\n"
      "v_cvt_pk_bf16_f32 v49, v166, v167\n"
      "v_mfma_f32_16x16x32_bf16 v[108:111], v[28:31], v[12:15], v[204:207]\n"
      "v_cvt_pk_bf16_f32 v50, v156, v157\n"
      "v_cvt_pk_bf16_f32 v51, v158, v159\n"
      "v_mfma_f32_16x16x32_bf16 v[112:115], v[32:35], v[12:15], v[208:211]\n"
      "v_cvt_pk_bf16_f32 v52, v168, v169\n"
      "v_cvt_pk_bf16_f32 v53, v170, v171\n"
      "v_mfma_f32_16x16x32_bf16 v[116:119], v[24:27], v[16:19], v[200:203]\n"
      "v_cvt_pk_bf16_f32 v54, v172, v173\n"
      "v_cvt_pk_bf16_f32 v55, v174, v175\n"
      "v_mfma_f32_16x16x32_bf16 v[120:123], v[36:39], v[16:19], v[208:211]\n"
      "v_cvt_pk_bf16_f32 v56, v68, v69\n"
      "v_cvt_pk_bf16_f32 v57, v70, v71\n"
      "v_mfma_f32_16x16x32_bf16 v[124:127], v[36:39], v[20:23], v[204:207]\n"
      "v_cvt_pk_bf16_f32 v58, v72, v73\n"
      "v_cvt_pk_bf16_f32 v59, v74, v75\n"
      "v_mfma_f32_16x16x32_bf16 v[128:131], v[40:43], v[20:23], v[208:211]\n"
      "v_cvt_pk_bf16_f32 v60, v76, v77\n"
      "v_cvt_pk_bf16_f32 v61, v78, v79\n"
      "v_cvt_pk_bf16_f32 v62, v68, v69\n"
      "v_cvt_pk_bf16_f32 v63, v70, v71\n"
      "v_cvt_pk_bf16_f32 v64, v80, v81\n"
      "v_cvt_pk_bf16_f32 v65, v82, v83\n"
      "v_cvt_pk_bf16_f32 v66, v84, v85\n"
      "v_cvt_pk_bf16_f32 v67, v86, v87\n"
      "s_mov_b32 s78, 0x3eae1a93\n"
      "v_max_f32 v100, v100, v104\n"
      "v_max_f32 v101, v101, v105\n"
      "v_max_f32 v102, v102, v106\n"
      "v_max_f32 v103, v103, v107\n"
      "v_max_f32 v108, v108, v112\n"
      "v_max_f32 v109, v109, v113\n"
      "v_max_f32 v110, v110, v114\n"
      "v_max_f32 v111, v111, v115\n"
      "v_max_f32 v116, v116, v120\n"
      "v_max_f32 v117, v117, v121\n"
      "v_max_f32 v118, v118, v122\n"
      "v_max_f32 v119, v119, v123\n"
      "v_max_f32 v124, v124, v128\n"
      "v_max_f32 v125, v125, v129\n"
      "v_max_f32 v126, v126, v130\n"
      "v_max_f32 v127, v127, v131\n"
      "v_max3_f32 v88, v100, v101, v102\n"
      "v_max3_f32 v89, v108, v109, v110\n"
      "v_max3_f32 v90, v116, v117, v118\n"
      "v_max3_f32 v91, v124, v125, v126\n"
      "v_max_f32 v88, v88, v103\n"
      "v_max_f32 v89, v89, v111\n"
      "v_max_f32 v90, v90, v119\n"
      "v_max_f32 v91, v91, v127\n"
      "v_mov_b32 v92, v88\n"
      "v_mov_b32 v93, v89\n"
      "v_mov_b32 v94, v90\n"
      "v_mov_b32 v95, v91\n"
      "v_permlane16_swap_b32 v92, v88\n"
      "v_permlane16_swap_b32 v93, v89\n"
      "v_permlane16_swap_b32 v94, v90\n"
      "v_permlane16_swap_b32 v95, v91\n"
      "v_max_f32 v88, v88, v92\n"
      "v_max_f32 v89, v89, v93\n"
      "v_max_f32 v90, v90, v94\n"
      "v_max_f32 v91, v91, v95\n"
      "v_mov_b32 v92, v88\n"
      "v_mov_b32 v93, v89\n"
      "v_mov_b32 v94, v90\n"
      "v_mov_b32 v95, v91\n"
      "v_permlane32_swap_b32 v92, v88\n"
      "v_permlane32_swap_b32 v93, v89\n"
      "v_permlane32_swap_b32 v94, v90\n"
      "v_permlane32_swap_b32 v95, v91\n"
      "v_max_f32 v88, v88, v92\n"
      "v_max_f32 v89, v89, v93\n"
      "v_max_f32 v90, v90, v94\n"
      "v_max_f32 v91, v91, v95\n"
      "v_mul_f32 v88, s78, v88\n"
      "v_mul_f32 v89, s78, v89\n"
      "v_mul_f32 v90, s78, v90\n"
      "v_mul_f32 v91, s78, v91\n"
      "v_fma_f32 v100, v100, s78, -v88\n"
      "v_fma_f32 v101, v101, s78, -v88\n"
      "v_fma_f32 v102, v102, s78, -v88\n"
      "v_fma_f32 v103, v103, s78, -v88\n"
      "v_exp_f32 v100, v100\n"
      "v_exp_f32 v101, v101\n"
      "v_exp_f32 v102, v102\n"
      "v_exp_f32 v103, v103\n"
      "v_cvt_pk_bf16_f32 v70, v100, v101\n"
      "v_cvt_pk_bf16_f32 v71, v102, v103\n"
      "v_and_b32 v68, v70, v212\n"
      "v_and_b32 v69, v71, v213\n"
      "v_xor_b32 v70, v70, v68\n"
      "v_xor_b32 v71, v71, v69\n"
      "v_fma_f32 v108, v108, s78, -v89\n"
      "v_fma_f32 v109, v109, s78, -v89\n"
      "v_mfma_f32_16x16x32_bf16 v[140:143], v[56:59], v[68:71], 0\n"
      "v_fma_f32 v110, v110, s78, -v89\n"
      "v_fma_f32 v111, v111, s78, -v89\n"
      "v_mfma_f32_16x16x32_bf16 v[136:139], v[44:47], v[68:71], 0\n"
      "v_exp_f32 v108, v108\n"
      "v_exp_f32 v109, v109\n"
      "v_exp_f32 v110, v110\n"
      "v_exp_f32 v111, v111\n"
      "v_cvt_pk_bf16_f32 v74, v108, v109\n"
      "v_cvt_pk_bf16_f32 v75, v110, v111\n"
      "v_and_b32 v72, v74, v214\n"
      "v_and_b32 v73, v75, v215\n"
      "v_xor_b32 v74, v74, v72\n"
      "v_xor_b32 v75, v75, v73\n"
      "v_fma_f32 v116, v116, s78, -v90\n"
      "v_fma_f32 v117, v117, s78, -v90\n"
      "v_mfma_f32_16x16x32_bf16 v[148:151], v[58:61], v[72:75], 0\n"
      "v_fma_f32 v118, v118, s78, -v90\n"
      "v_fma_f32 v119, v119, s78, -v90\n"
      "v_mfma_f32_16x16x32_bf16 v[144:147], v[46:49], v[72:75], 0\n"
      "v_exp_f32 v116, v116\n"
      "v_exp_f32 v117, v117\n"
      "v_exp_f32 v118, v118\n"
      "v_exp_f32 v119, v119\n"
      "v_cvt_pk_bf16_f32 v78, v116, v117\n"
      "v_cvt_pk_bf16_f32 v79, v118, v119\n"
      "v_and_b32 v76, v78, v218\n"
      "v_and_b32 v77, v79, v219\n"
      "v_xor_b32 v78, v78, v76\n"
      "v_xor_b32 v79, v79, v77\n"
      "v_fma_f32 v124, v124, s78, -v91\n"
      "v_fma_f32 v125, v125, s78, -v91\n"
      "v_mfma_f32_16x16x32_bf16 v[156:159], v[62:65], v[76:79], 0\n"
      "v_fma_f32 v126, v126, s78, -v91\n"
      "v_fma_f32 v127, v127, s78, -v91\n"
      "v_mfma_f32_16x16x32_bf16 v[152:155], v[50:53], v[76:79], 0\n"
      "v_exp_f32 v124, v124\n"
      "v_exp_f32 v125, v125\n"
      "v_exp_f32 v126, v126\n"
      "v_exp_f32 v127, v127\n"
      "v_cvt_pk_bf16_f32 v82, v124, v125\n"
      "v_cvt_pk_bf16_f32 v83, v126, v127\n"
      "v_and_b32 v80, v82, v214\n"
      "v_and_b32 v81, v83, v215\n"
      "v_xor_b32 v82, v82, v80\n"
      "v_xor_b32 v83, v83, v81\n"
      "v_rcp_f32 v88, v142\n"
      "v_rcp_f32 v89, v150\n"
      "v_mfma_f32_16x16x32_bf16 v[164:167], v[64:67], v[80:83], 0\n"
      "v_mfma_f32_16x16x32_bf16 v[160:163], v[52:55], v[80:83], 0\n"
      "v_mul_f32 v136, v136, v88\n"
      "v_mul_f32 v137, v137, v88\n"
      "v_mul_f32 v138, v138, v88\n"
      "v_mul_f32 v139, v139, v88\n"
      "v_mul_f32 v140, v140, v88\n"
      "v_mul_f32 v141, v141, v88\n"
      "v_cvt_pk_bf16_f32 v8, v136, v137\n"
      "v_cvt_pk_bf16_f32 v9, v138, v139\n"
      "v_cvt_pk_bf16_f32 v16, v140, v141\n"
      "v_mul_f32 v144, v144, v89\n"
      "v_mul_f32 v145, v145, v89\n"
      "v_mul_f32 v146, v146, v89\n"
      "v_mul_f32 v147, v147, v89\n"
      "v_mul_f32 v148, v148, v89\n"
      "v_mul_f32 v149, v149, v89\n"
      "v_cvt_pk_bf16_f32 v10, v144, v145\n"
      "v_cvt_pk_bf16_f32 v11, v146, v147\n"
      "v_cvt_pk_bf16_f32 v17, v148, v149\n"
      "v_rcp_f32 v90, v158\n"
      "s_nop 0\n"
      "v_mul_f32 v152, v152, v90\n"
      "v_mul_f32 v153, v153, v90\n"
      "v_mul_f32 v154, v154, v90\n"
      "v_mul_f32 v155, v155, v90\n"
      "v_mul_f32 v156, v156, v90\n"
      "v_mul_f32 v157, v157, v90\n"
      "v_cvt_pk_bf16_f32 v12, v152, v153\n"
      "v_cvt_pk_bf16_f32 v13, v154, v155\n"
      "v_cvt_pk_bf16_f32 v18, v156, v157\n"
      "v_permlane16_swap_b32 v8, v10\n"
      "v_permlane16_swap_b32 v9, v11\n"
      "buffer_store_dwordx4 v[8:11], v96, s[60:63], s20 offen sc1\n"
      "v_permlane16_swap_b32 v16, v17\n"
      "v_rcp_f32 v91, v166\n"
      "s_nop 0\n"
      "v_mul_f32 v160, v160, v91\n"
      "v_mul_f32 v161, v161, v91\n"
      "v_mul_f32 v162, v162, v91\n"
      "v_mul_f32 v163, v163, v91\n"
      "v_mul_f32 v164, v164, v91\n"
      "v_mul_f32 v165, v165, v91\n"
      "v_cvt_pk_bf16_f32 v14, v160, v161\n"
      "v_cvt_pk_bf16_f32 v15, v162, v163\n"
      "v_cvt_pk_bf16_f32 v19, v164, v165\n"
      "v_permlane16_swap_b32 v12, v14\n"
      "v_permlane16_swap_b32 v13, v15\n"
      "v_permlane16_swap_b32 v18, v19\n"
      "buffer_store_dwordx4 v[12:15], v96, s[60:63], s21 offen sc1\n"
      "s_nop 0\n"
      "v_permlane32_swap_b32 v16, v18\n"
      "buffer_store_dword v16, v97, s[60:63], s22 offen sc1\n"
      "v_mov_b32 v66, 1\n"
      "buffer_load_dwordx4 v[184:187], v4, s[36:39], s23 offen offset:0\n"
      "buffer_load_dwordx4 v[188:191], v4, s[36:39], s23 offen offset:64\n"
      "s_mov_b64 exec, 0xffff\n"
      "buffer_load_dwordx4 v[192:195], v5, s[36:39], s23 offen offset:128\n"
      "s_mov_b64 exec, -1\n"
      "s_add_u32 m0, s25, 92160\n"
      "s_add_u32 s80, s84, 0\n"
      "buffer_load_dwordx4 v234, s[60:63], s80 offen sc1 lds\n"
      "v_add_u32 v56, 92160, v234\n"
      "v_add_u32 v57, 92160, v242\n"
      "v_add_u32 v58, 73728, v234\n"
      "v_add_u32 v59, 73728, v242\n"
      "s_lshl2_add_u32 s69, s66, s64\n"
      "s_add_u32 m0, s25, 96256\n"
      "s_add_u32 s80, s84, 4096\n"
      "buffer_load_dwordx4 v234, s[60:63], s80 offen sc1 lds\n"
      "s_lshl3_add_u32 s71, s69, s65\n"
      "s_lshl_b32 s69, s69, 14\n"
      "s_add_u32 s28, s69, 524288\n"
      "s_mul_i32 s69, s71, 2304\n"
      "s_add_u32 s30, s69, 2883584\n"
      "s_add_u32 m0, s25, 100352\n"
      "s_add_u32 s80, s84, 8192\n"
      "buffer_load_dwordx4 v234, s[60:63], s80 offen sc1 lds\n"
      "s_lshl_b32 s71, s71, 7\n"
      "s_add_u32 s31, s71, 5242880\n"
      "s_lshr_b32 s69, s64, 1\n"
      "s_lshl1_add_u32 s69, s66, s69\n"
      "s_lshl_b32 s69, s69, 12\n"
      "s_add_u32 m0, s25, 104448\n"
      "s_add_u32 s80, s84, 12288\n"
      "buffer_load_dwordx4 v234, s[60:63], s80 offen sc1 lds\n"
      "s_and_b32 s70, s64, 1\n"
      "s_lshl_b32 s70, s70, 6\n"
      "s_add_u32 s69, s69, s70\n"
      "s_add_u32 s29, s69, 2621440\n"
      "v_and_b32 v4, 15, v1\n"
      "s_add_u32 m0, s25, 108544\n"
      "s_add_u32 s80, s84, 16384\n"
      "buffer_load_dwordx4 v234, s[60:63], s80 offen sc1 lds\n"
      "v_lshrrev_b32 v5, 4, v1\n"
      "v_lshlrev_b32 v65, 2, v4\n"
      "v_lshl_add_u32 v65, v5, 9, v65\n"
      "v_min_u32 v5, 1, v5\n"
      "v_lshlrev_b32 v5, 30, v5\n"
      "s_add_u32 m0, s25, 112640\n"
      "s_add_u32 s80, s84, 20480\n"
      "buffer_load_dwordx4 v234, s[60:63], s80 offen sc1 lds\n"
      "v_add_u32 v61, v234, v5\n"
      "v_lshl_add_u32 v62, v1, 3, v5\n"
      "v_lshlrev_b32 v4, 4, v4\n"
      "v_bfe_u32 v5, v1, 4, 1\n"
      "v_lshl_add_u32 v4, v5, 9, v4\n"
      "s_add_u32 m0, s25, 116736\n"
      "s_add_u32 s80, s84, 24576\n"
      "buffer_load_dwordx4 v234, s[60:63], s80 offen sc1 lds\n"
      "v_lshrrev_b32 v5, 5, v1\n"
      "v_lshl_add_u32 v60, v5, 8, v4\n"
      "v_add_u32 v63, -1, v5\n"
      "v_mov_b32 v64, 0x3f803f80\n"
      "v_mov_b32 v38, 0\n"
      "v_mov_b32 v39, 0\n"
      "s_waitcnt vmcnt(10)\n"
      "s_barrier\n"
      "s_cmp_eq_u32 s64, 3\n"
      "s_cbranch_scc0 NS14_%=\n"
      "s_mov_b64 exec, 0xff\n"
      "buffer_store_dwordx2 v[250:251], v253, s[60:63], s96 offen sc1\n"
      "s_mov_b64 exec, 1\n"
      "buffer_store_dword v66, off, s[60:63], s18 sc1\n"
      "s_mov_b64 exec, -1\n"
      "NS14_%=:\n"
      "s_add_u32 m0, s25, 120832\n"
      "s_add_u32 s80, s84, 28672\n"
      "buffer_load_dwordx4 v234, s[60:63], s80 offen sc1 lds\n"
      "v_mov_b32 v68, 0\n"
      "v_mov_b32 v69, 0\n"
      "s_lshl_b32 s69, s34, 7\n"
      "s_add_u32 m0, s25, 124928\n"
      "s_add_u32 s80, s84, 32768\n"
      "buffer_load_dwordx4 v234, s[60:63], s80 offen sc1 lds\n"
      "s_add_u32 s69, s69, 393216\n"
      "v_lshl_add_u32 v217, v1, 10, s69\n"
      "v_and_b32 v219, 7, v1\n"
      "s_add_u32 m0, s25, 129024\n"
      "s_add_u32 s80, s84, 36864\n"
      "buffer_load_dwordx4 v234, s[60:63], s80 offen sc1 lds\n"
      "s_lshl_b32 s69, s35, 10\n"
      "v_lshl_add_u32 v219, v219, 7, s69\n"
      "v_lshrrev_b32 v2, 4, v1\n"
      "s_add_u32 m0, s25, 133120\n"
      "s_add_u32 s80, s84, 40960\n"
      "buffer_load_dwordx4 v234, s[60:63], s80 offen sc1 lds\n"
      "v_bfe_u32 v3, v1, 2, 2\n"
      "v_lshlrev_b32 v2, 10, v2\n"
      "v_lshl_add_u32 v2, v3, 7, v2\n"
      "s_add_u32 m0, s25, 137216\n"
      "s_add_u32 s80, s84, 45056\n"
      "buffer_load_dwordx4 v234, s[60:63], s80 offen sc1 lds\n"
      "v_bfe_u32 v3, v1, 3, 2\n"
      "v_xor_b32 v3, s64, v3\n"
      "v_lshl_add_u32 v2, v3, 5, v2\n"
      "s_add_u32 m0, s25, 141312\n"
      "s_add_u32 s80, s84, 49152\n"
      "buffer_load_dwordx4 v234, s[60:63], s80 offen sc1 lds\n"
      "v_and_b32 v3, 3, v1\n"
      "v_lshl_add_u32 v81, v3, 3, v2\n"
      "v_add_u32 v82, 0x10000, v81\n"
      "s_add_u32 m0, s25, 145408\n"
      "s_add_u32 s80, s84, 53248\n"
      "buffer_load_dwordx4 v234, s[60:63], s80 offen sc1 lds\n"
      "v_add_u32 v83, 0x20000, v81\n"
      "v_lshlrev_b32 v80, 4, v1\n"
      "v_add_u32 v85, 0x10000, v80\n"
      "s_add_u32 m0, s25, 149504\n"
      "s_add_u32 s80, s84, 57344\n"
      "buffer_load_dwordx4 v234, s[60:63], s80 offen sc1 lds\n"
      "v_add_u32 v84, 55296, v80\n"
      "v_add_u32 v91, 0, v80\n"
      "s_lshr_b32 s69, s64, 1\n"
      "s_lshl_b32 s69, s69, 12\n"
      "s_and_b32 s70, s64, 1\n"
      "s_lshl_b32 s70, s70, 9\n"
      "s_add_u32 s69, s69, s70\n"
      "v_and_b32 v2, 15, v1\n"
      "s_add_u32 m0, s25, 153600\n"
      "s_add_u32 s80, s84, 61440\n"
      "buffer_load_dwordx4 v234, s[60:63], s80 offen sc1 lds\n"
      "v_lshlrev_b32 v2, 4, v2\n"
      "v_lshrrev_b32 v3, 5, v1\n"
      "v_lshl_add_u32 v2, v3, 8, v2\n"
      "s_add_u32 m0, s25, 73728\n"
      "s_add_u32 s80, s84, 65536\n"
      "buffer_load_dwordx4 v234, s[60:63], s80 offen sc1 lds\n"
      "v_bfe_u32 v3, v1, 4, 1\n"
      "v_lshl_add_u32 v2, v3, 3, v2\n"
      "v_add_u32 v86, s69, v2\n"
      "s_add_u32 m0, s25, 77824\n"
      "s_add_u32 s80, s84, 69632\n"
      "buffer_load_dwordx4 v234, s[60:63], s80 offen sc1 lds\n"
      "v_add_u32 v87, 0x10000, v86\n"
      "s_mul_i32 s69, s34, 1245184\n"
      "s_mul_i32 s70, s64, 19456\n"
      "s_add_u32 s69, s69, s70\n"
      "s_add_u32 s79, s69, 24379392\n"
      "s_mov_b32 s78, 0xbfb8aa3b\n"
      "s_add_u32 m0, s25, 81920\n"
      "s_add_u32 s80, s84, 73728\n"
      "buffer_load_dwordx4 v234, s[60:63], s80 offen sc1 lds\n"
      "s_mov_b32 s4, 0\n"
      "s_add_u32 s5, s4, 2048\n"
      "s_cmp_eq_u32 s5, 14336\n"
      "s_cselect_b32 s5, 0, s5\n"
      "s_add_u32 s6, s5, 2048\n"
      "s_cmp_eq_u32 s6, 14336\n"
      "s_cselect_b32 s6, 0, s6\n"
      "s_add_u32 m0, s25, 86016\n"
      "s_add_u32 s80, s84, 77824\n"
      "buffer_load_dwordx4 v234, s[60:63], s80 offen sc1 lds\n"
      "s_add_u32 s7, s6, 2048\n"
      "s_cmp_eq_u32 s7, 14336\n"
      "s_cselect_b32 s7, 0, s7\n"
      "s_add_u32 s8, s7, 2048\n"
      "s_cmp_eq_u32 s8, 14336\n"
      "s_cselect_b32 s8, 0, s8\n"
      "s_add_u32 s9, s8, 2048\n"
      "s_cmp_eq_u32 s9, 14336\n"
      "s_cselect_b32 s9, 0, s9\n"
      "s_mul_i32 s80, s64, -768\n"
      "s_add_u32 m0, s25, s80\n"
      "s_add_u32 m0, m0, 90112\n"
      "s_add_u32 s80, s80, s84\n"
      "s_add_u32 s80, s80, 81920\n"
      "s_mov_b64 exec, 0xffff\n"
      "buffer_load_dwordx4 v234, s[60:63], s80 offen sc1 lds\n"
      "s_mov_b64 exec, -1\n"
      "s_add_u32 s10, s9, 2048\n"
      "s_cmp_eq_u32 s10, 14336\n"
      "s_cselect_b32 s10, 0, s10\n"
      "s_mul_i32 s22, s64, 18432\n"
      "v_and_b32 v6, 0xff, v80\n"
      "s_add_u32 m0, s95, 0\n"
      "s_add_u32 s76, s94, 0\n"
      "buffer_load_dwordx4 v240, s[48:51], s76 offen lds\n"
      "v_add_u32 v6, 81920, v6\n"
      "v_lshlrev_b32 v7, 3, v1\n"
      "s_lshl2_add_u32 s69, s35, s64\n"
      "s_lshl_b32 s70, s69, 10\n"
      "s_add_u32 s21, s70, 5242880\n"
      "s_mul_i32 s69, s69, 18432\n"
      "s_add_u32 s20, s69, 2883584\n"
      "buffer_load_dwordx4 v241, s[48:51], s76 offen offset:1024 lds\n"
      "s_waitcnt vmcnt(25)\n"
      "v_permlane16_swap_b32 v184, v186\n"
      "v_permlane16_swap_b32 v185, v187\n"
      "s_waitcnt vmcnt(24)\n"
      "v_permlane16_swap_b32 v188, v190\n"
      "v_permlane16_swap_b32 v189, v191\n"
      "s_add_u32 m0, s95, 8192\n"
      "s_add_u32 s76, s94, 589824\n"
      "buffer_load_dwordx4 v240, s[48:51], s76 offen lds\n"
      "s_waitcnt vmcnt(24)\n"
      "v_permlane16_swap_b32 v192, v194\n"
      "v_permlane16_swap_b32 v193, v195\n"
      "v_lshlrev_b32 v8, 16, v184\n"
      "s_mov_b64 exec, 0xff\n"
      "buffer_load_dwordx2 v[246:247], v252, s[60:63], s96 offen sc1\n"
      "s_mov_b64 exec, -1\n"
      "v_and_b32 v9, 0xffff0000, v184\n"
      "v_lshlrev_b32 v10, 16, v185\n"
      "v_and_b32 v11, 0xffff0000, v185\n"
      "buffer_load_dwordx4 v241, s[48:51], s76 offen offset:1024 lds\n"
      "v_lshlrev_b32 v12, 16, v186\n"
      "v_and_b32 v13, 0xffff0000, v186\n"
      "v_lshlrev_b32 v14, 16, v187\n"
      "s_add_u32 m0, s95, 16384\n"
      "s_add_u32 s76, s94, 1179648\n"
      "buffer_load_dwordx4 v240, s[48:51], s76 offen lds\n"
      "v_and_b32 v15, 0xffff0000, v187\n"
      "v_lshlrev_b32 v16, 16, v188\n"
      "v_and_b32 v17, 0xffff0000, v188\n"
      "buffer_load_dwordx4 v241, s[48:51], s76 offen offset:1024 lds\n"
      "v_lshlrev_b32 v18, 16, v189\n"
      "v_and_b32 v19, 0xffff0000, v189\n"
      "v_lshlrev_b32 v20, 16, v190\n"
      "s_add_u32 m0, s95, 24576\n"
      "s_add_u32 s76, s94, 1769472\n"
      "buffer_load_dwordx4 v240, s[48:51], s76 offen lds\n"
      "v_and_b32 v21, 0xffff0000, v190\n"
      "v_lshlrev_b32 v22, 16, v191\n"
      "v_and_b32 v23, 0xffff0000, v191\n"
      "buffer_load_dwordx4 v241, s[48:51], s76 offen offset:1024 lds\n"
      "v_lshlrev_b32 v24, 16, v192\n"
      "v_and_b32 v25, 0xffff0000, v192\n"
      "v_lshlrev_b32 v26, 16, v193\n"
      "v_and_b32 v27, 0xffff0000, v193\n"
      "s_cmp_eq_u32 s64, 3\n"
      "s_cbranch_scc0 NP15_%=\n"
      "s_mov_b64 exec, 0xff\n"
      "s_waitcnt vmcnt(5)\n"
      "s_branch PC18_%=\n"
      "PL16_%=:\n"
      "buffer_load_dwordx2 v[246:247], v252, s[60:63], s96 offen sc1\n"
      "s_waitcnt vmcnt(0)\n"
      "PC18_%=:\n"
      "v_cmp_ne_u32 vcc, v246, v251\n"
      "s_nop 1\n"
      "s_cbranch_vccz NE17_%=\n"
      "v_mov_b32 v251, v246\n"
      "buffer_store_dwordx2 v[250:251], v253, s[60:63], s96 offen sc1\n"
      "NE17_%=:\n"
      "v_cmp_ne_u32 vcc, v247, v250\n"
      "s_nop 1\n"
      "s_cbranch_vccz NP15_%=\n"
      "s_cmp_eq_u32 s87, 0\n"
      "s_cbranch_scc1 NP15_%=\n"
      "s_sub_u32 s87, s87, 1\n"
      "s_branch PL16_%=\n"
      "NP15_%=:\n"
      "s_mov_b64 exec, -1\n"
      "s_waitcnt vmcnt(9)\n"
      "s_barrier\n"
      "buffer_load_dwordx4 a[0:3], v234, s[60:63], s28 offen offset:0 sc1\n"
      "ds_read_b128 a[72:75], v56 offset:0\n"
      "ds_read_b128 a[76:79], v56 offset:4608\n"
      "buffer_load_dwordx4 a[4:7], v234, s[60:63], s28 offen offset:1024 sc1\n"
      "ds_read_b128 a[80:83], v56 offset:1024\n"
      "ds_read_b128 a[84:87], v56 offset:5632\n"
      "ds_read_b128 a[88:91], v56 offset:2048\n"
      "buffer_load_dwordx4 a[8:11], v234, s[60:63], s28 offen offset:2048 sc1\n"
      "ds_read_b128 a[92:95], v56 offset:6656\n"
      "ds_read_b128 a[96:99], v56 offset:3072\n"
      "buffer_load_dwordx4 a[12:15], v234, s[60:63], s28 offen offset:3072 sc1\n"
      "ds_read_b128 a[100:103], v56 offset:7680\n"
      "ds_read_b128 a[104:107], v57 offset:4096\n"
      "ds_read_b128 a[108:111], v57 offset:8704\n"
      "s_add_u32 s28, s28, 4096\n"
      "buffer_load_dwordx4 a[16:19], v234, s[60:63], s28 offen offset:0 sc1\n"
      "ds_read_b128 a[112:115], v57 offset:13312\n"
      "ds_read_b128 a[116:119], v57 offset:17920\n"
      "buffer_load_dwordx4 a[20:23], v234, s[60:63], s28 offen offset:1024 sc1\n"
      "ds_read_b128 a[120:123], v56 offset:12288\n"
      "ds_read_b128 a[124:127], v56 offset:16896\n"
      "ds_read_b128 a[128:131], v56 offset:11264\n"
      "buffer_load_dwordx4 a[24:27], v234, s[60:63], s28 offen offset:2048 sc1\n"
      "ds_read_b128 a[132:135], v56 offset:15872\n"
      "ds_read_b128 a[136:139], v56 offset:10240\n"
      "buffer_load_dwordx4 a[28:31], v234, s[60:63], s28 offen offset:3072 sc1\n"
      "ds_read_b128 a[140:143], v56 offset:14848\n"
      "ds_read_b128 a[144:147], v56 offset:9216\n"
      "ds_read_b128 a[148:151], v56 offset:13824\n"
      "s_add_u32 s28, s28, 4096\n"
      "buffer_load_dwordx4 a[32:35], v234, s[60:63], s28 offen offset:0 sc1\n"
      "ds_read_b128 a[152:155], v56 offset:18432\n"
      "ds_read_b128 a[156:159], v56 offset:23040\n"
      "buffer_load_dwordx4 a[36:39], v234, s[60:63], s28 offen offset:1024 sc1\n"
      "ds_read_b128 a[160:163], v56 offset:19456\n"
      "ds_read_b128 a[164:167], v56 offset:24064\n"
      "ds_read_b128 a[168:171], v56 offset:20480\n"
      "buffer_load_dwordx4 a[40:43], v234, s[60:63], s28 offen offset:2048 sc1\n"
      "ds_read_b128 a[172:175], v56 offset:25088\n"
      "ds_read_b128 a[176:179], v56 offset:21504\n"
      "buffer_load_dwordx4 a[44:47], v234, s[60:63], s28 offen offset:3072 sc1\n"
      "ds_read_b128 a[180:183], v56 offset:26112\n"
      "ds_read_b128 a[184:187], v57 offset:22528\n"
      "ds_read_b128 a[188:191], v57 offset:27136\n"
      "s_add_u32 s28, s28, 4096\n"
      "buffer_load_dwordx4 a[48:51], v234, s[60:63], s28 offen offset:0 sc1\n"
      "ds_read_b128 a[192:195], v57 offset:31744\n"
      "ds_read_b128 a[196:199], v57 offset:36352\n"
      "ds_read_b128 a[200:203], v56 offset:30720\n"
      "s_waitcnt vmcnt(12)\n"
      "s_waitcnt lgkmcnt(15)\n"
      "v_mfma_f32_16x16x32_bf16 v[8:11], a[72:75], a[0:3], v[8:11]\n"
      "buffer_load_dwordx4 a[52:55], v234, s[60:63], s28 offen offset:1024 sc1\n"
      "s_waitcnt vmcnt(12)\n"
      "v_mfma_f32_16x16x32_bf16 v[8:11], a[76:79], a[4:7], v[8:11]\n"
      "ds_read_b128 a[204:207], v56 offset:35328\n"
      "ds_read_b128 a[208:211], v56 offset:29696\n"
      "v_mfma_f32_16x16x32_bf16 v[12:15], a[80:83], a[0:3], v[12:15]\n"
      "ds_read_b128 a[212:215], v56 offset:34304\n"
      "v_mfma_f32_16x16x32_bf16 v[12:15], a[84:87], a[4:7], v[12:15]\n"
      "buffer_load_dwordx4 a[56:59], v234, s[60:63], s28 offen offset:2048 sc1\n"
      "v_mfma_f32_16x16x32_bf16 v[16:19], a[88:91], a[0:3], v[16:19]\n"
      "ds_read_b128 a[216:219], v56 offset:28672\n"
      "ds_read_b128 a[220:223], v56 offset:33280\n"
      "v_mfma_f32_16x16x32_bf16 v[16:19], a[92:95], a[4:7], v[16:19]\n"
      "ds_read_b128 a[224:227], v56 offset:27648\n"
      "v_mfma_f32_16x16x32_bf16 v[20:23], a[96:99], a[0:3], v[20:23]\n"
      "buffer_load_dwordx4 a[60:63], v234, s[60:63], s28 offen offset:3072 sc1\n"
      "v_mfma_f32_16x16x32_bf16 v[20:23], a[100:103], a[4:7], v[20:23]\n"
      "ds_read_b128 a[228:231], v56 offset:32256\n"
      "ds_read_b128 a[232:235], v56 offset:36864\n"
      "v_mfma_f32_16x16x32_bf16 v[24:27], a[104:107], a[0:3], v[24:27]\n"
      "s_waitcnt vmcnt(12)\n"
      "ds_read_b128 a[236:239], v56 offset:41472\n"
      "v_mfma_f32_16x16x32_bf16 v[24:27], a[108:111], a[4:7], v[24:27]\n"
      "buffer_load_dword a64, v65, s[60:63], s29 offen offset:0 sc1\n"
      "v_mfma_f32_16x16x32_bf16 v[24:27], a[112:115], a[8:11], v[24:27]\n"
      "ds_read_b128 a[240:243], v56 offset:37888\n"
      "ds_read_b128 a[244:247], v56 offset:42496\n"
      "v_mfma_f32_16x16x32_bf16 v[24:27], a[116:119], a[12:15], v[24:27]\n"
      "ds_read_b128 a[248:251], v56 offset:38912\n"
      "v_mfma_f32_16x16x32_bf16 v[20:23], a[120:123], a[8:11], v[20:23]\n"
      "buffer_load_dword a65, v65, s[60:63], s29 offen offset:128 sc1\n"
      "v_mfma_f32_16x16x32_bf16 v[20:23], a[124:127], a[12:15], v[20:23]\n"
      "ds_read_b128 a[252:255], v56 offset:43520\n"
      "ds_read_b128 a[72:75], v56 offset:39936\n"
      "v_mfma_f32_16x16x32_bf16 v[16:19], a[128:131], a[8:11], v[16:19]\n"
      "ds_read_b128 a[76:79], v56 offset:44544\n"
      "v_mfma_f32_16x16x32_bf16 v[16:19], a[132:135], a[12:15], v[16:19]\n"
      "buffer_load_dword a66, v65, s[60:63], s29 offen offset:256 sc1\n"
      "v_mfma_f32_16x16x32_bf16 v[12:15], a[136:139], a[8:11], v[12:15]\n"
      "ds_read_b128 a[80:83], v57 offset:40960\n"
      "ds_read_b128 a[84:87], v57 offset:45568\n"
      "v_mfma_f32_16x16x32_bf16 v[12:15], a[140:143], a[12:15], v[12:15]\n"
      "ds_read_b128 a[88:91], v57 offset:50176\n"
      "s_waitcnt lgkmcnt(15)\n"
      "v_mfma_f32_16x16x32_bf16 v[8:11], a[144:147], a[8:11], v[8:11]\n"
      "buffer_load_dword a67, v65, s[60:63], s29 offen offset:384 sc1\n"
      "v_mfma_f32_16x16x32_bf16 v[8:11], a[148:151], a[12:15], v[8:11]\n"
      "s_waitcnt vmcnt(14)\n"
      "ds_read_b128 a[92:95], v57 offset:54784\n"
      "ds_read_b128 a[96:99], v56 offset:49152\n"
      "v_mfma_f32_16x16x32_bf16 v[8:11], a[152:155], a[16:19], v[8:11]\n"
      "ds_read_b128 a[100:103], v56 offset:53760\n"
      "v_mfma_f32_16x16x32_bf16 v[8:11], a[156:159], a[20:23], v[8:11]\n"
      "buffer_load_dword a68, v65, s[60:63], s29 offen offset:2048 sc1\n"
      "v_mfma_f32_16x16x32_bf16 v[12:15], a[160:163], a[16:19], v[12:15]\n"
      "ds_read_b128 a[104:107], v56 offset:48128\n"
      "ds_read_b128 a[108:111], v56 offset:52736\n"
      "v_mfma_f32_16x16x32_bf16 v[12:15], a[164:167], a[20:23], v[12:15]\n"
      "ds_read_b128 a[112:115], v56 offset:47104\n"
      "v_mfma_f32_16x16x32_bf16 v[16:19], a[168:171], a[16:19], v[16:19]\n"
      "buffer_load_dword a69, v65, s[60:63], s29 offen offset:2176 sc1\n"
      "v_mfma_f32_16x16x32_bf16 v[16:19], a[172:175], a[20:23], v[16:19]\n"
      "ds_read_b128 a[116:119], v56 offset:51712\n"
      "ds_read_b128 a[120:123], v56 offset:46080\n"
      "v_mfma_f32_16x16x32_bf16 v[20:23], a[176:179], a[16:19], v[20:23]\n"
      "ds_read_b128 a[124:127], v56 offset:50688\n"
      "v_mfma_f32_16x16x32_bf16 v[20:23], a[180:183], a[20:23], v[20:23]\n"
      "buffer_load_dword a70, v65, s[60:63], s29 offen offset:2304 sc1\n"
      "v_mfma_f32_16x16x32_bf16 v[24:27], a[184:187], a[16:19], v[24:27]\n"
      "ds_read_b128 a[128:131], v56 offset:55296\n"
      "ds_read_b128 a[132:135], v56 offset:59904\n"
      "v_mfma_f32_16x16x32_bf16 v[24:27], a[188:191], a[20:23], v[24:27]\n"
      "s_waitcnt vmcnt(15)\n"
      "ds_read_b128 a[136:139], v56 offset:56320\n"
      "v_mfma_f32_16x16x32_bf16 v[24:27], a[192:195], a[24:27], v[24:27]\n"
      "buffer_load_dword a71, v65, s[60:63], s29 offen offset:2432 sc1\n"
      "v_mfma_f32_16x16x32_bf16 v[24:27], a[196:199], a[28:31], v[24:27]\n"
      "ds_read_b128 a[140:143], v56 offset:60928\n"
      "ds_read_b128 a[144:147], v56 offset:57344\n"
      "v_mfma_f32_16x16x32_bf16 v[20:23], a[200:203], a[24:27], v[20:23]\n"
      "ds_read_b128 a[148:151], v56 offset:61952\n"
      "v_mfma_f32_16x16x32_bf16 v[20:23], a[204:207], a[28:31], v[20:23]\n"
      "s_add_u32 m0, s95, 32768\n"
      "s_add_u32 s76, s94, 2359296\n"
      "ds_read_b128 a[152:155], v56 offset:58368\n"
      "v_mfma_f32_16x16x32_bf16 v[16:19], a[208:211], a[24:27], v[16:19]\n"
      "buffer_load_dwordx4 v240, s[48:51], s76 offen lds\n"
      "v_mfma_f32_16x16x32_bf16 v[16:19], a[212:215], a[28:31], v[16:19]\n"
      "ds_read_b128 a[156:159], v56 offset:62976\n"
      "ds_read_b128 a[160:163], v57 offset:59392\n"
      "s_waitcnt lgkmcnt(15)\n"
      "v_mfma_f32_16x16x32_bf16 v[12:15], a[216:219], a[24:27], v[12:15]\n"
      "ds_read_b128 a[164:167], v57 offset:64000\n"
      "v_mfma_f32_16x16x32_bf16 v[12:15], a[220:223], a[28:31], v[12:15]\n"
      "ds_read_b128 a[168:171], v59 offset:3072\n"
      "v_mfma_f32_16x16x32_bf16 v[8:11], a[224:227], a[24:27], v[8:11]\n"
      "buffer_load_dwordx4 v241, s[48:51], s76 offen offset:1024 lds\n"
      "v_mfma_f32_16x16x32_bf16 v[8:11], a[228:231], a[28:31], v[8:11]\n"
      "s_waitcnt vmcnt(16)\n"
      "ds_read_b128 a[172:175], v59 offset:7680\n"
      "ds_read_b128 a[176:179], v58 offset:2048\n"
      "v_mfma_f32_16x16x32_bf16 v[8:11], a[232:235], a[32:35], v[8:11]\n"
      "ds_read_b128 a[180:183], v58 offset:6656\n"
      "v_mfma_f32_16x16x32_bf16 v[8:11], a[236:239], a[36:39], v[8:11]\n"
      "ds_read_b128 a[184:187], v58 offset:1024\n"
      "v_mfma_f32_16x16x32_bf16 v[12:15], a[240:243], a[32:35], v[12:15]\n"
      "ds_read_b128 a[188:191], v58 offset:5632\n"
      "v_mfma_f32_16x16x32_bf16 v[12:15], a[244:247], a[36:39], v[12:15]\n"
      "s_add_u32 m0, s95, 40960\n"
      "ds_read_b128 a[192:195], v58 offset:0\n"
      "v_mfma_f32_16x16x32_bf16 v[16:19], a[248:251], a[32:35], v[16:19]\n"
      "s_add_u32 s76, s94, 2949120\n"
      "ds_read_b128 a[196:199], v58 offset:4608\n"
      "v_mfma_f32_16x16x32_bf16 v[16:19], a[252:255], a[36:39], v[16:19]\n"
      "buffer_load_dwordx4 v240, s[48:51], s76 offen lds\n"
      "v_mfma_f32_16x16x32_bf16 v[20:23], a[72:75], a[32:35], v[20:23]\n"
      "ds_read_b128 a[200:203], v56 offset:64512\n"
      "ds_read_b128 a[204:207], v58 offset:3584\n"
      "v_mfma_f32_16x16x32_bf16 v[20:23], a[76:79], a[36:39], v[20:23]\n"
      "ds_read_b128 a[208:211], v58 offset:8192\n"
      "v_mfma_f32_16x16x32_bf16 v[24:27], a[80:83], a[32:35], v[24:27]\n"
      "ds_read_b128 a[212:215], v58 offset:12800\n"
      "v_mfma_f32_16x16x32_bf16 v[24:27], a[84:87], a[36:39], v[24:27]\n"
      "s_waitcnt vmcnt(15)\n"
      "ds_read_b128 a[216:219], v58 offset:9216\n"
      "v_mfma_f32_16x16x32_bf16 v[24:27], a[88:91], a[40:43], v[24:27]\n"
      "buffer_load_dwordx4 v241, s[48:51], s76 offen offset:1024 lds\n"
      "v_mfma_f32_16x16x32_bf16 v[24:27], a[92:95], a[44:47], v[24:27]\n"
      "ds_read_b128 a[220:223], v58 offset:13824\n"
      "ds_read_b128 a[224:227], v58 offset:10240\n"
      "v_mfma_f32_16x16x32_bf16 v[20:23], a[96:99], a[40:43], v[20:23]\n"
      "ds_read_b128 a[228:231], v58 offset:14848\n"
      "v_mfma_f32_16x16x32_bf16 v[20:23], a[100:103], a[44:47], v[20:23]\n"
      "ds_read_b128 a[232:235], v58 offset:11264\n"
      "s_waitcnt lgkmcnt(15)\n"
      "v_mfma_f32_16x16x32_bf16 v[16:19], a[104:107], a[40:43], v[16:19]\n"
      "s_add_u32 m0, s95, 49152\n"
      "ds_read_b128 a[236:239], v58 offset:15872\n"
      "v_mfma_f32_16x16x32_bf16 v[16:19], a[108:111], a[44:47], v[16:19]\n"
      "s_add_u32 s76, s94, 3538944\n"
      "ds_read_b128 a[240:243], v59 offset:12288\n"
      "v_mfma_f32_16x16x32_bf16 v[12:15], a[112:115], a[40:43], v[12:15]\n"
      "ds_read_b128 a[244:247], v59 offset:16896\n"
      "v_mfma_f32_16x16x32_bf16 v[12:15], a[116:119], a[44:47], v[12:15]\n"
      "buffer_load_dwordx4 v240, s[48:51], s76 offen lds\n"
      "v_mfma_f32_16x16x32_bf16 v[8:11], a[120:123], a[40:43], v[8:11]\n"
      "v_mfma_f32_16x16x32_bf16 v[8:11], a[124:127], a[44:47], v[8:11]\n"
      "s_waitcnt vmcnt(15)\n"
      "v_mfma_f32_16x16x32_bf16 v[8:11], a[128:131], a[48:51], v[8:11]\n"
      "v_mfma_f32_16x16x32_bf16 v[8:11], a[132:135], a[52:55], v[8:11]\n"
      "buffer_load_dwordx4 v241, s[48:51], s76 offen offset:1024 lds\n"
      "v_mfma_f32_16x16x32_bf16 v[12:15], a[136:139], a[48:51], v[12:15]\n"
      "v_mfma_f32_16x16x32_bf16 v[12:15], a[140:143], a[52:55], v[12:15]\n"
      "v_mfma_f32_16x16x32_bf16 v[16:19], a[144:147], a[48:51], v[16:19]\n"
      "v_mfma_f32_16x16x32_bf16 v[16:19], a[148:151], a[52:55], v[16:19]\n"
      "s_add_u32 m0, s95, 57344\n"
      "v_mfma_f32_16x16x32_bf16 v[20:23], a[152:155], a[48:51], v[20:23]\n"
      "s_add_u32 s76, s94, 4128768\n"
      "v_mfma_f32_16x16x32_bf16 v[20:23], a[156:159], a[52:55], v[20:23]\n"
      "v_mfma_f32_16x16x32_bf16 v[24:27], a[160:163], a[48:51], v[24:27]\n"
      "buffer_load_dwordx4 v240, s[48:51], s76 offen lds\n"
      "v_mfma_f32_16x16x32_bf16 v[24:27], a[164:167], a[52:55], v[24:27]\n"
      "s_waitcnt vmcnt(15)\n"
      "s_waitcnt lgkmcnt(0)\n"
      "s_barrier\n"
      "v_mfma_f32_16x16x32_bf16 v[24:27], a[168:171], a[56:59], v[24:27]\n"
      "v_mfma_f32_16x16x32_bf16 v[24:27], a[172:175], a[60:63], v[24:27]\n"
      "v_mfma_f32_16x16x32_bf16 v[20:23], a[176:179], a[56:59], v[20:23]\n"
      "buffer_load_dwordx4 v241, s[48:51], s76 offen offset:1024 lds\n"
      "v_mfma_f32_16x16x32_bf16 v[20:23], a[180:183], a[60:63], v[20:23]\n"
      "v_mfma_f32_16x16x32_bf16 v[16:19], a[184:187], a[56:59], v[16:19]\n"
      "v_mfma_f32_16x16x32_bf16 v[16:19], a[188:191], a[60:63], v[16:19]\n"
      "v_mfma_f32_16x16x32_bf16 v[12:15], a[192:195], a[56:59], v[12:15]\n"
      "s_add_u32 m0, s95, 65536\n"
      "v_mfma_f32_16x16x32_bf16 v[12:15], a[196:199], a[60:63], v[12:15]\n"
      "s_add_u32 s76, s94, 4718592\n"
      "v_mfma_f32_16x16x32_bf16 v[8:11], a[200:203], a[56:59], v[8:11]\n"
      "s_waitcnt vmcnt(8)\n"
      "v_mfma_f32_16x16x32_bf16 v[8:11], a[204:207], a[60:63], v[8:11]\n"
      "buffer_load_dwordx4 v240, s[48:51], s76 offen lds\n"
      "v_mfma_f32_16x16x32_bf16 v[8:11], a[208:211], a[64:67], v[8:11]\n"
      "v_mfma_f32_16x16x32_bf16 v[8:11], a[212:215], a[68:71], v[8:11]\n"
      "v_mfma_f32_16x16x32_bf16 v[12:15], a[216:219], a[64:67], v[12:15]\n"
      "v_mfma_f32_16x16x32_bf16 v[12:15], a[220:223], a[68:71], v[12:15]\n"
      "buffer_load_dwordx4 v241, s[48:51], s76 offen offset:1024 lds\n"
      "v_mfma_f32_16x16x32_bf16 v[16:19], a[224:227], a[64:67], v[16:19]\n"
      "v_mfma_f32_16x16x32_bf16 v[16:19], a[228:231], a[68:71], v[16:19]\n"
      "v_mfma_f32_16x16x32_bf16 v[20:23], a[232:235], a[64:67], v[20:23]\n"
      "v_mfma_f32_16x16x32_bf16 v[20:23], a[236:239], a[68:71], v[20:23]\n"
      "v_mfma_f32_16x16x32_bf16 v[24:27], a[240:243], a[64:67], v[24:27]\n"
      "v_mfma_f32_16x16x32_bf16 v[24:27], a[244:247], a[68:71], v[24:27]\n"
      "v_cvt_pk_bf16_f32 v28, v8, v9\n"
      "v_cvt_pk_bf16_f32 v29, v10, v11\n"
      "v_cvt_pk_bf16_f32 v30, v12, v13\n"
      "v_cvt_pk_bf16_f32 v31, v14, v15\n"
      "v_cvt_pk_bf16_f32 v32, v16, v17\n"
      "v_cvt_pk_bf16_f32 v33, v18, v19\n"
      "v_cvt_pk_bf16_f32 v34, v20, v21\n"
      "v_cvt_pk_bf16_f32 v35, v22, v23\n"
      "v_cvt_pk_bf16_f32 v36, v24, v25\n"
      "v_cvt_pk_bf16_f32 v37, v26, v27\n"
      "v_and_b32 v36, v36, v63\n"
      "v_and_b32 v37, v37, v63\n"
      "v_permlane16_swap_b32 v28, v30\n"
      "v_permlane16_swap_b32 v29, v31\n"
      "buffer_store_dwordx4 v[28:31], v60, s[60:63], s30 offen offset:0 sc1\n"
      "v_dot2c_f32_bf16 v68, v28, v64\n"
      "v_dot2c_f32_bf16 v69, v28, v28\n"
      "v_dot2c_f32_bf16 v68, v29, v64\n"
      "v_dot2c_f32_bf16 v69, v29, v29\n"
      "v_dot2c_f32_bf16 v68, v30, v64\n"
      "v_dot2c_f32_bf16 v69, v30, v30\n"
      "v_dot2c_f32_bf16 v68, v31, v64\n"
      "v_dot2c_f32_bf16 v69, v31, v31\n"
      "v_permlane16_swap_b32 v32, v34\n"
      "v_permlane16_swap_b32 v33, v35\n"
      "buffer_store_dwordx4 v[32:35], v60, s[60:63], s30 offen offset:1024 sc1\n"
      "v_dot2c_f32_bf16 v68, v32, v64\n"
      "v_dot2c_f32_bf16 v69, v32, v32\n"
      "v_dot2c_f32_bf16 v68, v33, v64\n"
      "v_dot2c_f32_bf16 v69, v33, v33\n"
      "v_dot2c_f32_bf16 v68, v34, v64\n"
      "v_dot2c_f32_bf16 v69, v34, v34\n"
      "v_dot2c_f32_bf16 v68, v35, v64\n"
      "v_dot2c_f32_bf16 v69, v35, v35\n"
      "v_permlane16_swap_b32 v36, v38\n"
      "v_permlane16_swap_b32 v37, v39\n"
      "buffer_store_dwordx4 v[36:39], v61, s[60:63], s30 offen offset:2048 sc1\n"
      "v_dot2c_f32_bf16 v68, v36, v64\n"
      "v_dot2c_f32_bf16 v69, v36, v36\n"
      "v_dot2c_f32_bf16 v68, v37, v64\n"
      "v_dot2c_f32_bf16 v69, v37, v37\n"
      "v_dot2c_f32_bf16 v68, v38, v64\n"
      "v_dot2c_f32_bf16 v69, v38, v38\n"
      "v_dot2c_f32_bf16 v68, v39, v64\n"
      "v_dot2c_f32_bf16 v69, v39, v39\n"
      "s_nop 1\n"
      "v_mov_b32 v70, v68\n"
      "v_mov_b32 v71, v69\n"
      "s_nop 0\n"
      "v_permlane16_swap_b32 v70, v68\n"
      "v_permlane16_swap_b32 v71, v69\n"
      "v_add_f32 v68, v68, v70\n"
      "v_add_f32 v69, v69, v71\n"
      "v_mov_b32 v70, v68\n"
      "v_mov_b32 v71, v69\n"
      "s_nop 0\n"
      "v_permlane32_swap_b32 v70, v68\n"
      "v_permlane32_swap_b32 v71, v69\n"
      "v_add_f32 v68, v68, v70\n"
      "v_add_f32 v69, v69, v71\n"
      "buffer_store_dwordx2 v[68:69], v62, s[60:63], s31 offen sc1\n"
      "s_mov_b32 exec_hi, 0\n"
      "buffer_load_dword v216, v217, s[60:63], s96 offen sc1\n"
      "s_mov_b64 exec, -1\n"
      "s_add_u32 m0, s95, 98304\n"
      "s_add_u32 s76, s94, 128\n"
      "buffer_load_dwordx4 v240, s[48:51], s76 offen lds\n"
      "ds_read_b64_tr_b16 v[220:221], v81 offset:0\n"
      "buffer_load_dwordx4 v241, s[48:51], s76 offen offset:1024 lds\n"
      "ds_read_b64_tr_b16 v[222:223], v81 offset:512\n"
      "ds_read_b64_tr_b16 v[228:229], v81 offset:4096\n"
      "s_add_u32 m0, s95, 106496\n"
      "s_add_u32 s76, s94, 589952\n"
      "buffer_load_dwordx4 v240, s[48:51], s76 offen lds\n"
      "ds_read_b64_tr_b16 v[230:231], v81 offset:4608\n"
      "buffer_load_dwordx4 v241, s[48:51], s76 offen offset:1024 lds\n"
      "ds_read_b64_tr_b16 v[236:237], v81 offset:8192\n"
      "ds_read_b64_tr_b16 v[238:239], v81 offset:8704\n"
      "s_add_u32 m0, s95, 114688\n"
      "s_add_u32 s76, s94, 1179776\n"
      "buffer_load_dwordx4 v240, s[48:51], s76 offen lds\n"
      "ds_read_b64_tr_b16 v[244:245], v81 offset:12288\n"
      "ds_read_b64_tr_b16 v[246:247], v81 offset:12800\n"
      "buffer_load_dwordx4 v241, s[48:51], s76 offen offset:1024 lds\n"
      "ds_read_b64_tr_b16 v[96:97], v81 offset:16384\n"
      "s_add_u32 m0, s95, 122880\n"
      "s_add_u32 s76, s94, 1769600\n"
      "buffer_load_dwordx4 v240, s[48:51], s76 offen lds\n"
      "ds_read_b64_tr_b16 v[98:99], v81 offset:16896\n"
      "ds_read_b64_tr_b16 v[104:105], v81 offset:20480\n"
      "buffer_load_dwordx4 v241, s[48:51], s76 offen offset:1024 lds\n"
      "ds_read_b64_tr_b16 v[106:107], v81 offset:20992\n"
      "s_add_u32 m0, s95, 131072\n"
      "s_add_u32 s76, s94, 2359424\n"
      "buffer_load_dwordx4 v240, s[48:51], s76 offen lds\n"
      "ds_read_b64_tr_b16 v[112:113], v81 offset:24576\n"
      "ds_read_b64_tr_b16 v[114:115], v81 offset:25088\n"
      "buffer_load_dwordx4 v241, s[48:51], s76 offen offset:1024 lds\n"
      "ds_read_b64_tr_b16 v[120:121], v81 offset:28672\n"
      "ds_read_b64_tr_b16 v[122:123], v81 offset:29184\n"
      "s_waitcnt vmcnt(11)\n"
      "s_barrier\n"
      "s_cmp_eq_u32 s64, 3\n"
      "s_cbranch_scc0 NS19_%=\n"
      "s_mov_b64 exec, 1\n"
      "buffer_store_dword v66, off, s[60:63], s97 sc1\n"
      "s_mov_b64 exec, -1\n"
      "NS19_%=:\n"
      "s_cmp_eq_u32 s64, 3\n"
      "s_cbranch_scc0 NP20_%=\n"
      "s_waitcnt vmcnt(10)\n"
      "s_branch PC22_%=\n"
      "PL21_%=:\n"
      "s_mov_b32 exec_hi, 0\n"
      "buffer_load_dword v216, v217, s[60:63], s96 offen sc1\n"
      "s_mov_b64 exec, -1\n"
      "s_waitcnt vmcnt(0)\n"
      "PC22_%=:\n"
      "v_cmp_eq_u32 vcc, 0, v216\n"
      "s_nop 1\n"
      "s_and_b32 vcc_lo, vcc_lo, -1\n"
      "s_cbranch_scc0 NP20_%=\n"
      "s_cmp_eq_u32 s87, 0\n"
      "s_cbranch_scc1 NP20_%=\n"
      "s_sub_u32 s87, s87, 1\n"
      "s_branch PL21_%=\n"
      "NP20_%=:\n"
      "ds_read_b64_tr_b16 v[128:129], v81 offset:32768\n"
      "ds_read_b64_tr_b16 v[130:131], v81 offset:33280\n"
      "ds_read_b64_tr_b16 v[136:137], v81 offset:36864\n"
      "ds_read_b64_tr_b16 v[138:139], v81 offset:37376\n"
      "ds_read_b64_tr_b16 v[144:145], v81 offset:40960\n"
      "s_add_u32 m0, s95, 139264\n"
      "s_add_u32 s76, s94, 2949248\n"
      "buffer_load_dwordx4 v240, s[48:51], s76 offen lds\n"
      "ds_read_b64_tr_b16 v[146:147], v81 offset:41472\n"
      "ds_read_b64_tr_b16 v[152:153], v81 offset:45056\n"
      "ds_read_b64_tr_b16 v[154:155], v81 offset:45568\n"
      "ds_read_b64_tr_b16 v[160:161], v81 offset:49152\n"
      "buffer_load_dwordx4 v241, s[48:51], s76 offen offset:1024 lds\n"
      "ds_read_b64_tr_b16 v[162:163], v81 offset:49664\n"
      "ds_read_b64_tr_b16 v[168:169], v81 offset:53248\n"
      "ds_read_b64_tr_b16 v[170:171], v81 offset:53760\n"
      "ds_read_b64_tr_b16 v[176:177], v81 offset:57344\n"
      "s_add_u32 m0, s95, 147456\n"
      "s_add_u32 s76, s94, 3539072\n"
      "buffer_load_dwordx4 v240, s[48:51], s76 offen lds\n"
      "ds_read_b64_tr_b16 v[178:179], v81 offset:57856\n"
      "ds_read_b64_tr_b16 v[184:185], v81 offset:61440\n"
      "ds_read_b64_tr_b16 v[186:187], v81 offset:61952\n"
      "ds_read_b64_tr_b16 v[192:193], v82 offset:0\n"
      "buffer_load_dwordx4 v241, s[48:51], s76 offen offset:1024 lds\n"
      "ds_read_b64_tr_b16 v[194:195], v82 offset:512\n"
      "ds_read_b64_tr_b16 v[200:201], v82 offset:4096\n"
      "ds_read_b64_tr_b16 v[202:203], v82 offset:4608\n"
      "v_mov_b32 v40, 0x3e803e80\n"
      "v_mov_b32 v41, 0x3e803e80\n"
      "s_add_u32 m0, s95, 90112\n"
      "s_add_u32 s76, s94, 4128896\n"
      "buffer_load_dwordx4 v240, s[48:51], s76 offen lds\n"
      "v_mov_b32 v42, 0x3e803e80\n"
      "v_mov_b32 v43, 0x3e803e80\n"
      "s_waitcnt lgkmcnt(15)\n"
      "s_nop 0\n"
      "v_mfma_f32_16x16x32_bf16 v[44:47], v[40:43], v[220:223], 0\n"
      "v_mfma_f32_16x16x32_bf16 v[44:47], v[40:43], v[228:231], v[44:47]\n"
      "buffer_load_dwordx4 v241, s[48:51], s76 offen offset:1024 lds\n"
      "v_mfma_f32_16x16x32_bf16 v[44:47], v[40:43], v[236:239], v[44:47]\n"
      "v_mfma_f32_16x16x32_bf16 v[44:47], v[40:43], v[244:247], v[44:47]\n"
      "v_mfma_f32_16x16x32_bf16 v[44:47], v[40:43], v[96:99], v[44:47]\n"
      "v_mfma_f32_16x16x32_bf16 v[44:47], v[40:43], v[104:107], v[44:47]\n"
      "s_add_u32 m0, s95, 155648\n"
      "s_add_u32 s76, s94, 4718720\n"
      "buffer_load_dwordx4 v240, s[48:51], s76 offen lds\n"
      "v_mfma_f32_16x16x32_bf16 v[44:47], v[40:43], v[112:115], v[44:47]\n"
      "v_mfma_f32_16x16x32_bf16 v[44:47], v[40:43], v[120:123], v[44:47]\n"
      "v_mfma_f32_16x16x32_bf16 v[44:47], v[40:43], v[128:131], v[44:47]\n"
      "v_mfma_f32_16x16x32_bf16 v[44:47], v[40:43], v[136:139], v[44:47]\n"
      "buffer_load_dwordx4 v241, s[48:51], s76 offen offset:1024 lds\n"
      "s_waitcnt lgkmcnt(14)\n"
      "v_mfma_f32_16x16x32_bf16 v[44:47], v[40:43], v[144:147], v[44:47]\n"
      "s_waitcnt lgkmcnt(12)\n"
      "v_mfma_f32_16x16x32_bf16 v[44:47], v[40:43], v[152:155], v[44:47]\n"
      "s_waitcnt lgkmcnt(10)\n"
      "v_mfma_f32_16x16x32_bf16 v[44:47], v[40:43], v[160:163], v[44:47]\n"
      "s_waitcnt lgkmcnt(8)\n"
      "v_mfma_f32_16x16x32_bf16 v[44:47], v[40:43], v[168:171], v[44:47]\n"
      "s_waitcnt lgkmcnt(6)\n"
      "v_mfma_f32_16x16x32_bf16 v[44:47], v[40:43], v[176:179], v[44:47]\n"
      "s_waitcnt lgkmcnt(4)\n"
      "v_mfma_f32_16x16x32_bf16 v[44:47], v[40:43], v[184:187], v[44:47]\n"
      "s_waitcnt lgkmcnt(2)\n"
      "v_mfma_f32_16x16x32_bf16 v[44:47], v[40:43], v[192:195], v[44:47]\n"
      "s_waitcnt lgkmcnt(0)\n"
      "v_mfma_f32_16x16x32_bf16 v[44:47], v[40:43], v[200:203], v[44:47]\n"
      "v_mov_b32 v210, 0\n"
      "s_mov_b64 exec, 0xff\n"
      "buffer_load_dword v4, v219, s[60:63], s96 offen sc1\n"
      "s_mov_b64 exec, -1\n"
      "v_mov_b32 v211, 0\n"
      "s_nop 2\n"
      "v_cvt_pk_bf16_f32 v208, v44, v44\n"
      "v_and_b32 v45, 0xffff0000, v208\n"
      "v_sub_f32 v45, v44, v45\n"
      "v_cvt_pk_bf16_f32 v208, v44, v45\n"
      "v_mov_b32 v209, v208\n"
      "s_cmp_eq_u32 s64, 3\n"
      "s_cbranch_scc0 NP23_%=\n"
      "s_waitcnt vmcnt(0)\n"
      "s_branch PC25_%=\n"
      "PL24_%=:\n"
      "s_mov_b64 exec, 0xff\n"
      "buffer_load_dword v4, v219, s[60:63], s96 offen sc1\n"
      "s_mov_b64 exec, -1\n"
      "s_waitcnt vmcnt(0)\n"
      "PC25_%=:\n"
      "v_cmp_eq_u32 vcc, 0, v4\n"
      "s_nop 1\n"
      "s_and_b32 vcc_lo, vcc_lo, 255\n"
      "s_cbranch_scc0 NP23_%=\n"
      "s_cmp_eq_u32 s87, 0\n"
      "s_cbranch_scc1 NP23_%=\n"
      "s_sub_u32 s87, s87, 1\n"
      "s_branch PL24_%=\n"
      "NP23_%=:\n"
      "s_waitcnt vmcnt(1)\n"
      "s_barrier\n"
      "s_add_u32 m0, s22, 0\n"
      "s_nop 0\n"
      "buffer_load_dwordx4 v80, s[60:63], s20 offen offset:0 sc1 lds\n"
      "buffer_load_dwordx4 v80, s[60:63], s20 offen offset:1024 sc1 lds\n"
      "buffer_load_dwordx2 v[72:73], v7, s[60:63], s21 offen offset:0 sc1\n"
      "buffer_load_dwordx2 v[74:75], v7, s[60:63], s21 offen offset:512 sc1\n"
      "buffer_load_dwordx4 v80, s[60:63], s20 offen offset:2048 sc1 lds\n"
      "buffer_load_dwordx4 v80, s[60:63], s20 offen offset:3072 sc1 lds\n"
      "s_add_u32 m0, s22, 4096\n"
      "s_add_u32 s20, s20, 4096\n"
      "buffer_load_dwordx4 v80, s[60:63], s20 offen offset:0 sc1 lds\n"
      "buffer_load_dwordx4 v80, s[60:63], s20 offen offset:1024 sc1 lds\n"
      "buffer_load_dwordx4 v80, s[60:63], s20 offen offset:2048 sc1 lds\n"
      "buffer_load_dwordx4 v80, s[60:63], s20 offen offset:3072 sc1 lds\n"
      "s_add_u32 m0, s22, 8192\n"
      "s_add_u32 s20, s20, 4096\n"
      "buffer_load_dwordx4 v80, s[60:63], s20 offen offset:0 sc1 lds\n"
      "buffer_load_dwordx4 v80, s[60:63], s20 offen offset:1024 sc1 lds\n"
      "ds_read_b64_tr_b16 v[224:225], v82 offset:32768\n"
      "ds_read_b64_tr_b16 v[226:227], v82 offset:33280\n"
      "ds_read_b64_tr_b16 v[232:233], v82 offset:36864\n"
      "ds_read_b64_tr_b16 v[234:235], v82 offset:37376\n"
      "ds_read_b64_tr_b16 v[240:241], v82 offset:40960\n"
      "ds_read_b64_tr_b16 v[242:243], v82 offset:41472\n"
      "ds_read_b64_tr_b16 v[248:249], v82 offset:45056\n"
      "ds_read_b64_tr_b16 v[250:251], v82 offset:45568\n"
      "ds_read_b64_tr_b16 v[100:101], v82 offset:49152\n"
      "ds_read_b64_tr_b16 v[102:103], v82 offset:49664\n"
      "ds_read_b64_tr_b16 v[108:109], v82 offset:53248\n"
      "ds_read_b64_tr_b16 v[110:111], v82 offset:53760\n"
      "ds_read_b64_tr_b16 v[116:117], v82 offset:57344\n"
      "ds_read_b64_tr_b16 v[118:119], v82 offset:57856\n"
      "ds_read_b64_tr_b16 v[124:125], v82 offset:61440\n"
      "ds_read_b64_tr_b16 v[126:127], v82 offset:61952\n"
      "ds_read_b64_tr_b16 v[132:133], v83 offset:0\n"
      "ds_read_b64_tr_b16 v[134:135], v83 offset:512\n"
      "ds_read_b64_tr_b16 v[140:141], v83 offset:4096\n"
      "ds_read_b64_tr_b16 v[142:143], v83 offset:4608\n"
      "ds_read_b64_tr_b16 v[148:149], v83 offset:8192\n"
      "ds_read_b64_tr_b16 v[150:151], v83 offset:8704\n"
      "ds_read_b64_tr_b16 v[156:157], v83 offset:12288\n"
      "ds_read_b64_tr_b16 v[158:159], v83 offset:12800\n"
      "ds_read_b64_tr_b16 v[164:165], v83 offset:16384\n"
      "ds_read_b64_tr_b16 v[166:167], v83 offset:16896\n"
      "ds_read_b64_tr_b16 v[172:173], v83 offset:20480\n"
      "ds_read_b64_tr_b16 v[174:175], v83 offset:20992\n"
      "ds_read_b64_tr_b16 v[180:181], v82 offset:24576\n"
      "ds_read_b64_tr_b16 v[182:183], v82 offset:25088\n"
      "ds_read_b64_tr_b16 v[188:189], v82 offset:28672\n"
      "ds_read_b64_tr_b16 v[190:191], v82 offset:29184\n"
      "ds_read_b64_tr_b16 v[196:197], v83 offset:24576\n"
      "ds_read_b64_tr_b16 v[198:199], v83 offset:25088\n"
      "ds_read_b64_tr_b16 v[204:205], v83 offset:28672\n"
      "ds_read_b64_tr_b16 v[206:207], v83 offset:29184\n"
      "s_waitcnt vmcnt(10)\n"
      "s_barrier\n"
      "ds_read_b128 a[0:3], v91 offset:0\n"
      "ds_read_b128 a[16:19], v91 offset:1024\n"
      "ds_read_b128 a[4:7], v91 offset:18432\n"
      "ds_read_b128 a[20:23], v91 offset:19456\n"
      "ds_read_b128 a[8:11], v91 offset:36864\n"
      "ds_read_b128 a[24:27], v91 offset:37888\n"
      "ds_read_b128 a[12:15], v84 offset:0\n"
      "ds_read_b128 a[28:31], v84 offset:1024\n"
      "s_waitcnt lgkmcnt(7)\n"
      "v_mfma_f32_16x16x32_bf16 v[8:11], v[220:223], a[0:3], 0\n"
      "buffer_load_dwordx4 v80, s[60:63], s20 offen offset:2048 sc1 lds\n"
      "s_waitcnt lgkmcnt(6)\n"
      "v_mfma_f32_16x16x32_bf16 v[8:11], v[228:231], a[16:19], v[8:11]\n"
      "buffer_load_dwordx4 v80, s[60:63], s20 offen offset:3072 sc1 lds\n"
      "s_waitcnt lgkmcnt(5)\n"
      "v_mfma_f32_16x16x32_bf16 v[12:15], v[220:223], a[4:7], 0\n"
      "s_waitcnt lgkmcnt(4)\n"
      "v_mfma_f32_16x16x32_bf16 v[12:15], v[228:231], a[20:23], v[12:15]\n"
      "s_waitcnt vmcnt(8)\n"
      "s_barrier\n"
      "s_waitcnt lgkmcnt(3)\n"
      "v_mfma_f32_16x16x32_bf16 v[16:19], v[220:223], a[8:11], 0\n"
      "s_add_u32 m0, s22, 12288\n"
      "ds_read_b128 a[32:35], v91 offset:2048\n"
      "ds_read_b128 a[48:51], v91 offset:3072\n"
      "s_waitcnt lgkmcnt(4)\n"
      "v_mfma_f32_16x16x32_bf16 v[16:19], v[228:231], a[24:27], v[16:19]\n"
      "s_add_u32 s20, s20, 4096\n"
      "ds_read_b128 a[36:39], v91 offset:20480\n"
      "s_waitcnt lgkmcnt(4)\n"
      "v_mfma_f32_16x16x32_bf16 v[20:23], v[220:223], a[12:15], 0\n"
      "buffer_load_dwordx4 v80, s[60:63], s20 offen offset:0 sc1 lds\n"
      "s_waitcnt lgkmcnt(3)\n"
      "v_mfma_f32_16x16x32_bf16 v[20:23], v[228:231], a[28:31], v[20:23]\n"
      "ds_read_b128 a[52:55], v91 offset:21504\n"
      "ds_read_b128 a[40:43], v91 offset:38912\n"
      "v_mfma_f32_16x16x32_bf16 v[48:51], v[40:43], v[224:227], 0\n"
      "v_mfma_f32_16x16x32_bf16 v[48:51], v[40:43], v[232:235], v[48:51]\n"
      "v_mfma_f32_16x16x32_bf16 v[24:27], v[224:227], a[0:3], 0\n"
      "v_add_f32 v72, v72, v74\n"
      "v_add_f32 v73, v73, v75\n"
      "v_mfma_f32_16x16x32_bf16 v[24:27], v[232:235], a[16:19], v[24:27]\n"
      "buffer_load_dwordx4 v80, s[60:63], s20 offen offset:1024 sc1 lds\n"
      "v_mfma_f32_16x16x32_bf16 v[28:31], v[224:227], a[4:7], 0\n"
      "ds_read_b128 a[56:59], v91 offset:39936\n"
      "ds_read_b128 a[44:47], v84 offset:2048\n"
      "v_mfma_f32_16x16x32_bf16 v[28:31], v[232:235], a[20:23], v[28:31]\n"
      "ds_read_b128 a[60:63], v84 offset:3072\n"
      "v_mov_b32 v74, v72\n"
      "v_mfma_f32_16x16x32_bf16 v[32:35], v[224:227], a[8:11], 0\n"
      "buffer_load_dwordx4 v80, s[60:63], s20 offen offset:2048 sc1 lds\n"
      "v_mfma_f32_16x16x32_bf16 v[32:35], v[232:235], a[24:27], v[32:35]\n"
      "v_mov_b32 v75, v73\n"
      "v_mfma_f32_16x16x32_bf16 v[36:39], v[224:227], a[12:15], 0\n"
      "s_waitcnt lgkmcnt(6)\n"
      "v_permlane16_swap_b32 v74, v72\n"
      "v_mfma_f32_16x16x32_bf16 v[36:39], v[232:235], a[28:31], v[36:39]\n"
      "buffer_load_dwordx4 v80, s[60:63], s20 offen offset:3072 sc1 lds\n"
      "v_mfma_f32_16x16x32_bf16 v[8:11], v[236:239], a[32:35], v[8:11]\n"
      "v_permlane16_swap_b32 v75, v73\n"
      "v_mfma_f32_16x16x32_bf16 v[8:11], v[244:247], a[48:51], v[8:11]\n"
      "v_add_f32 v72, v72, v74\n"
      "v_add_f32 v73, v73, v75\n"
      "s_waitcnt lgkmcnt(5)\n"
      "v_mfma_f32_16x16x32_bf16 v[12:15], v[236:239], a[36:39], v[12:15]\n"
      "s_add_u32 m0, s22, 16384\n"
      "v_mov_b32 v74, v72\n"
      "v_mov_b32 v75, v73\n"
      "s_waitcnt lgkmcnt(4)\n"
      "v_mfma_f32_16x16x32_bf16 v[12:15], v[244:247], a[52:55], v[12:15]\n"
      "s_add_u32 s20, s20, 4096\n"
      "v_permlane32_swap_b32 v74, v72\n"
      "s_waitcnt vmcnt(8)\n"
      "s_barrier\n"
      "s_waitcnt lgkmcnt(3)\n"
      "v_mfma_f32_16x16x32_bf16 v[16:19], v[236:239], a[40:43], v[16:19]\n"
      "buffer_load_dwordx4 v80, s[60:63], s20 offen offset:0 sc1 lds\n"
      "s_waitcnt lgkmcnt(2)\n"
      "v_mfma_f32_16x16x32_bf16 v[16:19], v[244:247], a[56:59], v[16:19]\n"
      "ds_read_b128 a[64:67], v91 offset:4096\n"
      "ds_read_b128 a[80:83], v91 offset:5120\n"
      "s_waitcnt lgkmcnt(3)\n"
      "v_mfma_f32_16x16x32_bf16 v[20:23], v[236:239], a[44:47], v[20:23]\n"
      "ds_read_b128 a[68:71], v91 offset:22528\n"
      "ds_read_b128 a[84:87], v91 offset:23552\n"
      "s_waitcnt lgkmcnt(4)\n"
      "v_mfma_f32_16x16x32_bf16 v[20:23], v[244:247], a[60:63], v[20:23]\n"
      "buffer_load_dwordx4 v80, s[60:63], s20 offen offset:1024 sc1 lds\n"
      "v_mfma_f32_16x16x32_bf16 v[48:51], v[40:43], v[240:243], v[48:51]\n"
      "v_mfma_f32_16x16x32_bf16 v[48:51], v[40:43], v[248:251], v[48:51]\n"
      "v_mfma_f32_16x16x32_bf16 v[24:27], v[240:243], a[32:35], v[24:27]\n"
      "ds_read_b128 a[72:75], v91 offset:40960\n"
      "v_mfma_f32_16x16x32_bf16 v[24:27], v[248:251], a[48:51], v[24:27]\n"
      "ds_read_b128 a[88:91], v91 offset:41984\n"
      "v_mfma_f32_16x16x32_bf16 v[28:31], v[240:243], a[36:39], v[28:31]\n"
      "ds_read_b128 a[76:79], v84 offset:4096\n"
      "v_mfma_f32_16x16x32_bf16 v[28:31], v[248:251], a[52:55], v[28:31]\n"
      "ds_read_b128 a[92:95], v84 offset:5120\n"
      "v_mfma_f32_16x16x32_bf16 v[32:35], v[240:243], a[40:43], v[32:35]\n"
      "v_permlane32_swap_b32 v75, v73\n"
      "v_mfma_f32_16x16x32_bf16 v[32:35], v[248:251], a[56:59], v[32:35]\n"
      "v_add_f32 v72, v72, v74\n"
      "v_add_f32 v73, v73, v75\n"
      "v_mfma_f32_16x16x32_bf16 v[36:39], v[240:243], a[44:47], v[36:39]\n"
      "v_mul_f32 v72, 0x3ae38e39, v72\n"
      "v_mul_f32 v73, 0x3ae38e39, v73\n"
      "v_mfma_f32_16x16x32_bf16 v[36:39], v[248:251], a[60:63], v[36:39]\n"
      "s_waitcnt lgkmcnt(6)\n"
      "v_fma_f32 v73, -v72, v72, v73\n"
      "v_max_f32 v73, 0, v73\n"
      "v_mfma_f32_16x16x32_bf16 v[8:11], v[96:99], a[64:67], v[8:11]\n"
      "s_mul_i32 s11, s4, 38\n"
      "s_add_u32 s11, s11, s79\n"
      "s_mov_b32 s77, s11\n"
      "v_mfma_f32_16x16x32_bf16 v[8:11], v[104:107], a[80:83], v[8:11]\n"
      "s_add_u32 s76, s77, 0\n"
      "v_add_f32 v73, 0x3727c5ac, v73\n"
      "s_waitcnt lgkmcnt(5)\n"
      "v_mfma_f32_16x16x32_bf16 v[12:15], v[96:99], a[68:71], v[12:15]\n"
      "v_rsq_f32 v74, v73\n"
      "s_waitcnt lgkmcnt(4)\n"
      "v_mfma_f32_16x16x32_bf16 v[12:15], v[104:107], a[84:87], v[12:15]\n"
      "buffer_load_dwordx4 v[216:219], v80, s[60:63], s76 offen offset:0 sc1\n"
      "s_waitcnt lgkmcnt(3)\n"
      "v_mfma_f32_16x16x32_bf16 v[16:19], v[96:99], a[72:75], v[16:19]\n"
      "ds_read_b128 a[96:99], v91 offset:6144\n"
      "ds_read_b128 a[112:115], v91 offset:7168\n"
      "s_waitcnt lgkmcnt(4)\n"
      "v_mfma_f32_16x16x32_bf16 v[16:19], v[104:107], a[88:91], v[16:19]\n"
      "ds_read_b128 a[100:103], v91 offset:24576\n"
      "v_cvt_pk_bf16_f32 v76, -v72, -v72\n"
      "s_waitcnt lgkmcnt(4)\n"
      "v_mfma_f32_16x16x32_bf16 v[20:23], v[96:99], a[76:79], v[20:23]\n"
      "ds_read_b128 a[116:119], v91 offset:25600\n"
      "v_mul_f32 v74, s78, v74\n"
      "s_waitcnt lgkmcnt(4)\n"
      "v_mfma_f32_16x16x32_bf16 v[20:23], v[104:107], a[92:95], v[20:23]\n"
      "buffer_load_dwordx4 v[96:99], v80, s[60:63], s76 offen offset:1024 sc1\n"
      "v_mfma_f32_16x16x32_bf16 v[48:51], v[40:43], v[100:103], v[48:51]\n"
      "v_mfma_f32_16x16x32_bf16 v[48:51], v[40:43], v[108:111], v[48:51]\n"
      "v_mfma_f32_16x16x32_bf16 v[24:27], v[100:103], a[64:67], v[24:27]\n"
      "s_mul_i32 s12, s34, 77824\n"
      "ds_read_b128 a[104:107], v91 offset:43008\n"
      "v_and_b32 v75, 0xffff0000, v76\n"
      "v_mfma_f32_16x16x32_bf16 v[24:27], v[108:111], a[80:83], v[24:27]\n"
      "ds_read_b128 a[120:123], v91 offset:44032\n"
      "v_add_f32 v75, v72, v75\n"
      "v_mfma_f32_16x16x32_bf16 v[28:31], v[100:103], a[68:71], v[28:31]\n"
      "ds_read_b128 a[108:111], v84 offset:6144\n"
      "v_cvt_pk_bf16_f32 v77, -v75, -v75\n"
      "v_mfma_f32_16x16x32_bf16 v[28:31], v[108:111], a[84:87], v[28:31]\n"
      "buffer_load_dwordx4 v[104:107], v80, s[60:63], s76 offen offset:2048 sc1\n"
      "v_mfma_f32_16x16x32_bf16 v[32:35], v[100:103], a[72:75], v[32:35]\n"
      "ds_read_b128 a[124:127], v84 offset:7168\n"
      "v_mov_b32 v78, v76\n"
      "v_mfma_f32_16x16x32_bf16 v[32:35], v[108:111], a[88:91], v[32:35]\n"
      "v_mov_b32 v79, v77\n"
      "v_lshl_add_u32 v72, s64, 8, v6\n"
      "v_mfma_f32_16x16x32_bf16 v[36:39], v[100:103], a[76:79], v[36:39]\n"
      "s_waitcnt lgkmcnt(6)\n"
      "v_lshl_add_u32 v73, s64, 2, v6\n"
      "v_mfma_f32_16x16x32_bf16 v[36:39], v[108:111], a[92:95], v[36:39]\n"
      "buffer_load_dwordx4 v[100:103], v80, s[60:63], s76 offen offset:3072 sc1\n"
      "v_mfma_f32_16x16x32_bf16 v[8:11], v[112:115], a[96:99], v[8:11]\n"
      "s_add_u32 s76, s77, 4096\n"
      "s_add_u32 s12, s12, s79\n"
      "v_mfma_f32_16x16x32_bf16 v[8:11], v[120:123], a[112:115], v[8:11]\n"
      "s_mov_b64 exec, 0xffff\n"
      "ds_write_b128 v72, v[76:79]\n"
      "ds_write_b32 v73, v74 offset:1024\n"
      "s_mov_b64 exec, -1\n"
      "s_waitcnt lgkmcnt(7)\n"
      "v_mfma_f32_16x16x32_bf16 v[12:15], v[112:115], a[100:103], v[12:15]\n"
      "s_waitcnt lgkmcnt(6)\n"
      "v_mfma_f32_16x16x32_bf16 v[12:15], v[120:123], a[116:119], v[12:15]\n"
      "buffer_load_dwordx4 v[108:111], v80, s[60:63], s76 offen offset:0 sc1\n"
      "s_waitcnt lgkmcnt(0)\n"
      "s_waitcnt vmcnt(11)\n"
      "s_barrier\n"
      "v_mfma_f32_16x16x32_bf16 v[16:19], v[112:115], a[104:107], v[16:19]\n"
      "ds_read_b128 a[128:131], v91 offset:8192\n"
      "ds_read_b128 a[144:147], v91 offset:9216\n"
      "v_mfma_f32_16x16x32_bf16 v[16:19], v[120:123], a[120:123], v[16:19]\n"
      "ds_read_b128 a[132:135], v91 offset:26624\n"
      "v_mfma_f32_16x16x32_bf16 v[20:23], v[112:115], a[108:111], v[20:23]\n"
      "ds_read_b128 a[148:151], v91 offset:27648\n"
      "v_mfma_f32_16x16x32_bf16 v[20:23], v[120:123], a[124:127], v[20:23]\n"
      "buffer_load_dwordx4 v[112:115], v80, s[60:63], s76 offen offset:1024 sc1\n"
      "v_mfma_f32_16x16x32_bf16 v[48:51], v[40:43], v[116:119], v[48:51]\n"
      "v_mfma_f32_16x16x32_bf16 v[48:51], v[40:43], v[124:127], v[48:51]\n"
      "v_mfma_f32_16x16x32_bf16 v[24:27], v[116:119], a[96:99], v[24:27]\n"
      "s_add_u32 s12, s12, 544768\n"
      "ds_read_b128 a[136:139], v91 offset:45056\n"
      "v_mfma_f32_16x16x32_bf16 v[24:27], v[124:127], a[112:115], v[24:27]\n"
      "ds_read_b128 a[152:155], v91 offset:46080\n"
      "v_mfma_f32_16x16x32_bf16 v[28:31], v[116:119], a[100:103], v[28:31]\n"
      "ds_read_b128 a[140:143], v84 offset:8192\n"
      "v_mfma_f32_16x16x32_bf16 v[28:31], v[124:127], a[116:119], v[28:31]\n"
      "buffer_load_dwordx4 v[120:123], v80, s[60:63], s76 offen offset:2048 sc1\n"
      "v_mfma_f32_16x16x32_bf16 v[32:35], v[116:119], a[104:107], v[32:35]\n"
      "ds_read_b128 a[156:159], v84 offset:9216\n"
      "v_mfma_f32_16x16x32_bf16 v[32:35], v[124:127], a[120:123], v[32:35]\n"
      "v_mfma_f32_16x16x32_bf16 v[36:39], v[116:119], a[108:111], v[36:39]\n"
      "s_waitcnt lgkmcnt(6)\n"
      "v_mfma_f32_16x16x32_bf16 v[36:39], v[124:127], a[124:127], v[36:39]\n"
      "buffer_load_dwordx4 v[116:119], v80, s[60:63], s76 offen offset:3072 sc1\n"
      "v_mfma_f32_16x16x32_bf16 v[8:11], v[128:131], a[128:131], v[8:11]\n"
      "s_add_u32 s76, s77, 8192\n"
      "s_mul_i32 s13, s64, 18432\n"
      "v_mfma_f32_16x16x32_bf16 v[8:11], v[136:139], a[144:147], v[8:11]\n"
      "s_waitcnt lgkmcnt(5)\n"
      "v_mfma_f32_16x16x32_bf16 v[12:15], v[128:131], a[132:135], v[12:15]\n"
      "s_waitcnt lgkmcnt(4)\n"
      "v_mfma_f32_16x16x32_bf16 v[12:15], v[136:139], a[148:151], v[12:15]\n"
      "buffer_load_dwordx4 v[124:127], v80, s[60:63], s76 offen offset:0 sc1\n"
      "s_waitcnt lgkmcnt(3)\n"
      "v_mfma_f32_16x16x32_bf16 v[16:19], v[128:131], a[136:139], v[16:19]\n"
      "ds_read_b128 a[160:163], v91 offset:10240\n"
      "ds_read_b128 a[176:179], v91 offset:11264\n"
      "s_waitcnt lgkmcnt(4)\n"
      "v_mfma_f32_16x16x32_bf16 v[16:19], v[136:139], a[152:155], v[16:19]\n"
      "ds_read_b128 a[164:167], v91 offset:28672\n"
      "s_waitcnt lgkmcnt(4)\n"
      "v_mfma_f32_16x16x32_bf16 v[20:23], v[128:131], a[140:143], v[20:23]\n"
      "ds_read_b128 a[180:183], v91 offset:29696\n"
      "s_waitcnt lgkmcnt(4)\n"
      "v_mfma_f32_16x16x32_bf16 v[20:23], v[136:139], a[156:159], v[20:23]\n"
      "buffer_load_dwordx4 v[128:131], v80, s[60:63], s76 offen offset:1024 sc1\n"
      "v_mfma_f32_16x16x32_bf16 v[48:51], v[40:43], v[132:135], v[48:51]\n"
      "v_mfma_f32_16x16x32_bf16 v[48:51], v[40:43], v[140:143], v[48:51]\n"
      "v_mfma_f32_16x16x32_bf16 v[24:27], v[132:135], a[128:131], v[24:27]\n"
      "ds_read_b128 a[168:171], v91 offset:47104\n"
      "v_mfma_f32_16x16x32_bf16 v[24:27], v[140:143], a[144:147], v[24:27]\n"
      "s_add_u32 s13, s13, 90112\n"
      "ds_read_b128 a[184:187], v91 offset:48128\n"
      "v_mfma_f32_16x16x32_bf16 v[28:31], v[132:135], a[132:135], v[28:31]\n"
      "ds_read_b128 a[172:175], v84 offset:10240\n"
      "v_mfma_f32_16x16x32_bf16 v[28:31], v[140:143], a[148:151], v[28:31]\n"
      "buffer_load_dwordx4 v[136:139], v80, s[60:63], s76 offen offset:2048 sc1\n"
      "v_mfma_f32_16x16x32_bf16 v[32:35], v[132:135], a[136:139], v[32:35]\n"
      "ds_read_b128 a[188:191], v84 offset:11264\n"
      "v_mfma_f32_16x16x32_bf16 v[32:35], v[140:143], a[152:155], v[32:35]\n"
      "v_mfma_f32_16x16x32_bf16 v[36:39], v[132:135], a[140:143], v[36:39]\n"
      "s_waitcnt lgkmcnt(6)\n"
      "v_mfma_f32_16x16x32_bf16 v[36:39], v[140:143], a[156:159], v[36:39]\n"
      "buffer_load_dwordx4 v[132:135], v80, s[60:63], s76 offen offset:3072 sc1\n"
      "v_mfma_f32_16x16x32_bf16 v[8:11], v[144:147], a[160:163], v[8:11]\n"
      "s_mul_i32 s11, s5, 38\n"
      "s_add_u32 s76, s77, 12288\n"
      "v_mfma_f32_16x16x32_bf16 v[8:11], v[152:155], a[176:179], v[8:11]\n"
      "v_add_u32 v88, s13, v80\n"
      "s_waitcnt lgkmcnt(5)\n"
      "v_mfma_f32_16x16x32_bf16 v[12:15], v[144:147], a[164:167], v[12:15]\n"
      "s_waitcnt lgkmcnt(4)\n"
      "v_mfma_f32_16x16x32_bf16 v[12:15], v[152:155], a[180:183], v[12:15]\n"
      "buffer_load_dwordx4 v[140:143], v80, s[60:63], s76 offen offset:0 sc1\n"
      "s_waitcnt vmcnt(15)\n"
      "s_barrier\n"
      "s_waitcnt lgkmcnt(3)\n"
      "v_mfma_f32_16x16x32_bf16 v[16:19], v[144:147], a[168:171], v[16:19]\n"
      "s_add_u32 s11, s11, s79\n"
      "ds_read_b128 a[192:195], v91 offset:12288\n"
      "ds_read_b128 a[208:211], v91 offset:13312\n"
      "s_waitcnt lgkmcnt(4)\n"
      "v_mfma_f32_16x16x32_bf16 v[16:19], v[152:155], a[184:187], v[16:19]\n"
      "ds_read_b128 a[196:199], v91 offset:30720\n"
      "s_waitcnt lgkmcnt(4)\n"
      "v_mfma_f32_16x16x32_bf16 v[20:23], v[144:147], a[172:175], v[20:23]\n"
      "ds_read_b128 a[212:215], v91 offset:31744\n"
      "s_waitcnt lgkmcnt(4)\n"
      "v_mfma_f32_16x16x32_bf16 v[20:23], v[152:155], a[188:191], v[20:23]\n"
      "buffer_load_dwordx4 v[144:147], v80, s[60:63], s76 offen offset:1024 sc1\n"
      "v_mfma_f32_16x16x32_bf16 v[48:51], v[40:43], v[148:151], v[48:51]\n"
      "v_mfma_f32_16x16x32_bf16 v[48:51], v[40:43], v[156:159], v[48:51]\n"
      "v_mfma_f32_16x16x32_bf16 v[24:27], v[148:151], a[160:163], v[24:27]\n"
      "ds_read_b128 a[200:203], v91 offset:49152\n"
      "v_mfma_f32_16x16x32_bf16 v[24:27], v[156:159], a[176:179], v[24:27]\n"
      "ds_read_b128 a[216:219], v91 offset:50176\n"
      "v_mfma_f32_16x16x32_bf16 v[28:31], v[148:151], a[164:167], v[28:31]\n"
      "ds_read_b128 a[204:207], v84 offset:12288\n"
      "v_mfma_f32_16x16x32_bf16 v[28:31], v[156:159], a[180:183], v[28:31]\n"
      "buffer_load_dwordx4 v[152:155], v80, s[60:63], s76 offen offset:2048 sc1\n"
      "v_mfma_f32_16x16x32_bf16 v[32:35], v[148:151], a[168:171], v[32:35]\n"
      "ds_read_b128 a[220:223], v84 offset:13312\n"
      "v_mfma_f32_16x16x32_bf16 v[32:35], v[156:159], a[184:187], v[32:35]\n"
      "v_mfma_f32_16x16x32_bf16 v[36:39], v[148:151], a[172:175], v[36:39]\n"
      "s_waitcnt lgkmcnt(6)\n"
      "v_mfma_f32_16x16x32_bf16 v[36:39], v[156:159], a[188:191], v[36:39]\n"
      "buffer_load_dwordx4 v[148:151], v80, s[60:63], s76 offen offset:3072 sc1\n"
      "v_mfma_f32_16x16x32_bf16 v[8:11], v[160:163], a[192:195], v[8:11]\n"
      "s_add_u32 s76, s77, 16384\n"
      "v_mfma_f32_16x16x32_bf16 v[8:11], v[168:171], a[208:211], v[8:11]\n"
      "s_waitcnt lgkmcnt(5)\n"
      "v_mfma_f32_16x16x32_bf16 v[12:15], v[160:163], a[196:199], v[12:15]\n"
      "s_waitcnt lgkmcnt(4)\n"
      "v_mfma_f32_16x16x32_bf16 v[12:15], v[168:171], a[212:215], v[12:15]\n"
      "buffer_load_dwordx4 v[156:159], v80, s[60:63], s76 offen offset:0 sc1\n"
      "s_waitcnt lgkmcnt(3)\n"
      "v_mfma_f32_16x16x32_bf16 v[16:19], v[160:163], a[200:203], v[16:19]\n"
      "ds_read_b128 a[224:227], v91 offset:14336\n"
      "ds_read_b128 a[240:243], v91 offset:15360\n"
      "s_waitcnt lgkmcnt(4)\n"
      "v_mfma_f32_16x16x32_bf16 v[16:19], v[168:171], a[216:219], v[16:19]\n"
      "ds_read_b128 a[228:231], v91 offset:32768\n"
      "s_waitcnt lgkmcnt(4)\n"
      "v_mfma_f32_16x16x32_bf16 v[20:23], v[160:163], a[204:207], v[20:23]\n"
      "ds_read_b128 a[244:247], v91 offset:33792\n"
      "s_waitcnt lgkmcnt(4)\n"
      "v_mfma_f32_16x16x32_bf16 v[20:23], v[168:171], a[220:223], v[20:23]\n"
      "buffer_load_dwordx4 v[160:163], v80, s[60:63], s76 offen offset:1024 sc1\n"
      "v_mfma_f32_16x16x32_bf16 v[48:51], v[40:43], v[164:167], v[48:51]\n"
      "v_mfma_f32_16x16x32_bf16 v[48:51], v[40:43], v[172:175], v[48:51]\n"
      "v_mfma_f32_16x16x32_bf16 v[24:27], v[164:167], a[192:195], v[24:27]\n"
      "ds_read_b128 a[232:235], v91 offset:51200\n"
      "v_mfma_f32_16x16x32_bf16 v[24:27], v[172:175], a[208:211], v[24:27]\n"
      "ds_read_b128 a[248:251], v91 offset:52224\n"
      "v_mfma_f32_16x16x32_bf16 v[28:31], v[164:167], a[196:199], v[28:31]\n"
      "ds_read_b128 a[236:239], v84 offset:14336\n"
      "v_mfma_f32_16x16x32_bf16 v[28:31], v[172:175], a[212:215], v[28:31]\n"
      "buffer_load_dwordx4 v[168:171], v80, s[60:63], s76 offen offset:2048 sc1\n"
      "v_mfma_f32_16x16x32_bf16 v[32:35], v[164:167], a[200:203], v[32:35]\n"
      "s_mov_b32 s77, s11\n"
      "s_add_u32 s76, s77, 0\n"
      "ds_read_b128 a[252:255], v84 offset:15360\n"
      "v_mfma_f32_16x16x32_bf16 v[32:35], v[172:175], a[216:219], v[32:35]\n"
      "v_mfma_f32_16x16x32_bf16 v[36:39], v[164:167], a[204:207], v[36:39]\n"
      "s_waitcnt lgkmcnt(6)\n"
      "v_mfma_f32_16x16x32_bf16 v[36:39], v[172:175], a[220:223], v[36:39]\n"
      "buffer_load_dwordx4 v[164:167], v80, s[60:63], s76 offen offset:0 sc1\n"
      "v_mfma_f32_16x16x32_bf16 v[8:11], v[176:179], a[224:227], v[8:11]\n"
      "v_mfma_f32_16x16x32_bf16 v[8:11], v[184:187], a[240:243], v[8:11]\n"
      "s_waitcnt lgkmcnt(5)\n"
      "v_mfma_f32_16x16x32_bf16 v[12:15], v[176:179], a[228:231], v[12:15]\n"
      "s_waitcnt lgkmcnt(4)\n"
      "v_mfma_f32_16x16x32_bf16 v[12:15], v[184:187], a[244:247], v[12:15]\n"
      "buffer_load_dwordx4 v[172:175], v80, s[60:63], s76 offen offset:1024 sc1\n"
      "s_waitcnt vmcnt(21)\n"
      "s_barrier\n"
      "s_waitcnt lgkmcnt(3)\n"
      "v_mfma_f32_16x16x32_bf16 v[16:19], v[176:179], a[232:235], v[16:19]\n"
      "ds_read_b128 v[220:223], v91 offset:16384\n"
      "ds_read_b128 v[236:239], v91 offset:17408\n"
      "s_waitcnt lgkmcnt(4)\n"
      "v_mfma_f32_16x16x32_bf16 v[16:19], v[184:187], a[248:251], v[16:19]\n"
      "ds_read_b128 v[224:227], v91 offset:34816\n"
      "s_waitcnt lgkmcnt(4)\n"
      "v_mfma_f32_16x16x32_bf16 v[20:23], v[176:179], a[236:239], v[20:23]\n"
      "ds_read_b128 v[240:243], v91 offset:35840\n"
      "s_waitcnt lgkmcnt(4)\n"
      "v_mfma_f32_16x16x32_bf16 v[20:23], v[184:187], a[252:255], v[20:23]\n"
      "buffer_load_dwordx4 v[176:179], v80, s[60:63], s76 offen offset:2048 sc1\n"
      "v_mfma_f32_16x16x32_bf16 v[48:51], v[40:43], v[180:183], v[48:51]\n"
      "v_mfma_f32_16x16x32_bf16 v[48:51], v[40:43], v[188:191], v[48:51]\n"
      "v_mfma_f32_16x16x32_bf16 v[24:27], v[180:183], a[224:227], v[24:27]\n"
      "ds_read_b128 v[228:231], v91 offset:53248\n"
      "v_mfma_f32_16x16x32_bf16 v[24:27], v[188:191], a[240:243], v[24:27]\n"
      "ds_read_b128 v[244:247], v91 offset:54272\n"
      "v_mfma_f32_16x16x32_bf16 v[28:31], v[180:183], a[228:231], v[28:31]\n"
      "ds_read_b128 v[232:235], v84 offset:16384\n"
      "v_mfma_f32_16x16x32_bf16 v[28:31], v[188:191], a[244:247], v[28:31]\n"
      "buffer_load_dwordx4 v[184:187], v80, s[60:63], s76 offen offset:3072 sc1\n"
      "v_mfma_f32_16x16x32_bf16 v[32:35], v[180:183], a[232:235], v[32:35]\n"
      "s_add_u32 s76, s77, 4096\n"
      "ds_read_b128 v[248:251], v84 offset:17408\n"
      "v_mfma_f32_16x16x32_bf16 v[32:35], v[188:191], a[248:251], v[32:35]\n"
      "v_mfma_f32_16x16x32_bf16 v[36:39], v[180:183], a[236:239], v[36:39]\n"
      "s_waitcnt lgkmcnt(6)\n"
      "v_mfma_f32_16x16x32_bf16 v[36:39], v[188:191], a[252:255], v[36:39]\n"
      "buffer_load_dwordx4 v[180:183], v80, s[60:63], s76 offen offset:0 sc1\n"
      "v_mfma_f32_16x16x32_bf16 v[8:11], v[192:195], v[220:223], v[8:11]\n"
      "ds_read_b128 v[56:59], v6 offset:0\n"
      "v_mfma_f32_16x16x32_bf16 v[8:11], v[200:203], v[236:239], v[8:11]\n"
      "ds_read_b128 v[60:63], v6 offset:256\n"
      "s_waitcnt lgkmcnt(7)\n"
      "v_mfma_f32_16x16x32_bf16 v[12:15], v[192:195], v[224:227], v[12:15]\n"
      "s_waitcnt lgkmcnt(6)\n"
      "v_mfma_f32_16x16x32_bf16 v[12:15], v[200:203], v[240:243], v[12:15]\n"
      "buffer_load_dwordx4 v[188:191], v80, s[60:63], s76 offen offset:1024 sc1\n"
      "s_waitcnt lgkmcnt(5)\n"
      "v_mfma_f32_16x16x32_bf16 v[16:19], v[192:195], v[228:231], v[16:19]\n"
      "ds_read_b128 v[64:67], v6 offset:512\n"
      "s_waitcnt lgkmcnt(5)\n"
      "v_mfma_f32_16x16x32_bf16 v[16:19], v[200:203], v[244:247], v[16:19]\n"
      "ds_read_b128 v[68:71], v6 offset:768\n"
      "s_waitcnt lgkmcnt(5)\n"
      "v_mfma_f32_16x16x32_bf16 v[20:23], v[192:195], v[232:235], v[20:23]\n"
      "s_waitcnt lgkmcnt(4)\n"
      "v_mfma_f32_16x16x32_bf16 v[20:23], v[200:203], v[248:251], v[20:23]\n"
      "buffer_load_dwordx4 v[192:195], v80, s[60:63], s76 offen offset:2048 sc1\n"
      "v_mfma_f32_16x16x32_bf16 v[48:51], v[40:43], v[196:199], v[48:51]\n"
      "v_mfma_f32_16x16x32_bf16 v[48:51], v[40:43], v[204:207], v[48:51]\n"
      "v_mfma_f32_16x16x32_bf16 v[24:27], v[196:199], v[220:223], v[24:27]\n"
      "ds_read_b128 v[92:95], v6 offset:1024\n"
      "v_mfma_f32_16x16x32_bf16 v[24:27], v[204:207], v[236:239], v[24:27]\n"
      "v_mfma_f32_16x16x32_bf16 v[28:31], v[196:199], v[224:227], v[28:31]\n"
      "v_mfma_f32_16x16x32_bf16 v[28:31], v[204:207], v[240:243], v[28:31]\n"
      "buffer_load_dwordx4 v[200:203], v80, s[60:63], s76 offen offset:3072 sc1\n"
      "v_mfma_f32_16x16x32_bf16 v[32:35], v[196:199], v[228:231], v[32:35]\n"
      "s_add_u32 s76, s77, 8192\n"
      "v_mfma_f32_16x16x32_bf16 v[32:35], v[204:207], v[244:247], v[32:35]\n"
      "v_mfma_f32_16x16x32_bf16 v[36:39], v[196:199], v[232:235], v[36:39]\n"
      "s_waitcnt lgkmcnt(4)\n"
      "v_mfma_f32_16x16x32_bf16 v[36:39], v[204:207], v[248:251], v[36:39]\n"
      "buffer_load_dwordx4 v[196:199], v80, s[60:63], s76 offen offset:0 sc1\n"
      "v_mov_b32 v214, 0\n"
      "v_mov_b32 v215, 0\n"
      "v_mfma_f32_16x16x32_bf16 v[8:11], v[208:211], v[56:59], v[8:11]\n"
      "v_cvt_pk_bf16_f32 v212, v48, v48\n"
      "v_and_b32 v49, 0xffff0000, v212\n"
      "s_waitcnt lgkmcnt(3)\n"
      "v_mfma_f32_16x16x32_bf16 v[12:15], v[208:211], v[60:63], v[12:15]\n"
      "v_sub_f32 v49, v48, v49\n"
      "v_cvt_pk_bf16_f32 v212, v48, v49\n"
      "s_waitcnt lgkmcnt(2)\n"
      "v_mfma_f32_16x16x32_bf16 v[16:19], v[208:211], v[64:67], v[16:19]\n"
      "v_mov_b32 v213, v212\n"
      "s_waitcnt lgkmcnt(1)\n"
      "v_mfma_f32_16x16x32_bf16 v[20:23], v[208:211], v[68:71], v[20:23]\n"
      "buffer_load_dwordx4 v[204:207], v80, s[60:63], s76 offen offset:1024 sc1\n"
      "v_mfma_f32_16x16x32_bf16 v[24:27], v[212:215], v[56:59], v[24:27]\n"
      "v_mfma_f32_16x16x32_bf16 v[28:31], v[212:215], v[60:63], v[28:31]\n"
      "v_mfma_f32_16x16x32_bf16 v[32:35], v[212:215], v[64:67], v[32:35]\n"
      "s_waitcnt vmcnt(27)\n"
      "v_mfma_f32_16x16x32_bf16 v[36:39], v[212:215], v[68:71], v[36:39]\n"
      "buffer_load_dwordx4 v[208:211], v80, s[60:63], s76 offen offset:2048 sc1\n"
      "v_mfma_f32_16x16x32_bf16 v[40:43], v[216:219], a[0:3], 0\n"
      "v_mfma_f32_16x16x32_bf16 v[40:43], v[96:99], a[16:19], v[40:43]\n"
      "s_barrier\n"
      "v_mfma_f32_16x16x32_bf16 v[44:47], v[216:219], a[4:7], 0\n"
      "v_mfma_f32_16x16x32_bf16 v[44:47], v[96:99], a[20:23], v[44:47]\n"
      "buffer_load_dwordx4 v[212:215], v80, s[60:63], s76 offen offset:3072 sc1\n"
      "s_waitcnt lgkmcnt(0)\n"
      "v_mul_f32 v8, v8, v92\n"
      "v_mul_f32 v9, v9, v92\n"
      "v_mul_f32 v10, v10, v92\n"
      "v_mfma_f32_16x16x32_bf16 v[48:51], v[216:219], a[8:11], 0\n"
      "s_mul_i32 s11, s6, 38\n"
      "s_add_u32 s76, s77, 12288\n"
      "s_add_u32 m0, s13, 0\n"
      "v_mul_f32 v11, v11, v92\n"
      "v_exp_f32 v72, v8\n"
      "v_mfma_f32_16x16x32_bf16 v[48:51], v[96:99], a[24:27], v[48:51]\n"
      "s_add_u32 s14, s12, 0\n"
      "v_exp_f32 v73, v9\n"
      "v_exp_f32 v74, v10\n"
      "v_mfma_f32_16x16x32_bf16 v[52:55], v[216:219], a[12:15], 0\n"
      "buffer_load_dwordx4 v80, s[60:63], s14 offen sc1 lds\n"
      "v_exp_f32 v75, v11\n"
      "v_add_f32 v72, 1.0, v72\n"
      "v_mfma_f32_16x16x32_bf16 v[52:55], v[96:99], a[28:31], v[52:55]\n"
      "buffer_load_dwordx4 v[216:219], v80, s[60:63], s76 offen offset:0 sc1\n"
      "v_add_f32 v73, 1.0, v73\n"
      "v_add_f32 v74, 1.0, v74\n"
      "v_add_f32 v75, 1.0, v75\n"
      "s_waitcnt vmcnt(30)\n"
      "v_mfma_f32_16x16x32_bf16 v[52:55], v[104:107], a[44:47], v[52:55]\n"
      "s_add_u32 s11, s11, s79\n"
      "v_rcp_f32 v72, v72\n"
      "v_rcp_f32 v73, v73\n"
      "s_waitcnt vmcnt(29)\n"
      "v_mfma_f32_16x16x32_bf16 v[52:55], v[100:103], a[60:63], v[52:55]\n"
      "s_add_u32 m0, s13, 1024\n"
      "v_rcp_f32 v74, v74\n"
      "v_rcp_f32 v75, v75\n"
      "v_mul_f32 v72, v72, v8\n"
      "v_mfma_f32_16x16x32_bf16 v[48:51], v[104:107], a[40:43], v[48:51]\n"
      "v_mul_f32 v73, v73, v9\n"
      "v_mul_f32 v74, v74, v10\n"
      "v_mul_f32 v75, v75, v11\n"
      "v_cvt_pk_bf16_f32 v76, v72, v73\n"
      "v_cvt_pk_bf16_f32 v77, v74, v75\n"
      "v_mfma_f32_16x16x32_bf16 v[48:51], v[100:103], a[56:59], v[48:51]\n"
      "buffer_load_dwordx4 v[96:99], v80, s[60:63], s76 offen offset:1024 sc1\n"
      "ds_write_b64 v86, v[76:77] offset:0\n"
      "v_mfma_f32_16x16x32_bf16 v[44:47], v[104:107], a[36:39], v[44:47]\n"
      "s_add_u32 s14, s12, 1024\n"
      "v_mul_f32 v12, v12, v93\n"
      "v_mul_f32 v13, v13, v93\n"
      "v_mul_f32 v14, v14, v93\n"
      "v_mul_f32 v15, v15, v93\n"
      "v_mfma_f32_16x16x32_bf16 v[44:47], v[100:103], a[52:55], v[44:47]\n"
      "buffer_load_dwordx4 v80, s[60:63], s14 offen sc1 lds\n"
      "v_exp_f32 v72, v12\n"
      "v_mfma_f32_16x16x32_bf16 v[40:43], v[104:107], a[32:35], v[40:43]\n"
      "s_waitcnt vmcnt(29)\n"
      "v_exp_f32 v73, v13\n"
      "v_exp_f32 v74, v14\n"
      "v_mfma_f32_16x16x32_bf16 v[40:43], v[100:103], a[48:51], v[40:43]\n"
      "buffer_load_dwordx4 v[104:107], v80, s[60:63], s76 offen offset:2048 sc1\n"
      "v_exp_f32 v75, v15\n"
      "v_add_f32 v72, 1.0, v72\n"
      "v_mfma_f32_16x16x32_bf16 v[40:43], v[108:111], a[64:67], v[40:43]\n"
      "s_add_u32 m0, s13, 2048\n"
      "v_add_f32 v73, 1.0, v73\n"
      "v_add_f32 v74, 1.0, v74\n"
      "v_add_f32 v75, 1.0, v75\n"
      "v_rcp_f32 v72, v72\n"
      "v_mfma_f32_16x16x32_bf16 v[40:43], v[112:115], a[80:83], v[40:43]\n"
      "v_rcp_f32 v73, v73\n"
      "v_rcp_f32 v74, v74\n"
      "v_mfma_f32_16x16x32_bf16 v[44:47], v[108:111], a[68:71], v[44:47]\n"
      "s_add_u32 s14, s12, 2048\n"
      "v_rcp_f32 v75, v75\n"
      "v_mul_f32 v72, v72, v12\n"
      "v_mul_f32 v73, v73, v13\n"
      "v_mul_f32 v74, v74, v14\n"
      "v_mfma_f32_16x16x32_bf16 v[44:47], v[112:115], a[84:87], v[44:47]\n"
      "buffer_load_dwordx4 v[100:103], v80, s[60:63], s76 offen offset:3072 sc1\n"
      "v_mul_f32 v75, v75, v15\n"
      "v_cvt_pk_bf16_f32 v78, v72, v73\n"
      "v_cvt_pk_bf16_f32 v79, v74, v75\n"
      "v_mfma_f32_16x16x32_bf16 v[48:51], v[108:111], a[72:75], v[48:51]\n"
      "s_add_u32 s76, s77, 16384\n"
      "ds_write_b64 v86, v[78:79] offset:1024\n"
      "v_mul_f32 v16, v16, v94\n"
      "v_mul_f32 v17, v17, v94\n"
      "v_mfma_f32_16x16x32_bf16 v[48:51], v[112:115], a[88:91], v[48:51]\n"
      "buffer_load_dwordx4 v80, s[60:63], s14 offen sc1 lds\n"
      "v_mul_f32 v18, v18, v94\n"
      "v_mul_f32 v19, v19, v94\n"
      "v_mfma_f32_16x16x32_bf16 v[52:55], v[108:111], a[76:79], v[52:55]\n"
      "s_waitcnt vmcnt(30)\n"
      "v_exp_f32 v72, v16\n"
      "v_exp_f32 v73, v17\n"
      "v_mfma_f32_16x16x32_bf16 v[52:55], v[112:115], a[92:95], v[52:55]\n"
      "buffer_load_dwordx4 v[108:111], v80, s[60:63], s76 offen offset:0 sc1\n"
      "v_exp_f32 v74, v18\n"
      "v_mfma_f32_16x16x32_bf16 v[52:55], v[120:123], a[108:111], v[52:55]\n"
      "s_add_u32 m0, s13, 3072\n"
      "s_add_u32 s14, s12, 3072\n"
      "v_exp_f32 v75, v19\n"
      "v_add_f32 v72, 1.0, v72\n"
      "v_add_f32 v73, 1.0, v73\n"
      "v_mfma_f32_16x16x32_bf16 v[52:55], v[116:119], a[124:127], v[52:55]\n"
      "v_add_f32 v74, 1.0, v74\n"
      "v_add_f32 v75, 1.0, v75\n"
      "v_rcp_f32 v72, v72\n"
      "v_mfma_f32_16x16x32_bf16 v[48:51], v[120:123], a[104:107], v[48:51]\n"
      "buffer_load_dwordx4 v80, s[60:63], s14 offen sc1 lds\n"
      "v_rcp_f32 v73, v73\n"
      "v_mfma_f32_16x16x32_bf16 v[48:51], v[116:119], a[120:123], v[48:51]\n"
      "buffer_load_dwordx4 v[112:115], v80, s[60:63], s76 offen offset:1024 sc1\n"
      "v_rcp_f32 v74, v74\n"
      "v_mfma_f32_16x16x32_bf16 v[44:47], v[120:123], a[100:103], v[44:47]\n"
      "v_rcp_f32 v75, v75\n"
      "v_mul_f32 v72, v72, v16\n"
      "v_mul_f32 v73, v73, v17\n"
      "v_mul_f32 v74, v74, v18\n"
      "v_mfma_f32_16x16x32_bf16 v[44:47], v[116:119], a[116:119], v[44:47]\n"
      "s_waitcnt vmcnt(31)\n"
      "v_mul_f32 v75, v75, v19\n"
      "v_cvt_pk_bf16_f32 v76, v72, v73\n"
      "v_cvt_pk_bf16_f32 v77, v74, v75\n"
      "v_mfma_f32_16x16x32_bf16 v[40:43], v[120:123], a[96:99], v[40:43]\n"
      "s_add_u32 m0, s13, 4096\n"
      "ds_write_b64 v86, v[76:77] offset:2048\n"
      "v_mul_f32 v20, v20, v95\n"
      "v_mul_f32 v21, v21, v95\n"
      "v_mfma_f32_16x16x32_bf16 v[40:43], v[116:119], a[112:115], v[40:43]\n"
      "buffer_load_dwordx4 v[120:123], v80, s[60:63], s76 offen offset:2048 sc1\n"
      "v_mul_f32 v22, v22, v95\n"
      "v_mul_f32 v23, v23, v95\n"
      "v_mfma_f32_16x16x32_bf16 v[40:43], v[124:127], a[128:131], v[40:43]\n"
      "s_mov_b32 s77, s11\n"
      "s_add_u32 s76, s77, 0\n"
      "s_add_u32 s14, s12, 4096\n"
      "v_exp_f32 v72, v20\n"
      "v_mfma_f32_16x16x32_bf16 v[40:43], v[128:131], a[144:147], v[40:43]\n"
      "buffer_load_dwordx4 v80, s[60:63], s14 offen sc1 lds\n"
      "v_exp_f32 v73, v21\n"
      "v_mfma_f32_16x16x32_bf16 v[44:47], v[124:127], a[132:135], v[44:47]\n"
      "v_exp_f32 v74, v22\n"
      "v_exp_f32 v75, v23\n"
      "v_add_f32 v72, 1.0, v72\n"
      "v_mfma_f32_16x16x32_bf16 v[44:47], v[128:131], a[148:151], v[44:47]\n"
      "buffer_load_dwordx4 v[116:119], v80, s[60:63], s76 offen offset:0 sc1\n"
      "v_add_f32 v73, 1.0, v73\n"
      "v_add_f32 v74, 1.0, v74\n"
      "v_add_f32 v75, 1.0, v75\n"
      "v_mfma_f32_16x16x32_bf16 v[48:51], v[124:127], a[136:139], v[48:51]\n"
      "s_waitcnt vmcnt(32)\n"
      "v_rcp_f32 v72, v72\n"
      "v_rcp_f32 v73, v73\n"
      "v_mfma_f32_16x16x32_bf16 v[48:51], v[128:131], a[152:155], v[48:51]\n"
      "s_add_u32 m0, s13, 5120\n"
      "v_rcp_f32 v74, v74\n"
      "v_rcp_f32 v75, v75\n"
      "v_mul_f32 v72, v72, v20\n"
      "v_mfma_f32_16x16x32_bf16 v[52:55], v[124:127], a[140:143], v[52:55]\n"
      "s_add_u32 s14, s12, 5120\n"
      "v_mul_f32 v73, v73, v21\n"
      "v_mul_f32 v74, v74, v22\n"
      "v_mul_f32 v75, v75, v23\n"
      "v_cvt_pk_bf16_f32 v78, v72, v73\n"
      "v_cvt_pk_bf16_f32 v79, v74, v75\n"
      "v_mfma_f32_16x16x32_bf16 v[52:55], v[128:131], a[156:159], v[52:55]\n"
      "buffer_load_dwordx4 v[124:127], v80, s[60:63], s76 offen offset:1024 sc1\n"
      "ds_write_b64 v86, v[78:79] offset:3072\n"
      "v_mfma_f32_16x16x32_bf16 v[52:55], v[136:139], a[172:175], v[52:55]\n"
      "buffer_load_dwordx4 v80, s[60:63], s14 offen sc1 lds\n"
      "v_mul_f32 v24, v24, v92\n"
      "v_mul_f32 v25, v25, v92\n"
      "v_mul_f32 v26, v26, v92\n"
      "v_mfma_f32_16x16x32_bf16 v[52:55], v[132:135], a[188:191], v[52:55]\n"
      "v_mul_f32 v27, v27, v92\n"
      "v_exp_f32 v72, v24\n"
      "v_exp_f32 v73, v25\n"
      "v_mfma_f32_16x16x32_bf16 v[48:51], v[136:139], a[168:171], v[48:51]\n"
      "v_exp_f32 v74, v26\n"
      "v_exp_f32 v75, v27\n"
      "v_add_f32 v72, 1.0, v72\n"
      "v_mfma_f32_16x16x32_bf16 v[48:51], v[132:135], a[184:187], v[48:51]\n"
      "buffer_load_dwordx4 v[128:131], v80, s[60:63], s76 offen offset:2048 sc1\n"
      "v_add_f32 v73, 1.0, v73\n"
      "v_add_f32 v74, 1.0, v74\n"
      "v_add_f32 v75, 1.0, v75\n"
      "v_mfma_f32_16x16x32_bf16 v[44:47], v[136:139], a[164:167], v[44:47]\n"
      "s_add_u32 m0, s13, 6144\n"
      "v_rcp_f32 v72, v72\n"
      "v_rcp_f32 v73, v73\n"
      "v_mfma_f32_16x16x32_bf16 v[44:47], v[132:135], a[180:183], v[44:47]\n"
      "s_add_u32 s14, s12, 6144\n"
      "v_rcp_f32 v74, v74\n"
      "v_rcp_f32 v75, v75\n"
      "v_mul_f32 v72, v72, v24\n"
      "v_mfma_f32_16x16x32_bf16 v[40:43], v[136:139], a[160:163], v[40:43]\n"
      "buffer_load_dwordx4 v80, s[60:63], s14 offen sc1 lds\n"
      "v_mul_f32 v73, v73, v25\n"
      "v_mul_f32 v74, v74, v26\n"
      "v_mul_f32 v75, v75, v27\n"
      "v_mfma_f32_16x16x32_bf16 v[40:43], v[132:135], a[176:179], v[40:43]\n"
      "buffer_load_dwordx4 v[136:139], v80, s[60:63], s76 offen offset:3072 sc1\n"
      "v_cvt_pk_bf16_f32 v76, v72, v73\n"
      "v_cvt_pk_bf16_f32 v77, v74, v75\n"
      "s_waitcnt vmcnt(36)\n"
      "v_mfma_f32_16x16x32_bf16 v[40:43], v[140:143], a[192:195], v[40:43]\n"
      "s_add_u32 s76, s77, 4096\n"
      "ds_write_b64 v86, v[76:77] offset:8192\n"
      "v_mul_f32 v28, v28, v93\n"
      "v_mul_f32 v29, v29, v93\n"
      "s_waitcnt vmcnt(35)\n"
      "v_mfma_f32_16x16x32_bf16 v[40:43], v[144:147], a[208:211], v[40:43]\n"
      "v_mul_f32 v30, v30, v93\n"
      "v_mul_f32 v31, v31, v93\n"
      "v_exp_f32 v72, v28\n"
      "v_mfma_f32_16x16x32_bf16 v[44:47], v[140:143], a[196:199], v[44:47]\n"
      "v_exp_f32 v73, v29\n"
      "v_exp_f32 v74, v30\n"
      "v_mfma_f32_16x16x32_bf16 v[44:47], v[144:147], a[212:215], v[44:47]\n"
      "buffer_load_dwordx4 v[132:135], v80, s[60:63], s76 offen offset:0 sc1\n"
      "v_exp_f32 v75, v31\n"
      "v_add_f32 v72, 1.0, v72\n"
      "v_mfma_f32_16x16x32_bf16 v[48:51], v[140:143], a[200:203], v[48:51]\n"
      "s_add_u32 m0, s13, 7168\n"
      "s_add_u32 s14, s12, 7168\n"
      "v_add_f32 v73, 1.0, v73\n"
      "v_add_f32 v74, 1.0, v74\n"
      "v_add_f32 v75, 1.0, v75\n"
      "v_mfma_f32_16x16x32_bf16 v[48:51], v[144:147], a[216:219], v[48:51]\n"
      "buffer_load_dwordx4 v80, s[60:63], s14 offen sc1 lds\n"
      "v_rcp_f32 v72, v72\n"
      "v_mfma_f32_16x16x32_bf16 v[52:55], v[140:143], a[204:207], v[52:55]\n"
      "s_waitcnt vmcnt(35)\n"
      "v_rcp_f32 v73, v73\n"
      "v_rcp_f32 v74, v74\n"
      "v_mfma_f32_16x16x32_bf16 v[52:55], v[144:147], a[220:223], v[52:55]\n"
      "buffer_load_dwordx4 v[140:143], v80, s[60:63], s76 offen offset:1024 sc1\n"
      "v_rcp_f32 v75, v75\n"
      "v_mul_f32 v72, v72, v28\n"
      "v_mfma_f32_16x16x32_bf16 v[52:55], v[152:155], a[236:239], v[52:55]\n"
      "v_mul_f32 v73, v73, v29\n"
      "v_mul_f32 v74, v74, v30\n"
      "v_mul_f32 v75, v75, v31\n"
      "v_cvt_pk_bf16_f32 v78, v72, v73\n"
      "v_cvt_pk_bf16_f32 v79, v74, v75\n"
      "v_mfma_f32_16x16x32_bf16 v[52:55], v[148:151], a[252:255], v[52:55]\n"
      "s_add_u32 m0, s13, 8192\n"
      "ds_write_b64 v86, v[78:79] offset:9216\n"
      "v_mul_f32 v32, v32, v94\n"
      "v_mul_f32 v33, v33, v94\n"
      "v_mfma_f32_16x16x32_bf16 v[48:51], v[152:155], a[232:235], v[48:51]\n"
      "v_mul_f32 v34, v34, v94\n"
      "v_mul_f32 v35, v35, v94\n"
      "v_exp_f32 v72, v32\n"
      "v_mfma_f32_16x16x32_bf16 v[48:51], v[148:151], a[248:251], v[48:51]\n"
      "buffer_load_dwordx4 v[144:147], v80, s[60:63], s76 offen offset:2048 sc1\n"
      "v_exp_f32 v73, v33\n"
      "v_mfma_f32_16x16x32_bf16 v[44:47], v[152:155], a[228:231], v[44:47]\n"
      "s_add_u32 s14, s12, 8192\n"
      "v_exp_f32 v74, v34\n"
      "v_exp_f32 v75, v35\n"
      "v_add_f32 v72, 1.0, v72\n"
      "v_mfma_f32_16x16x32_bf16 v[44:47], v[148:151], a[244:247], v[44:47]\n"
      "buffer_load_dwordx4 v80, s[60:63], s14 offen sc1 lds\n"
      "v_add_f32 v73, 1.0, v73\n"
      "v_add_f32 v74, 1.0, v74\n"
      "v_add_f32 v75, 1.0, v75\n"
      "v_mfma_f32_16x16x32_bf16 v[40:43], v[152:155], a[224:227], v[40:43]\n"
      "s_waitcnt vmcnt(36)\n"
      "v_rcp_f32 v72, v72\n"
      "v_rcp_f32 v73, v73\n"
      "v_mfma_f32_16x16x32_bf16 v[40:43], v[148:151], a[240:243], v[40:43]\n"
      "buffer_load_dwordx4 v[152:155], v80, s[60:63], s76 offen offset:3072 sc1\n"
      "v_rcp_f32 v74, v74\n"
      "v_mfma_f32_16x16x32_bf16 v[40:43], v[156:159], v[220:223], v[40:43]\n"
      "s_add_u32 s76, s77, 8192\n"
      "s_add_u32 m0, s13, 9216\n"
      "v_rcp_f32 v75, v75\n"
      "v_mul_f32 v72, v72, v32\n"
      "v_mul_f32 v73, v73, v33\n"
      "v_mfma_f32_16x16x32_bf16 v[40:43], v[160:163], v[236:239], v[40:43]\n"
      "v_mul_f32 v74, v74, v34\n"
      "v_mul_f32 v75, v75, v35\n"
      "v_cvt_pk_bf16_f32 v76, v72, v73\n"
      "v_cvt_pk_bf16_f32 v77, v74, v75\n"
      "v_mfma_f32_16x16x32_bf16 v[44:47], v[156:159], v[224:227], v[44:47]\n"
      "s_add_u32 s14, s12, 9216\n"
      "ds_write_b64 v86, v[76:77] offset:10240\n"
      "v_mul_f32 v36, v36, v95\n"
      "v_mul_f32 v37, v37, v95\n"
      "v_mfma_f32_16x16x32_bf16 v[44:47], v[160:163], v[240:243], v[44:47]\n"
      "buffer_load_dwordx4 v[148:151], v80, s[60:63], s76 offen offset:0 sc1\n"
      "v_mul_f32 v38, v38, v95\n"
      "v_mul_f32 v39, v39, v95\n"
      "v_mfma_f32_16x16x32_bf16 v[48:51], v[156:159], v[228:231], v[48:51]\n"
      "buffer_load_dwordx4 v80, s[60:63], s14 offen sc1 lds\n"
      "v_exp_f32 v72, v36\n"
      "v_mfma_f32_16x16x32_bf16 v[48:51], v[160:163], v[244:247], v[48:51]\n"
      "v_exp_f32 v73, v37\n"
      "v_exp_f32 v74, v38\n"
      "v_mfma_f32_16x16x32_bf16 v[52:55], v[156:159], v[232:235], v[52:55]\n"
      "s_waitcnt vmcnt(38)\n"
      "v_exp_f32 v75, v39\n"
      "v_add_f32 v72, 1.0, v72\n"
      "v_add_f32 v73, 1.0, v73\n"
      "v_add_f32 v74, 1.0, v74\n"
      "v_mfma_f32_16x16x32_bf16 v[52:55], v[160:163], v[248:251], v[52:55]\n"
      "buffer_load_dwordx4 v[156:159], v80, s[60:63], s76 offen offset:1024 sc1\n"
      "v_add_f32 v75, 1.0, v75\n"
      "v_rcp_f32 v72, v72\n"
      "v_mfma_f32_16x16x32_bf16 v[52:55], v[168:171], v[68:71], v[52:55]\n"
      "v_rcp_f32 v73, v73\n"
      "v_rcp_f32 v74, v74\n"
      "v_mfma_f32_16x16x32_bf16 v[48:51], v[168:171], v[64:67], v[48:51]\n"
      "v_rcp_f32 v75, v75\n"
      "v_mul_f32 v72, v72, v36\n"
      "v_mul_f32 v73, v73, v37\n"
      "v_mul_f32 v74, v74, v38\n"
      "v_mfma_f32_16x16x32_bf16 v[44:47], v[168:171], v[60:63], v[44:47]\n"
      "s_waitcnt vmcnt(37)\n"
      "v_mul_f32 v75, v75, v39\n"
      "v_cvt_pk_bf16_f32 v78, v72, v73\n"
      "v_cvt_pk_bf16_f32 v79, v74, v75\n"
      "v_mfma_f32_16x16x32_bf16 v[40:43], v[168:171], v[56:59], v[40:43]\n"
      "buffer_load_dwordx4 v[160:163], v80, s[60:63], s76 offen offset:2048 sc1\n"
      "ds_write_b64 v86, v[78:79] offset:11264\n"
      "v_mfma_f32_16x16x32_bf16 v[8:11], v[164:167], a[0:3], 0\n"
      "s_lshl_b32 s69, s35, 19\n"
      "v_mfma_f32_16x16x32_bf16 v[8:11], v[172:175], a[16:19], v[8:11]\n"
      "v_mfma_f32_16x16x32_bf16 v[12:15], v[164:167], a[4:7], 0\n"
      "v_mfma_f32_16x16x32_bf16 v[12:15], v[172:175], a[20:23], v[12:15]\n"
      "buffer_load_dwordx4 v[168:171], v80, s[60:63], s76 offen offset:3072 sc1\n"
      "v_mfma_f32_16x16x32_bf16 v[16:19], v[164:167], a[8:11], 0\n"
      "s_mul_i32 s11, s7, 38\n"
      "s_add_u32 s76, s77, 12288\n"
      "s_lshl_b32 s70, s64, 11\n"
      "v_mul_f32 v40, v40, v92\n"
      "v_mfma_f32_16x16x32_bf16 v[16:19], v[172:175], a[24:27], v[16:19]\n"
      "v_mul_f32 v41, v41, v92\n"
      "v_mul_f32 v42, v42, v92\n"
      "v_mul_f32 v43, v43, v92\n"
      "v_mfma_f32_16x16x32_bf16 v[20:23], v[164:167], a[12:15], 0\n"
      "s_waitcnt vmcnt(37)\n"
      "v_exp_f32 v72, v40\n"
      "v_mfma_f32_16x16x32_bf16 v[20:23], v[172:175], a[28:31], v[20:23]\n"
      "buffer_load_dwordx4 v[164:167], v80, s[60:63], s76 offen offset:0 sc1\n"
      "v_mfma_f32_16x16x32_bf16 v[20:23], v[176:179], a[44:47], v[20:23]\n"
      "s_add_u32 s11, s11, s79\n"
      "v_exp_f32 v73, v41\n"
      "v_mfma_f32_16x16x32_bf16 v[20:23], v[184:187], a[60:63], v[20:23]\n"
      "s_add_u32 s69, s69, s70\n"
      "v_exp_f32 v74, v42\n"
      "v_mfma_f32_16x16x32_bf16 v[16:19], v[176:179], a[40:43], v[16:19]\n"
      "v_exp_f32 v75, v43\n"
      "v_add_f32 v72, 1.0, v72\n"
      "v_mfma_f32_16x16x32_bf16 v[16:19], v[184:187], a[56:59], v[16:19]\n"
      "buffer_load_dwordx4 v[172:175], v80, s[60:63], s76 offen offset:1024 sc1\n"
      "v_mfma_f32_16x16x32_bf16 v[12:15], v[176:179], a[36:39], v[12:15]\n"
      "v_add_f32 v73, 1.0, v73\n"
      "v_add_f32 v74, 1.0, v74\n"
      "v_add_f32 v75, 1.0, v75\n"
      "v_mfma_f32_16x16x32_bf16 v[12:15], v[184:187], a[52:55], v[12:15]\n"
      "s_waitcnt vmcnt(37)\n"
      "v_rcp_f32 v72, v72\n"
      "v_mfma_f32_16x16x32_bf16 v[8:11], v[176:179], a[32:35], v[8:11]\n"
      "s_add_u32 s69, s69, 7602176\n"
      "v_rcp_f32 v73, v73\n"
      "v_mfma_f32_16x16x32_bf16 v[8:11], v[184:187], a[48:51], v[8:11]\n"
      "buffer_load_dwordx4 v[176:179], v80, s[60:63], s76 offen offset:2048 sc1\n"
      "v_mfma_f32_16x16x32_bf16 v[8:11], v[180:183], a[64:67], v[8:11]\n"
      "v_rcp_f32 v74, v74\n"
      "v_mfma_f32_16x16x32_bf16 v[8:11], v[188:191], a[80:83], v[8:11]\n"
      "v_rcp_f32 v75, v75\n"
      "v_mul_f32 v72, v72, v40\n"
      "v_mfma_f32_16x16x32_bf16 v[12:15], v[180:183], a[68:71], v[12:15]\n"
      "v_mul_f32 v73, v73, v41\n"
      "v_mul_f32 v74, v74, v42\n"
      "v_mul_f32 v75, v75, v43\n"
      "v_mfma_f32_16x16x32_bf16 v[12:15], v[188:191], a[84:87], v[12:15]\n"
      "buffer_load_dwordx4 v[184:187], v80, s[60:63], s76 offen offset:3072 sc1\n"
      "v_mfma_f32_16x16x32_bf16 v[16:19], v[180:183], a[72:75], v[16:19]\n"
      "s_add_u32 s76, s77, 16384\n"
      "s_lshl_b32 s71, s34, 13\n"
      "v_cvt_pk_bf16_f32 v76, v72, v73\n"
      "v_cvt_pk_bf16_f32 v77, v74, v75\n"
      "v_mfma_f32_16x16x32_bf16 v[16:19], v[188:191], a[88:91], v[16:19]\n"
      "ds_write_b64 v86, v[76:77] offset:16384\n"
      "v_mfma_f32_16x16x32_bf16 v[20:23], v[180:183], a[76:79], v[20:23]\n"
      "s_waitcnt vmcnt(37)\n"
      "v_mul_f32 v44, v44, v93\n"
      "v_mul_f32 v45, v45, v93\n"
      "v_mul_f32 v46, v46, v93\n"
      "v_mfma_f32_16x16x32_bf16 v[20:23], v[188:191], a[92:95], v[20:23]\n"
      "buffer_load_dwordx4 v[180:183], v80, s[60:63], s76 offen offset:0 sc1\n"
      "v_mfma_f32_16x16x32_bf16 v[20:23], v[192:195], a[108:111], v[20:23]\n"
      "v_mul_f32 v47, v47, v93\n"
      "v_exp_f32 v72, v44\n"
      "v_mfma_f32_16x16x32_bf16 v[20:23], v[200:203], a[124:127], v[20:23]\n"
      "s_add_u32 s26, s69, s71\n"
      "v_exp_f32 v73, v45\n"
      "v_mfma_f32_16x16x32_bf16 v[16:19], v[192:195], a[104:107], v[16:19]\n"
      "v_exp_f32 v74, v46\n"
      "v_mfma_f32_16x16x32_bf16 v[16:19], v[200:203], a[120:123], v[16:19]\n"
      "buffer_load_dwordx4 v[188:191], v80, s[60:63], s76 offen offset:1024 sc1\n"
      "v_mfma_f32_16x16x32_bf16 v[12:15], v[192:195], a[100:103], v[12:15]\n"
      "v_exp_f32 v75, v47\n"
      "v_add_f32 v72, 1.0, v72\n"
      "v_mfma_f32_16x16x32_bf16 v[12:15], v[200:203], a[116:119], v[12:15]\n"
      "s_waitcnt vmcnt(37)\n"
      "v_add_f32 v73, 1.0, v73\n"
      "v_add_f32 v74, 1.0, v74\n"
      "v_add_f32 v75, 1.0, v75\n"
      "v_mfma_f32_16x16x32_bf16 v[8:11], v[192:195], a[96:99], v[8:11]\n"
      "s_lshl_b32 s72, s34, 16\n"
      "v_rcp_f32 v72, v72\n"
      "v_mfma_f32_16x16x32_bf16 v[8:11], v[200:203], a[112:115], v[8:11]\n"
      "buffer_load_dwordx4 v[192:195], v80, s[60:63], s76 offen offset:2048 sc1\n"
      "v_mfma_f32_16x16x32_bf16 v[8:11], v[196:199], a[128:131], v[8:11]\n"
      "s_mov_b32 s77, s11\n"
      "s_add_u32 s76, s77, 0\n"
      "v_rcp_f32 v73, v73\n"
      "v_mfma_f32_16x16x32_bf16 v[8:11], v[204:207], a[144:147], v[8:11]\n"
      "v_rcp_f32 v74, v74\n"
      "v_mfma_f32_16x16x32_bf16 v[12:15], v[196:199], a[132:135], v[12:15]\n"
      "v_rcp_f32 v75, v75\n"
      "v_mul_f32 v72, v72, v44\n"
      "v_mfma_f32_16x16x32_bf16 v[12:15], v[204:207], a[148:151], v[12:15]\n"
      "buffer_load_dwordx4 v[200:203], v80, s[60:63], s76 offen offset:0 sc1\n"
      "v_mfma_f32_16x16x32_bf16 v[16:19], v[196:199], a[136:139], v[16:19]\n"
      "s_add_u32 s27, s69, s72\n"
      "v_mul_f32 v73, v73, v45\n"
      "v_mul_f32 v74, v74, v46\n"
      "v_mul_f32 v75, v75, v47\n"
      "v_mfma_f32_16x16x32_bf16 v[16:19], v[204:207], a[152:155], v[16:19]\n"
      "v_cvt_pk_bf16_f32 v78, v72, v73\n"
      "v_cvt_pk_bf16_f32 v79, v74, v75\n"
      "v_mfma_f32_16x16x32_bf16 v[20:23], v[196:199], a[140:143], v[20:23]\n"
      "s_waitcnt vmcnt(37)\n"
      "ds_write_b64 v86, v[78:79] offset:17408\n"
      "v_mfma_f32_16x16x32_bf16 v[20:23], v[204:207], a[156:159], v[20:23]\n"
      "buffer_load_dwordx4 v[196:199], v80, s[60:63], s76 offen offset:1024 sc1\n"
      "v_mfma_f32_16x16x32_bf16 v[20:23], v[208:211], a[172:175], v[20:23]\n"
      "s_lshl_b32 s69, s35, 16\n"
      "v_mul_f32 v48, v48, v94\n"
      "v_mul_f32 v49, v49, v94\n"
      "v_mul_f32 v50, v50, v94\n"
      "v_mfma_f32_16x16x32_bf16 v[20:23], v[212:215], a[188:191], v[20:23]\n"
      "v_mul_f32 v51, v51, v94\n"
      "v_exp_f32 v72, v48\n"
      "v_mfma_f32_16x16x32_bf16 v[16:19], v[208:211], a[168:171], v[16:19]\n"
      "v_exp_f32 v73, v49\n"
      "v_mfma_f32_16x16x32_bf16 v[16:19], v[212:215], a[184:187], v[16:19]\n"
      "buffer_load_dwordx4 v[204:207], v80, s[60:63], s76 offen offset:2048 sc1\n"
      "v_mfma_f32_16x16x32_bf16 v[12:15], v[208:211], a[164:167], v[12:15]\n"
      "v_exp_f32 v74, v50\n"
      "v_mfma_f32_16x16x32_bf16 v[12:15], v[212:215], a[180:183], v[12:15]\n"
      "s_waitcnt vmcnt(36)\n"
      "v_exp_f32 v75, v51\n"
      "v_add_f32 v72, 1.0, v72\n"
      "v_mfma_f32_16x16x32_bf16 v[8:11], v[208:211], a[160:163], v[8:11]\n"
      "s_add_u32 s69, s69, s70\n"
      "v_add_f32 v73, 1.0, v73\n"
      "v_add_f32 v74, 1.0, v74\n"
      "v_add_f32 v75, 1.0, v75\n"
      "v_mfma_f32_16x16x32_bf16 v[8:11], v[212:215], a[176:179], v[8:11]\n"
      "buffer_load_dwordx4 v[208:211], v80, s[60:63], s76 offen offset:3072 sc1\n"
      "v_mfma_f32_16x16x32_bf16 v[8:11], v[216:219], a[192:195], v[8:11]\n"
      "s_add_u32 s76, s77, 4096\n"
      "v_rcp_f32 v72, v72\n"
      "v_mfma_f32_16x16x32_bf16 v[8:11], v[96:99], a[208:211], v[8:11]\n"
      "v_rcp_f32 v73, v73\n"
      "v_mfma_f32_16x16x32_bf16 v[12:15], v[216:219], a[196:199], v[12:15]\n"
      "v_rcp_f32 v74, v74\n"
      "v_mfma_f32_16x16x32_bf16 v[12:15], v[96:99], a[212:215], v[12:15]\n"
      "buffer_load_dwordx4 v[212:215], v80, s[60:63], s76 offen offset:0 sc1\n"
      "v_mfma_f32_16x16x32_bf16 v[16:19], v[216:219], a[200:203], v[16:19]\n"
      "s_add_u32 s69, s69, 5505024\n"
      "v_rcp_f32 v75, v75\n"
      "v_mul_f32 v72, v72, v48\n"
      "v_mfma_f32_16x16x32_bf16 v[16:19], v[96:99], a[216:219], v[16:19]\n"
      "v_mul_f32 v73, v73, v49\n"
      "v_mul_f32 v74, v74, v50\n"
      "v_mul_f32 v75, v75, v51\n"
      "v_mfma_f32_16x16x32_bf16 v[20:23], v[216:219], a[204:207], v[20:23]\n"
      "s_waitcnt vmcnt(35)\n"
      "v_cvt_pk_bf16_f32 v76, v72, v73\n"
      "v_cvt_pk_bf16_f32 v77, v74, v75\n"
      "v_mfma_f32_16x16x32_bf16 v[20:23], v[96:99], a[220:223], v[20:23]\n"
      "buffer_load_dwordx4 v[216:219], v80, s[60:63], s76 offen offset:1024 sc1\n"
      "v_mfma_f32_16x16x32_bf16 v[20:23], v[104:107], a[236:239], v[20:23]\n"
      "s_add_u32 s29, s69, s71\n"
      "ds_write_b64 v86, v[76:77] offset:18432\n"
      "v_mfma_f32_16x16x32_bf16 v[20:23], v[100:103], a[252:255], v[20:23]\n"
      "v_mul_f32 v52, v52, v95\n"
      "v_mul_f32 v53, v53, v95\n"
      "v_mul_f32 v54, v54, v95\n"
      "v_mfma_f32_16x16x32_bf16 v[16:19], v[104:107], a[232:235], v[16:19]\n"
      "v_mul_f32 v55, v55, v95\n"
      "v_exp_f32 v72, v52\n"
      "v_mfma_f32_16x16x32_bf16 v[16:19], v[100:103], a[248:251], v[16:19]\n"
      "buffer_load_dwordx4 v[96:99], v80, s[60:63], s76 offen offset:2048 sc1\n"
      "v_mfma_f32_16x16x32_bf16 v[12:15], v[104:107], a[228:231], v[12:15]\n"
      "v_exp_f32 v73, v53\n"
      "v_mfma_f32_16x16x32_bf16 v[12:15], v[100:103], a[244:247], v[12:15]\n"
      "s_lshl_b32 s71, s34, 7\n"
      "v_exp_f32 v74, v54\n"
      "v_mfma_f32_16x16x32_bf16 v[8:11], v[104:107], a[224:227], v[8:11]\n"
      "s_waitcnt vmcnt(33)\n"
      "v_exp_f32 v75, v55\n"
      "v_add_f32 v72, 1.0, v72\n"
      "v_mfma_f32_16x16x32_bf16 v[8:11], v[100:103], a[240:243], v[8:11]\n"
      "buffer_load_dwordx4 v[104:107], v80, s[60:63], s76 offen offset:3072 sc1\n"
      "v_mfma_f32_16x16x32_bf16 v[8:11], v[108:111], v[220:223], v[8:11]\n"
      "s_add_u32 s76, s77, 8192\n"
      "v_add_f32 v73, 1.0, v73\n"
      "v_add_f32 v74, 1.0, v74\n"
      "v_add_f32 v75, 1.0, v75\n"
      "v_mfma_f32_16x16x32_bf16 v[8:11], v[112:115], v[236:239], v[8:11]\n"
      "v_rcp_f32 v72, v72\n"
      "v_mfma_f32_16x16x32_bf16 v[12:15], v[108:111], v[224:227], v[12:15]\n"
      "v_rcp_f32 v73, v73\n"
      "v_mfma_f32_16x16x32_bf16 v[12:15], v[112:115], v[240:243], v[12:15]\n"
      "buffer_load_dwordx4 v[100:103], v80, s[60:63], s76 offen offset:0 sc1\n"
      "v_mfma_f32_16x16x32_bf16 v[16:19], v[108:111], v[228:231], v[16:19]\n"
      "s_add_u32 s28, s69, s71\n"
      "v_rcp_f32 v74, v74\n"
      "v_mfma_f32_16x16x32_bf16 v[16:19], v[112:115], v[244:247], v[16:19]\n"
      "v_rcp_f32 v75, v75\n"
      "v_mul_f32 v72, v72, v52\n"
      "v_mfma_f32_16x16x32_bf16 v[20:23], v[108:111], v[232:235], v[20:23]\n"
      "s_waitcnt vmcnt(34)\n"
      "v_mul_f32 v73, v73, v53\n"
      "v_mul_f32 v74, v74, v54\n"
      "v_mul_f32 v75, v75, v55\n"
      "v_mfma_f32_16x16x32_bf16 v[20:23], v[112:115], v[248:251], v[20:23]\n"
      "buffer_load_dwordx4 v[108:111], v80, s[60:63], s76 offen offset:1024 sc1\n"
      "v_mfma_f32_16x16x32_bf16 v[20:23], v[120:123], v[68:71], v[20:23]\n"
      "s_lshl_b32 s69, s35, 13\n"
      "v_cvt_pk_bf16_f32 v78, v72, v73\n"
      "v_cvt_pk_bf16_f32 v79, v74, v75\n"
      "v_mfma_f32_16x16x32_bf16 v[16:19], v[120:123], v[64:67], v[16:19]\n"
      "ds_write_b64 v86, v[78:79] offset:19456\n"
      "v_mfma_f32_16x16x32_bf16 v[12:15], v[120:123], v[60:63], v[12:15]\n"
      "s_waitcnt vmcnt(32)\n"
      "v_mfma_f32_16x16x32_bf16 v[8:11], v[120:123], v[56:59], v[8:11]\n"
      "buffer_load_dwordx4 v[112:115], v80, s[60:63], s76 offen offset:2048 sc1\n"
      "v_mfma_f32_16x16x32_bf16 v[24:27], v[116:119], a[0:3], 0\n"
      "v_mfma_f32_16x16x32_bf16 v[24:27], v[124:127], a[16:19], v[24:27]\n"
      "s_lshl_b32 s70, s64, 7\n"
      "v_mfma_f32_16x16x32_bf16 v[28:31], v[116:119], a[4:7], 0\n"
      "v_mfma_f32_16x16x32_bf16 v[28:31], v[124:127], a[20:23], v[28:31]\n"
      "buffer_load_dwordx4 v[120:123], v80, s[60:63], s76 offen offset:3072 sc1\n"
      "v_mfma_f32_16x16x32_bf16 v[32:35], v[116:119], a[8:11], 0\n"
      "s_mul_i32 s11, s8, 38\n"
      "s_add_u32 s76, s77, 12288\n"
      "v_mul_f32 v8, v8, v92\n"
      "v_mul_f32 v9, v9, v92\n"
      "v_mfma_f32_16x16x32_bf16 v[32:35], v[124:127], a[24:27], v[32:35]\n"
      "s_waitcnt vmcnt(30)\n"
      "v_mul_f32 v10, v10, v92\n"
      "v_mul_f32 v11, v11, v92\n"
      "v_mfma_f32_16x16x32_bf16 v[36:39], v[116:119], a[12:15], 0\n"
      "s_add_u32 s69, s69, s70\n"
      "v_exp_f32 v72, v8\n"
      "v_mfma_f32_16x16x32_bf16 v[36:39], v[124:127], a[28:31], v[36:39]\n"
      "buffer_load_dwordx4 v[116:119], v80, s[60:63], s76 offen offset:0 sc1\n"
      "v_mfma_f32_16x16x32_bf16 v[36:39], v[128:131], a[44:47], v[36:39]\n"
      "s_add_u32 s11, s11, s79\n"
      "v_exp_f32 v73, v9\n"
      "v_mfma_f32_16x16x32_bf16 v[36:39], v[136:139], a[60:63], v[36:39]\n"
      "v_exp_f32 v74, v10\n"
      "v_mfma_f32_16x16x32_bf16 v[32:35], v[128:131], a[40:43], v[32:35]\n"
      "v_exp_f32 v75, v11\n"
      "v_add_f32 v72, 1.0, v72\n"
      "v_mfma_f32_16x16x32_bf16 v[32:35], v[136:139], a[56:59], v[32:35]\n"
      "buffer_load_dwordx4 v[124:127], v80, s[60:63], s76 offen offset:1024 sc1\n"
      "v_mfma_f32_16x16x32_bf16 v[28:31], v[128:131], a[36:39], v[28:31]\n"
      "s_add_u32 s69, s69, 65536\n"
      "v_add_f32 v73, 1.0, v73\n"
      "v_add_f32 v74, 1.0, v74\n"
      "v_add_f32 v75, 1.0, v75\n"
      "v_mfma_f32_16x16x32_bf16 v[28:31], v[136:139], a[52:55], v[28:31]\n"
      "v_rcp_f32 v72, v72\n"
      "v_mfma_f32_16x16x32_bf16 v[24:27], v[128:131], a[32:35], v[24:27]\n"
      "s_waitcnt vmcnt(29)\n"
      "v_rcp_f32 v73, v73\n"
      "v_mfma_f32_16x16x32_bf16 v[24:27], v[136:139], a[48:51], v[24:27]\n"
      "buffer_load_dwordx4 v[128:131], v80, s[60:63], s76 offen offset:2048 sc1\n"
      "v_mfma_f32_16x16x32_bf16 v[24:27], v[132:135], a[64:67], v[24:27]\n"
      "v_rcp_f32 v74, v74\n"
      "v_mfma_f32_16x16x32_bf16 v[24:27], v[140:143], a[80:83], v[24:27]\n"
      "s_lshl_b32 s70, s34, 10\n"
      "v_rcp_f32 v75, v75\n"
      "v_mfma_f32_16x16x32_bf16 v[28:31], v[132:135], a[68:71], v[28:31]\n"
      "v_mul_f32 v72, v72, v8\n"
      "v_mul_f32 v73, v73, v9\n"
      "v_mul_f32 v74, v74, v10\n"
      "v_mfma_f32_16x16x32_bf16 v[28:31], v[140:143], a[84:87], v[28:31]\n"
      "buffer_load_dwordx4 v[136:139], v80, s[60:63], s76 offen offset:3072 sc1\n"
      "v_mfma_f32_16x16x32_bf16 v[32:35], v[132:135], a[72:75], v[32:35]\n"
      "s_add_u32 s76, s77, 16384\n"
      "v_mul_f32 v75, v75, v11\n"
      "v_cvt_pk_bf16_f32 v76, v72, v73\n"
      "v_cvt_pk_bf16_f32 v77, v74, v75\n"
      "v_mfma_f32_16x16x32_bf16 v[32:35], v[140:143], a[88:91], v[32:35]\n"
      "s_waitcnt vmcnt(28)\n"
      "ds_write_b64 v86, v[76:77] offset:24576\n"
      "v_mfma_f32_16x16x32_bf16 v[36:39], v[132:135], a[76:79], v[36:39]\n"
      "s_add_u32 s30, s69, s70\n"
      "v_mul_f32 v12, v12, v93\n"
      "v_mul_f32 v13, v13, v93\n"
      "v_mul_f32 v14, v14, v93\n"
      "v_mfma_f32_16x16x32_bf16 v[36:39], v[140:143], a[92:95], v[36:39]\n"
      "buffer_load_dwordx4 v[132:135], v80, s[60:63], s76 offen offset:0 sc1\n"
      "v_mfma_f32_16x16x32_bf16 v[36:39], v[144:147], a[108:111], v[36:39]\n"
      "v_mul_f32 v15, v15, v93\n"
      "v_exp_f32 v72, v12\n"
      "v_mfma_f32_16x16x32_bf16 v[36:39], v[152:155], a[124:127], v[36:39]\n"
      "v_exp_f32 v73, v13\n"
      "v_mfma_f32_16x16x32_bf16 v[32:35], v[144:147], a[104:107], v[32:35]\n"
      "v_exp_f32 v74, v14\n"
      "v_mfma_f32_16x16x32_bf16 v[32:35], v[152:155], a[120:123], v[32:35]\n"
      "buffer_load_dwordx4 v[140:143], v80, s[60:63], s76 offen offset:1024 sc1\n"
      "v_mfma_f32_16x16x32_bf16 v[28:31], v[144:147], a[100:103], v[28:31]\n"
      "s_mov_b32 s31, s69\n"
      "v_exp_f32 v75, v15\n"
      "v_add_f32 v72, 1.0, v72\n"
      "v_mfma_f32_16x16x32_bf16 v[28:31], v[152:155], a[116:119], v[28:31]\n"
      "v_add_f32 v73, 1.0, v73\n"
      "v_add_f32 v74, 1.0, v74\n"
      "v_mfma_f32_16x16x32_bf16 v[24:27], v[144:147], a[96:99], v[24:27]\n"
      "s_waitcnt vmcnt(27)\n"
      "v_add_f32 v75, 1.0, v75\n"
      "v_rcp_f32 v72, v72\n"
      "v_mfma_f32_16x16x32_bf16 v[24:27], v[152:155], a[112:115], v[24:27]\n"
      "buffer_load_dwordx4 v[144:147], v80, s[60:63], s76 offen offset:2048 sc1\n"
      "v_mfma_f32_16x16x32_bf16 v[24:27], v[148:151], a[128:131], v[24:27]\n"
      "s_mov_b32 s77, s11\n"
      "s_add_u32 s76, s77, 0\n"
      "v_rcp_f32 v73, v73\n"
      "v_mfma_f32_16x16x32_bf16 v[24:27], v[156:159], a[144:147], v[24:27]\n"
      "s_mul_i32 s69, s35, 73728\n"
      "v_rcp_f32 v74, v74\n"
      "v_mfma_f32_16x16x32_bf16 v[28:31], v[148:151], a[132:135], v[28:31]\n"
      "v_rcp_f32 v75, v75\n"
      "v_mul_f32 v72, v72, v12\n"
      "v_mfma_f32_16x16x32_bf16 v[28:31], v[156:159], a[148:151], v[28:31]\n"
      "buffer_load_dwordx4 v[152:155], v80, s[60:63], s76 offen offset:0 sc1\n"
      "v_mfma_f32_16x16x32_bf16 v[32:35], v[148:151], a[136:139], v[32:35]\n"
      "v_mul_f32 v73, v73, v13\n"
      "v_mul_f32 v74, v74, v14\n"
      "v_mul_f32 v75, v75, v15\n"
      "v_mfma_f32_16x16x32_bf16 v[32:35], v[156:159], a[152:155], v[32:35]\n"
      "s_waitcnt vmcnt(27)\n"
      "v_cvt_pk_bf16_f32 v78, v72, v73\n"
      "v_cvt_pk_bf16_f32 v79, v74, v75\n"
      "v_mfma_f32_16x16x32_bf16 v[36:39], v[148:151], a[140:143], v[36:39]\n"
      "s_lshl2_add_u32 s70, s34, s64\n"
      "ds_write_b64 v86, v[78:79] offset:25600\n"
      "v_mfma_f32_16x16x32_bf16 v[36:39], v[156:159], a[156:159], v[36:39]\n"
      "buffer_load_dwordx4 v[148:151], v80, s[60:63], s76 offen offset:1024 sc1\n"
      "v_mfma_f32_16x16x32_bf16 v[36:39], v[160:163], a[172:175], v[36:39]\n"
      "v_mul_f32 v16, v16, v94\n"
      "v_mul_f32 v17, v17, v94\n"
      "v_mul_f32 v18, v18, v94\n"
      "v_mfma_f32_16x16x32_bf16 v[36:39], v[168:171], a[188:191], v[36:39]\n"
      "v_mul_f32 v19, v19, v94\n"
      "v_mfma_f32_16x16x32_bf16 v[32:35], v[160:163], a[168:171], v[32:35]\n"
      "v_exp_f32 v72, v16\n"
      "v_mfma_f32_16x16x32_bf16 v[32:35], v[168:171], a[184:187], v[32:35]\n"
      "buffer_load_dwordx4 v[156:159], v80, s[60:63], s76 offen offset:2048 sc1\n"
      "v_mfma_f32_16x16x32_bf16 v[28:31], v[160:163], a[164:167], v[28:31]\n"
      "s_lshl_b32 s70, s70, 9\n"
      "v_exp_f32 v73, v17\n"
      "v_mfma_f32_16x16x32_bf16 v[28:31], v[168:171], a[180:183], v[28:31]\n"
      "v_exp_f32 v74, v18\n"
      "v_mfma_f32_16x16x32_bf16 v[24:27], v[160:163], a[160:163], v[24:27]\n"
      "s_waitcnt vmcnt(27)\n"
      "v_exp_f32 v75, v19\n"
      "v_add_f32 v72, 1.0, v72\n"
      "v_mfma_f32_16x16x32_bf16 v[24:27], v[168:171], a[176:179], v[24:27]\n"
      "buffer_load_dwordx4 v[160:163], v80, s[60:63], s76 offen offset:3072 sc1\n"
      "v_mfma_f32_16x16x32_bf16 v[24:27], v[164:167], a[192:195], v[24:27]\n"
      "s_add_u32 s76, s77, 4096\n"
      "s_add_u32 s69, s69, s70\n"
      "v_add_f32 v73, 1.0, v73\n"
      "v_add_f32 v74, 1.0, v74\n"
      "v_mfma_f32_16x16x32_bf16 v[24:27], v[172:175], a[208:211], v[24:27]\n"
      "v_add_f32 v75, 1.0, v75\n"
      "v_mfma_f32_16x16x32_bf16 v[28:31], v[164:167], a[196:199], v[28:31]\n"
      "v_rcp_f32 v72, v72\n"
      "v_mfma_f32_16x16x32_bf16 v[28:31], v[172:175], a[212:215], v[28:31]\n"
      "buffer_load_dwordx4 v[168:171], v80, s[60:63], s76 offen offset:0 sc1\n"
      "v_mfma_f32_16x16x32_bf16 v[32:35], v[164:167], a[200:203], v[32:35]\n"
      "v_rcp_f32 v73, v73\n"
      "v_mfma_f32_16x16x32_bf16 v[32:35], v[172:175], a[216:219], v[32:35]\n"
      "s_waitcnt vmcnt(27)\n"
      "v_rcp_f32 v74, v74\n"
      "v_mfma_f32_16x16x32_bf16 v[36:39], v[164:167], a[204:207], v[36:39]\n"
      "s_add_u32 s25, s69, 2883584\n"
      "v_rcp_f32 v75, v75\n"
      "v_mul_f32 v72, v72, v16\n"
      "v_mfma_f32_16x16x32_bf16 v[36:39], v[172:175], a[220:223], v[36:39]\n"
      "buffer_load_dwordx4 v[164:167], v80, s[60:63], s76 offen offset:1024 sc1\n"
      "v_mfma_f32_16x16x32_bf16 v[36:39], v[176:179], a[236:239], v[36:39]\n"
      "v_mul_f32 v73, v73, v17\n"
      "v_mul_f32 v74, v74, v18\n"
      "v_mul_f32 v75, v75, v19\n"
      "v_mfma_f32_16x16x32_bf16 v[36:39], v[184:187], a[252:255], v[36:39]\n"
      "v_cvt_pk_bf16_f32 v76, v72, v73\n"
      "v_cvt_pk_bf16_f32 v77, v74, v75\n"
      "v_mfma_f32_16x16x32_bf16 v[32:35], v[176:179], a[232:235], v[32:35]\n"
      "ds_write_b64 v86, v[76:77] offset:26624\n"
      "v_mfma_f32_16x16x32_bf16 v[32:35], v[184:187], a[248:251], v[32:35]\n"
      "buffer_load_dwordx4 v[172:175], v80, s[60:63], s76 offen offset:2048 sc1\n"
      "v_mfma_f32_16x16x32_bf16 v[28:31], v[176:179], a[228:231], v[28:31]\n"
      "s_mov_b32 s16, 0xffff0000\n"
      "v_mul_f32 v20, v20, v95\n"
      "v_mul_f32 v21, v21, v95\n"
      "v_mul_f32 v22, v22, v95\n"
      "v_mfma_f32_16x16x32_bf16 v[28:31], v[184:187], a[244:247], v[28:31]\n"
      "v_mul_f32 v23, v23, v95\n"
      "v_mfma_f32_16x16x32_bf16 v[24:27], v[176:179], a[224:227], v[24:27]\n"
      "s_waitcnt vmcnt(27)\n"
      "v_exp_f32 v72, v20\n"
      "v_mfma_f32_16x16x32_bf16 v[24:27], v[184:187], a[240:243], v[24:27]\n"
      "buffer_load_dwordx4 v[176:179], v80, s[60:63], s76 offen offset:3072 sc1\n"
      "v_mfma_f32_16x16x32_bf16 v[24:27], v[180:183], v[220:223], v[24:27]\n"
      "s_add_u32 s76, s77, 8192\n"
      "s_mov_b32 s17, 0xbf317218\n"
      "v_exp_f32 v73, v21\n"
      "v_mfma_f32_16x16x32_bf16 v[24:27], v[188:191], v[236:239], v[24:27]\n"
      "v_exp_f32 v74, v22\n"
      "v_mfma_f32_16x16x32_bf16 v[28:31], v[180:183], v[224:227], v[28:31]\n"
      "v_exp_f32 v75, v23\n"
      "v_add_f32 v72, 1.0, v72\n"
      "v_mfma_f32_16x16x32_bf16 v[28:31], v[188:191], v[240:243], v[28:31]\n"
      "buffer_load_dwordx4 v[184:187], v80, s[60:63], s76 offen offset:0 sc1\n"
      "v_mfma_f32_16x16x32_bf16 v[32:35], v[180:183], v[228:231], v[32:35]\n"
      "v_add_f32 v73, 1.0, v73\n"
      "v_add_f32 v74, 1.0, v74\n"
      "v_add_f32 v75, 1.0, v75\n"
      "v_mfma_f32_16x16x32_bf16 v[32:35], v[188:191], v[244:247], v[32:35]\n"
      "v_rcp_f32 v72, v72\n"
      "v_mfma_f32_16x16x32_bf16 v[36:39], v[180:183], v[232:235], v[36:39]\n"
      "s_waitcnt vmcnt(28)\n"
      "v_rcp_f32 v73, v73\n"
      "v_mfma_f32_16x16x32_bf16 v[36:39], v[188:191], v[248:251], v[36:39]\n"
      "buffer_load_dwordx4 v[180:183], v80, s[60:63], s76 offen offset:1024 sc1\n"
      "v_mfma_f32_16x16x32_bf16 v[36:39], v[192:195], v[68:71], v[36:39]\n"
      "v_rcp_f32 v74, v74\n"
      "v_mfma_f32_16x16x32_bf16 v[32:35], v[192:195], v[64:67], v[32:35]\n"
      "v_rcp_f32 v75, v75\n"
      "v_mfma_f32_16x16x32_bf16 v[28:31], v[192:195], v[60:63], v[28:31]\n"
      "s_waitcnt vmcnt(27)\n"
      "v_mul_f32 v72, v72, v20\n"
      "v_mul_f32 v73, v73, v21\n"
      "v_mul_f32 v74, v74, v22\n"
      "v_mfma_f32_16x16x32_bf16 v[24:27], v[192:195], v[56:59], v[24:27]\n"
      "buffer_load_dwordx4 v[188:191], v80, s[60:63], s76 offen offset:2048 sc1\n"
      "v_mfma_f32_16x16x32_bf16 v[40:43], v[200:203], a[0:3], 0\n"
      "v_mul_f32 v75, v75, v23\n"
      "v_cvt_pk_bf16_f32 v78, v72, v73\n"
      "v_cvt_pk_bf16_f32 v79, v74, v75\n"
      "v_mfma_f32_16x16x32_bf16 v[40:43], v[196:199], a[16:19], v[40:43]\n"
      "ds_write_b64 v86, v[78:79] offset:27648\n"
      "v_mfma_f32_16x16x32_bf16 v[44:47], v[200:203], a[4:7], 0\n"
      "v_mfma_f32_16x16x32_bf16 v[44:47], v[196:199], a[20:23], v[44:47]\n"
      "buffer_load_dwordx4 v[192:195], v80, s[60:63], s76 offen offset:3072 sc1\n"
      "v_mfma_f32_16x16x32_bf16 v[48:51], v[200:203], a[8:11], 0\n"
      "s_mul_i32 s11, s9, 38\n"
      "s_add_u32 s76, s77, 12288\n"
      "v_mov_b32 v2, -1\n"
      "v_mul_f32 v24, v24, v92\n"
      "v_mfma_f32_16x16x32_bf16 v[48:51], v[196:199], a[24:27], v[48:51]\n"
      "v_mov_b32 v3, -1\n"
      "v_mul_f32 v25, v25, v92\n"
      "v_mul_f32 v26, v26, v92\n"
      "v_mfma_f32_16x16x32_bf16 v[52:55], v[200:203], a[12:15], 0\n"
      "s_waitcnt vmcnt(27)\n"
      "v_mul_f32 v27, v27, v92\n"
      "v_exp_f32 v72, v24\n"
      "v_mfma_f32_16x16x32_bf16 v[52:55], v[196:199], a[28:31], v[52:55]\n"
      "buffer_load_dwordx4 v[200:203], v80, s[60:63], s76 offen offset:0 sc1\n"
      "v_mfma_f32_16x16x32_bf16 v[52:55], v[204:207], a[44:47], v[52:55]\n"
      "s_add_u32 s11, s11, s79\n"
      "v_mov_b32 v4, -1\n"
      "v_exp_f32 v73, v25\n"
      "v_mfma_f32_16x16x32_bf16 v[52:55], v[208:211], a[60:63], v[52:55]\n"
      "v_exp_f32 v74, v26\n"
      "v_mfma_f32_16x16x32_bf16 v[48:51], v[204:207], a[40:43], v[48:51]\n"
      "v_mov_b32 v5, -1\n"
      "v_exp_f32 v75, v27\n"
      "v_mfma_f32_16x16x32_bf16 v[48:51], v[208:211], a[56:59], v[48:51]\n"
      "buffer_load_dwordx4 v[196:199], v80, s[60:63], s76 offen offset:1024 sc1\n"
      "v_mfma_f32_16x16x32_bf16 v[44:47], v[204:207], a[36:39], v[44:47]\n"
      "s_lshr_b32 s22, s10, 11\n"
      "v_add_f32 v72, 1.0, v72\n"
      "v_add_f32 v73, 1.0, v73\n"
      "v_add_f32 v74, 1.0, v74\n"
      "v_mfma_f32_16x16x32_bf16 v[44:47], v[208:211], a[52:55], v[44:47]\n"
      "s_waitcnt vmcnt(27)\n"
      "v_add_f32 v75, 1.0, v75\n"
      "v_rcp_f32 v72, v72\n"
      "v_mfma_f32_16x16x32_bf16 v[40:43], v[204:207], a[32:35], v[40:43]\n"
      "s_add_u32 s22, s22, 1\n"
      "v_rcp_f32 v73, v73\n"
      "v_mfma_f32_16x16x32_bf16 v[40:43], v[208:211], a[48:51], v[40:43]\n"
      "buffer_load_dwordx4 v[204:207], v80, s[60:63], s76 offen offset:2048 sc1\n"
      "v_mfma_f32_16x16x32_bf16 v[40:43], v[212:215], a[64:67], v[40:43]\n"
      "v_rcp_f32 v74, v74\n"
      "v_mfma_f32_16x16x32_bf16 v[40:43], v[216:219], a[80:83], v[40:43]\n"
      "s_add_u32 s22, s34, s22\n"
      "v_rcp_f32 v75, v75\n"
      "v_mul_f32 v72, v72, v24\n"
      "v_mfma_f32_16x16x32_bf16 v[44:47], v[212:215], a[68:71], v[44:47]\n"
      "v_mul_f32 v73, v73, v25\n"
      "v_mul_f32 v74, v74, v26\n"
      "v_mul_f32 v75, v75, v27\n"
      "v_mfma_f32_16x16x32_bf16 v[44:47], v[216:219], a[84:87], v[44:47]\n"
      "buffer_load_dwordx4 v[208:211], v80, s[60:63], s76 offen offset:3072 sc1\n"
      "v_mfma_f32_16x16x32_bf16 v[48:51], v[212:215], a[72:75], v[48:51]\n"
      "s_add_u32 s76, s77, 16384\n"
      "s_and_b32 s22, s22, 7\n"
      "v_cvt_pk_bf16_f32 v76, v72, v73\n"
      "v_cvt_pk_bf16_f32 v77, v74, v75\n"
      "v_mfma_f32_16x16x32_bf16 v[48:51], v[216:219], a[88:91], v[48:51]\n"
      "s_lshl_b32 s22, s22, 16\n"
      "ds_write_b64 v86, v[76:77] offset:32768\n"
      "v_mfma_f32_16x16x32_bf16 v[52:55], v[212:215], a[76:79], v[52:55]\n"
      "s_waitcnt vmcnt(27)\n"
      "v_mul_f32 v28, v28, v93\n"
      "v_mul_f32 v29, v29, v93\n"
      "v_mul_f32 v30, v30, v93\n"
      "v_mfma_f32_16x16x32_bf16 v[52:55], v[216:219], a[92:95], v[52:55]\n"
      "buffer_load_dwordx4 v[212:215], v80, s[60:63], s76 offen offset:0 sc1\n"
      "v_mfma_f32_16x16x32_bf16 v[52:55], v[96:99], a[108:111], v[52:55]\n"
      "s_add_u32 s22, s22, s26\n"
      "v_mul_f32 v31, v31, v93\n"
      "v_exp_f32 v72, v28\n"
      "v_mfma_f32_16x16x32_bf16 v[52:55], v[104:107], a[124:127], v[52:55]\n"
      "v_exp_f32 v73, v29\n"
      "v_mfma_f32_16x16x32_bf16 v[48:51], v[96:99], a[104:107], v[48:51]\n"
      "buffer_store_dwordx4 v[2:5], v80, s[60:63], s22 offen offset:0 sc1\n"
      "v_mfma_f32_16x16x32_bf16 v[48:51], v[104:107], a[120:123], v[48:51]\n"
      "buffer_load_dwordx4 v[216:219], v80, s[60:63], s76 offen offset:1024 sc1\n"
      "v_mfma_f32_16x16x32_bf16 v[44:47], v[96:99], a[100:103], v[44:47]\n"
      "v_exp_f32 v74, v30\n"
      "v_mfma_f32_16x16x32_bf16 v[44:47], v[104:107], a[116:119], v[44:47]\n"
      "v_exp_f32 v75, v31\n"
      "v_add_f32 v72, 1.0, v72\n"
      "v_mfma_f32_16x16x32_bf16 v[40:43], v[96:99], a[96:99], v[40:43]\n"
      "s_waitcnt vmcnt(28)\n"
      "v_add_f32 v73, 1.0, v73\n"
      "v_add_f32 v74, 1.0, v74\n"
      "v_add_f32 v75, 1.0, v75\n"
      "v_mfma_f32_16x16x32_bf16 v[40:43], v[104:107], a[112:115], v[40:43]\n"
      "buffer_load_dwordx4 v[96:99], v80, s[60:63], s76 offen offset:2048 sc1\n"
      "v_mfma_f32_16x16x32_bf16 v[40:43], v[100:103], a[128:131], v[40:43]\n"
      "s_mov_b32 s77, s11\n"
      "s_add_u32 s76, s77, 0\n"
      "v_rcp_f32 v72, v72\n"
      "v_mfma_f32_16x16x32_bf16 v[40:43], v[108:111], a[144:147], v[40:43]\n"
      "buffer_store_dwordx4 v[2:5], v80, s[60:63], s22 offen offset:1024 sc1\n"
      "v_mfma_f32_16x16x32_bf16 v[44:47], v[100:103], a[132:135], v[44:47]\n"
      "v_rcp_f32 v73, v73\n"
      "v_mfma_f32_16x16x32_bf16 v[44:47], v[108:111], a[148:151], v[44:47]\n"
      "buffer_load_dwordx4 v[104:107], v80, s[60:63], s76 offen offset:0 sc1\n"
      "v_mfma_f32_16x16x32_bf16 v[48:51], v[100:103], a[136:139], v[48:51]\n"
      "v_rcp_f32 v74, v74\n"
      "v_mfma_f32_16x16x32_bf16 v[48:51], v[108:111], a[152:155], v[48:51]\n"
      "v_rcp_f32 v75, v75\n"
      "v_mul_f32 v72, v72, v28\n"
      "v_mfma_f32_16x16x32_bf16 v[52:55], v[100:103], a[140:143], v[52:55]\n"
      "s_waitcnt vmcnt(29)\n"
      "v_mul_f32 v73, v73, v29\n"
      "v_mul_f32 v74, v74, v30\n"
      "v_mul_f32 v75, v75, v31\n"
      "v_mfma_f32_16x16x32_bf16 v[52:55], v[108:111], a[156:159], v[52:55]\n"
      "buffer_load_dwordx4 v[100:103], v80, s[60:63], s76 offen offset:1024 sc1\n"
      "v_mfma_f32_16x16x32_bf16 v[52:55], v[112:115], a[172:175], v[52:55]\n"
      "v_cvt_pk_bf16_f32 v78, v72, v73\n"
      "v_cvt_pk_bf16_f32 v79, v74, v75\n"
      "v_mfma_f32_16x16x32_bf16 v[52:55], v[120:123], a[188:191], v[52:55]\n"
      "ds_write_b64 v86, v[78:79] offset:33792\n"
      "v_mfma_f32_16x16x32_bf16 v[48:51], v[112:115], a[168:171], v[48:51]\n"
      "v_mul_f32 v32, v32, v94\n"
      "v_mul_f32 v33, v33, v94\n"
      "v_mul_f32 v34, v34, v94\n"
      "v_mfma_f32_16x16x32_bf16 v[48:51], v[120:123], a[184:187], v[48:51]\n"
      "buffer_load_dwordx4 v[108:111], v80, s[60:63], s76 offen offset:2048 sc1\n"
      "v_mfma_f32_16x16x32_bf16 v[44:47], v[112:115], a[164:167], v[44:47]\n"
      "v_mul_f32 v35, v35, v94\n"
      "v_exp_f32 v72, v32\n"
      "v_mfma_f32_16x16x32_bf16 v[44:47], v[120:123], a[180:183], v[44:47]\n"
      "v_exp_f32 v73, v33\n"
      "v_mfma_f32_16x16x32_bf16 v[40:43], v[112:115], a[160:163], v[40:43]\n"
      "s_waitcnt vmcnt(29)\n"
      "v_exp_f32 v74, v34\n"
      "v_mfma_f32_16x16x32_bf16 v[40:43], v[120:123], a[176:179], v[40:43]\n"
      "buffer_load_dwordx4 v[112:115], v80, s[60:63], s76 offen offset:3072 sc1\n"
      "v_mfma_f32_16x16x32_bf16 v[40:43], v[116:119], a[192:195], v[40:43]\n"
      "s_add_u32 s76, s77, 4096\n"
      "v_exp_f32 v75, v35\n"
      "v_add_f32 v72, 1.0, v72\n"
      "v_mfma_f32_16x16x32_bf16 v[40:43], v[124:127], a[208:211], v[40:43]\n"
      "v_add_f32 v73, 1.0, v73\n"
      "v_add_f32 v74, 1.0, v74\n"
      "v_add_f32 v75, 1.0, v75\n"
      "v_mfma_f32_16x16x32_bf16 v[44:47], v[116:119], a[196:199], v[44:47]\n"
      "v_rcp_f32 v72, v72\n"
      "v_mfma_f32_16x16x32_bf16 v[44:47], v[124:127], a[212:215], v[44:47]\n"
      "buffer_load_dwordx4 v[120:123], v80, s[60:63], s76 offen offset:0 sc1\n"
      "v_mfma_f32_16x16x32_bf16 v[48:51], v[116:119], a[200:203], v[48:51]\n"
      "v_rcp_f32 v73, v73\n"
      "v_mfma_f32_16x16x32_bf16 v[48:51], v[124:127], a[216:219], v[48:51]\n"
      "v_rcp_f32 v74, v74\n"
      "v_mfma_f32_16x16x32_bf16 v[52:55], v[116:119], a[204:207], v[52:55]\n"
      "s_waitcnt vmcnt(29)\n"
      "v_rcp_f32 v75, v75\n"
      "v_mul_f32 v72, v72, v32\n"
      "v_mfma_f32_16x16x32_bf16 v[52:55], v[124:127], a[220:223], v[52:55]\n"
      "buffer_load_dwordx4 v[116:119], v80, s[60:63], s76 offen offset:1024 sc1\n"
      "v_mfma_f32_16x16x32_bf16 v[52:55], v[128:131], a[236:239], v[52:55]\n"
      "v_mul_f32 v73, v73, v33\n"
      "v_mul_f32 v74, v74, v34\n"
      "v_mul_f32 v75, v75, v35\n"
      "v_mfma_f32_16x16x32_bf16 v[52:55], v[136:139], a[252:255], v[52:55]\n"
      "v_cvt_pk_bf16_f32 v76, v72, v73\n"
      "v_cvt_pk_bf16_f32 v77, v74, v75\n"
      "v_mfma_f32_16x16x32_bf16 v[48:51], v[128:131], a[232:235], v[48:51]\n"
      "ds_write_b64 v86, v[76:77] offset:34816\n"
      "v_mfma_f32_16x16x32_bf16 v[48:51], v[136:139], a[248:251], v[48:51]\n"
      "buffer_load_dwordx4 v[124:127], v80, s[60:63], s76 offen offset:2048 sc1\n"
      "v_mfma_f32_16x16x32_bf16 v[44:47], v[128:131], a[228:231], v[44:47]\n"
      "v_mul_f32 v36, v36, v95\n"
      "v_mul_f32 v37, v37, v95\n"
      "v_mul_f32 v38, v38, v95\n"
      "v_mfma_f32_16x16x32_bf16 v[44:47], v[136:139], a[244:247], v[44:47]\n"
      "v_mul_f32 v39, v39, v95\n"
      "v_exp_f32 v72, v36\n"
      "v_mfma_f32_16x16x32_bf16 v[40:43], v[128:131], a[224:227], v[40:43]\n"
      "s_waitcnt vmcnt(29)\n"
      "v_exp_f32 v73, v37\n"
      "v_mfma_f32_16x16x32_bf16 v[40:43], v[136:139], a[240:243], v[40:43]\n"
      "buffer_load_dwordx4 v[128:131], v80, s[60:63], s76 offen offset:3072 sc1\n"
      "v_mfma_f32_16x16x32_bf16 v[40:43], v[132:135], v[220:223], v[40:43]\n"
      "s_add_u32 s76, s77, 8192\n"
      "v_exp_f32 v74, v38\n"
      "v_mfma_f32_16x16x32_bf16 v[40:43], v[140:143], v[236:239], v[40:43]\n"
      "v_exp_f32 v75, v39\n"
      "v_add_f32 v72, 1.0, v72\n"
      "v_mfma_f32_16x16x32_bf16 v[44:47], v[132:135], v[224:227], v[44:47]\n"
      "v_add_f32 v73, 1.0, v73\n"
      "v_add_f32 v74, 1.0, v74\n"
      "v_add_f32 v75, 1.0, v75\n"
      "v_mfma_f32_16x16x32_bf16 v[44:47], v[140:143], v[240:243], v[44:47]\n"
      "buffer_load_dwordx4 v[136:139], v80, s[60:63], s76 offen offset:0 sc1\n"
      "v_mfma_f32_16x16x32_bf16 v[48:51], v[132:135], v[228:231], v[48:51]\n"
      "v_rcp_f32 v72, v72\n"
      "v_mfma_f32_16x16x32_bf16 v[48:51], v[140:143], v[244:247], v[48:51]\n"
      "v_rcp_f32 v73, v73\n"
      "v_mfma_f32_16x16x32_bf16 v[52:55], v[132:135], v[232:235], v[52:55]\n"
      "s_waitcnt vmcnt(30)\n"
      "v_rcp_f32 v74, v74\n"
      "v_mfma_f32_16x16x32_bf16 v[52:55], v[140:143], v[248:251], v[52:55]\n"
      "buffer_load_dwordx4 v[132:135], v80, s[60:63], s76 offen offset:1024 sc1\n"
      "v_mfma_f32_16x16x32_bf16 v[52:55], v[144:147], v[68:71], v[52:55]\n"
      "v_rcp_f32 v75, v75\n"
      "v_mul_f32 v72, v72, v36\n"
      "v_mfma_f32_16x16x32_bf16 v[48:51], v[144:147], v[64:67], v[48:51]\n"
      "v_mul_f32 v73, v73, v37\n"
      "v_mul_f32 v74, v74, v38\n"
      "v_mul_f32 v75, v75, v39\n"
      "v_mfma_f32_16x16x32_bf16 v[44:47], v[144:147], v[60:63], v[44:47]\n"
      "s_waitcnt vmcnt(29)\n"
      "v_cvt_pk_bf16_f32 v78, v72, v73\n"
      "v_cvt_pk_bf16_f32 v79, v74, v75\n"
      "v_mfma_f32_16x16x32_bf16 v[40:43], v[144:147], v[56:59], v[40:43]\n"
      "buffer_load_dwordx4 v[140:143], v80, s[60:63], s76 offen offset:2048 sc1\n"
      "v_mfma_f32_16x16x32_bf16 v[8:11], v[152:155], a[0:3], 0\n"
      "ds_write_b64 v86, v[78:79] offset:35840\n"
      "v_mfma_f32_16x16x32_bf16 v[8:11], v[148:151], a[16:19], v[8:11]\n"
      "v_mfma_f32_16x16x32_bf16 v[12:15], v[152:155], a[4:7], 0\n"
      "v_mfma_f32_16x16x32_bf16 v[12:15], v[148:151], a[20:23], v[12:15]\n"
      "buffer_load_dwordx4 v[144:147], v80, s[60:63], s76 offen offset:3072 sc1\n"
      "v_mfma_f32_16x16x32_bf16 v[16:19], v[152:155], a[8:11], 0\n"
      "s_mul_i32 s11, s10, 38\n"
      "s_add_u32 s76, s77, 12288\n"
      "v_mul_f32 v40, v40, v92\n"
      "v_mul_f32 v41, v41, v92\n"
      "v_mfma_f32_16x16x32_bf16 v[16:19], v[148:151], a[24:27], v[16:19]\n"
      "v_mul_f32 v42, v42, v92\n"
      "v_mul_f32 v43, v43, v92\n"
      "v_mfma_f32_16x16x32_bf16 v[20:23], v[152:155], a[12:15], 0\n"
      "s_waitcnt vmcnt(29)\n"
      "v_exp_f32 v72, v40\n"
      "v_mfma_f32_16x16x32_bf16 v[20:23], v[148:151], a[28:31], v[20:23]\n"
      "buffer_load_dwordx4 v[152:155], v80, s[60:63], s76 offen offset:0 sc1\n"
      "v_mfma_f32_16x16x32_bf16 v[20:23], v[156:159], a[44:47], v[20:23]\n"
      "s_add_u32 s11, s11, s79\n"
      "v_exp_f32 v73, v41\n"
      "v_mfma_f32_16x16x32_bf16 v[20:23], v[160:163], a[60:63], v[20:23]\n"
      "v_exp_f32 v74, v42\n"
      "v_mfma_f32_16x16x32_bf16 v[16:19], v[156:159], a[40:43], v[16:19]\n"
      "v_exp_f32 v75, v43\n"
      "v_add_f32 v72, 1.0, v72\n"
      "v_mfma_f32_16x16x32_bf16 v[16:19], v[160:163], a[56:59], v[16:19]\n"
      "buffer_load_dwordx4 v[148:151], v80, s[60:63], s76 offen offset:1024 sc1\n"
      "v_mfma_f32_16x16x32_bf16 v[12:15], v[156:159], a[36:39], v[12:15]\n"
      "v_add_f32 v73, 1.0, v73\n"
      "v_add_f32 v74, 1.0, v74\n"
      "v_add_f32 v75, 1.0, v75\n"
      "v_mfma_f32_16x16x32_bf16 v[12:15], v[160:163], a[52:55], v[12:15]\n"
      "v_rcp_f32 v72, v72\n"
      "v_mfma_f32_16x16x32_bf16 v[8:11], v[156:159], a[32:35], v[8:11]\n"
      "s_waitcnt vmcnt(29)\n"
      "v_rcp_f32 v73, v73\n"
      "v_mfma_f32_16x16x32_bf16 v[8:11], v[160:163], a[48:51], v[8:11]\n"
      "buffer_load_dwordx4 v[156:159], v80, s[60:63], s76 offen offset:2048 sc1\n"
      "v_mfma_f32_16x16x32_bf16 v[8:11], v[168:171], a[64:67], v[8:11]\n"
      "v_rcp_f32 v74, v74\n"
      "v_mfma_f32_16x16x32_bf16 v[8:11], v[164:167], a[80:83], v[8:11]\n"
      "v_rcp_f32 v75, v75\n"
      "v_mfma_f32_16x16x32_bf16 v[12:15], v[168:171], a[68:71], v[12:15]\n"
      "v_mul_f32 v72, v72, v40\n"
      "v_mul_f32 v73, v73, v41\n"
      "v_mul_f32 v74, v74, v42\n"
      "v_mfma_f32_16x16x32_bf16 v[12:15], v[164:167], a[84:87], v[12:15]\n"
      "buffer_load_dwordx4 v[160:163], v80, s[60:63], s76 offen offset:3072 sc1\n"
      "v_mfma_f32_16x16x32_bf16 v[16:19], v[168:171], a[72:75], v[16:19]\n"
      "s_add_u32 s76, s77, 16384\n"
      "v_mul_f32 v75, v75, v43\n"
      "v_cvt_pk_bf16_f32 v76, v72, v73\n"
      "v_cvt_pk_bf16_f32 v77, v74, v75\n"
      "v_mfma_f32_16x16x32_bf16 v[16:19], v[164:167], a[88:91], v[16:19]\n"
      "ds_write_b64 v86, v[76:77] offset:40960\n"
      "v_mfma_f32_16x16x32_bf16 v[20:23], v[168:171], a[76:79], v[20:23]\n"
      "s_waitcnt vmcnt(29)\n"
      "v_mul_f32 v44, v44, v93\n"
      "v_mul_f32 v45, v45, v93\n"
      "v_mul_f32 v46, v46, v93\n"
      "v_mfma_f32_16x16x32_bf16 v[20:23], v[164:167], a[92:95], v[20:23]\n"
      "buffer_load_dwordx4 v[168:171], v80, s[60:63], s76 offen offset:0 sc1\n"
      "v_mfma_f32_16x16x32_bf16 v[20:23], v[172:175], a[108:111], v[20:23]\n"
      "v_mul_f32 v47, v47, v93\n"
      "v_exp_f32 v72, v44\n"
      "v_mfma_f32_16x16x32_bf16 v[20:23], v[176:179], a[124:127], v[20:23]\n"
      "v_exp_f32 v73, v45\n"
      "v_mfma_f32_16x16x32_bf16 v[16:19], v[172:175], a[104:107], v[16:19]\n"
      "v_exp_f32 v74, v46\n"
      "v_mfma_f32_16x16x32_bf16 v[16:19], v[176:179], a[120:123], v[16:19]\n"
      "buffer_load_dwordx4 v[164:167], v80, s[60:63], s76 offen offset:1024 sc1\n"
      "v_mfma_f32_16x16x32_bf16 v[12:15], v[172:175], a[100:103], v[12:15]\n"
      "v_exp_f32 v75, v47\n"
      "v_add_f32 v72, 1.0, v72\n"
      "v_mfma_f32_16x16x32_bf16 v[12:15], v[176:179], a[116:119], v[12:15]\n"
      "v_add_f32 v73, 1.0, v73\n"
      "v_add_f32 v74, 1.0, v74\n"
      "v_mfma_f32_16x16x32_bf16 v[8:11], v[172:175], a[96:99], v[8:11]\n"
      "s_waitcnt vmcnt(29)\n"
      "v_add_f32 v75, 1.0, v75\n"
      "v_rcp_f32 v72, v72\n"
      "v_mfma_f32_16x16x32_bf16 v[8:11], v[176:179], a[112:115], v[8:11]\n"
      "buffer_load_dwordx4 v[172:175], v80, s[60:63], s76 offen offset:2048 sc1\n"
      "v_mfma_f32_16x16x32_bf16 v[8:11], v[184:187], a[128:131], v[8:11]\n"
      "s_mov_b32 s77, s11\n"
      "s_add_u32 s76, s77, 0\n"
      "v_rcp_f32 v73, v73\n"
      "v_mfma_f32_16x16x32_bf16 v[8:11], v[180:183], a[144:147], v[8:11]\n"
      "v_rcp_f32 v74, v74\n"
      "v_mfma_f32_16x16x32_bf16 v[12:15], v[184:187], a[132:135], v[12:15]\n"
      "v_rcp_f32 v75, v75\n"
      "v_mul_f32 v72, v72, v44\n"
      "v_mfma_f32_16x16x32_bf16 v[12:15], v[180:183], a[148:151], v[12:15]\n"
      "buffer_load_dwordx4 v[176:179], v80, s[60:63], s76 offen offset:0 sc1\n"
      "v_mfma_f32_16x16x32_bf16 v[16:19], v[184:187], a[136:139], v[16:19]\n"
      "v_mul_f32 v73, v73, v45\n"
      "v_mul_f32 v74, v74, v46\n"
      "v_mul_f32 v75, v75, v47\n"
      "v_mfma_f32_16x16x32_bf16 v[16:19], v[180:183], a[152:155], v[16:19]\n"
      "v_cvt_pk_bf16_f32 v78, v72, v73\n"
      "v_cvt_pk_bf16_f32 v79, v74, v75\n"
      "v_mfma_f32_16x16x32_bf16 v[20:23], v[184:187], a[140:143], v[20:23]\n"
      "s_waitcnt vmcnt(29)\n"
      "ds_write_b64 v86, v[78:79] offset:41984\n"
      "v_mfma_f32_16x16x32_bf16 v[20:23], v[180:183], a[156:159], v[20:23]\n"
      "buffer_load_dwordx4 v[184:187], v80, s[60:63], s76 offen offset:1024 sc1\n"
      "v_mfma_f32_16x16x32_bf16 v[20:23], v[188:191], a[172:175], v[20:23]\n"
      "v_mul_f32 v48, v48, v94\n"
      "v_mul_f32 v49, v49, v94\n"
      "v_mul_f32 v50, v50, v94\n"
      "v_mfma_f32_16x16x32_bf16 v[20:23], v[192:195], a[188:191], v[20:23]\n"
      "v_mul_f32 v51, v51, v94\n"
      "v_mfma_f32_16x16x32_bf16 v[16:19], v[188:191], a[168:171], v[16:19]\n"
      "v_exp_f32 v72, v48\n"
      "v_mfma_f32_16x16x32_bf16 v[16:19], v[192:195], a[184:187], v[16:19]\n"
      "buffer_load_dwordx4 v[180:183], v80, s[60:63], s76 offen offset:2048 sc1\n"
      "v_mfma_f32_16x16x32_bf16 v[12:15], v[188:191], a[164:167], v[12:15]\n"
      "v_exp_f32 v73, v49\n"
      "v_mfma_f32_16x16x32_bf16 v[12:15], v[192:195], a[180:183], v[12:15]\n"
      "v_exp_f32 v74, v50\n"
      "v_mfma_f32_16x16x32_bf16 v[8:11], v[188:191], a[160:163], v[8:11]\n"
      "s_waitcnt vmcnt(29)\n"
      "v_exp_f32 v75, v51\n"
      "v_add_f32 v72, 1.0, v72\n"
      "v_mfma_f32_16x16x32_bf16 v[8:11], v[192:195], a[176:179], v[8:11]\n"
      "buffer_load_dwordx4 v[188:191], v80, s[60:63], s76 offen offset:3072 sc1\n"
      "v_mfma_f32_16x16x32_bf16 v[8:11], v[200:203], a[192:195], v[8:11]\n"
      "s_add_u32 s76, s77, 4096\n"
      "v_add_f32 v73, 1.0, v73\n"
      "v_add_f32 v74, 1.0, v74\n"
      "v_add_f32 v75, 1.0, v75\n"
      "v_mfma_f32_16x16x32_bf16 v[8:11], v[196:199], a[208:211], v[8:11]\n"
      "v_rcp_f32 v72, v72\n"
      "v_mfma_f32_16x16x32_bf16 v[12:15], v[200:203], a[196:199], v[12:15]\n"
      "v_rcp_f32 v73, v73\n"
      "v_mfma_f32_16x16x32_bf16 v[12:15], v[196:199], a[212:215], v[12:15]\n"
      "buffer_load_dwordx4 v[192:195], v80, s[60:63], s76 offen offset:0 sc1\n"
      "v_mfma_f32_16x16x32_bf16 v[16:19], v[200:203], a[200:203], v[16:19]\n"
      "v_rcp_f32 v74, v74\n"
      "v_mfma_f32_16x16x32_bf16 v[16:19], v[196:199], a[216:219], v[16:19]\n"
      "v_rcp_f32 v75, v75\n"
      "v_mfma_f32_16x16x32_bf16 v[20:23], v[200:203], a[204:207], v[20:23]\n"
      "s_waitcnt vmcnt(29)\n"
      "v_mul_f32 v72, v72, v48\n"
      "v_mul_f32 v73, v73, v49\n"
      "v_mul_f32 v74, v74, v50\n"
      "v_mfma_f32_16x16x32_bf16 v[20:23], v[196:199], a[220:223], v[20:23]\n"
      "buffer_load_dwordx4 v[200:203], v80, s[60:63], s76 offen offset:1024 sc1\n"
      "v_mfma_f32_16x16x32_bf16 v[20:23], v[204:207], a[236:239], v[20:23]\n"
      "v_mul_f32 v75, v75, v51\n"
      "v_cvt_pk_bf16_f32 v76, v72, v73\n"
      "v_cvt_pk_bf16_f32 v77, v74, v75\n"
      "v_mfma_f32_16x16x32_bf16 v[20:23], v[208:211], a[252:255], v[20:23]\n"
      "ds_write_b64 v86, v[76:77] offset:43008\n"
      "v_mfma_f32_16x16x32_bf16 v[16:19], v[204:207], a[232:235], v[16:19]\n"
      "v_mul_f32 v52, v52, v95\n"
      "v_mul_f32 v53, v53, v95\n"
      "v_mul_f32 v54, v54, v95\n"
      "v_mfma_f32_16x16x32_bf16 v[16:19], v[208:211], a[248:251], v[16:19]\n"
      "buffer_load_dwordx4 v[196:199], v80, s[60:63], s76 offen offset:2048 sc1\n"
      "v_mfma_f32_16x16x32_bf16 v[12:15], v[204:207], a[228:231], v[12:15]\n"
      "v_mul_f32 v55, v55, v95\n"
      "v_exp_f32 v72, v52\n"
      "v_mfma_f32_16x16x32_bf16 v[12:15], v[208:211], a[244:247], v[12:15]\n"
      "v_exp_f32 v73, v53\n"
      "v_mfma_f32_16x16x32_bf16 v[8:11], v[204:207], a[224:227], v[8:11]\n"
      "s_waitcnt vmcnt(28)\n"
      "v_exp_f32 v74, v54\n"
      "v_mfma_f32_16x16x32_bf16 v[8:11], v[208:211], a[240:243], v[8:11]\n"
      "buffer_load_dwordx4 v[204:207], v80, s[60:63], s76 offen offset:3072 sc1\n"
      "v_mfma_f32_16x16x32_bf16 v[8:11], v[212:215], v[220:223], v[8:11]\n"
      "s_add_u32 s76, s77, 8192\n"
      "v_exp_f32 v75, v55\n"
      "v_add_f32 v72, 1.0, v72\n"
      "v_mfma_f32_16x16x32_bf16 v[8:11], v[216:219], v[236:239], v[8:11]\n"
      "v_add_f32 v73, 1.0, v73\n"
      "v_add_f32 v74, 1.0, v74\n"
      "v_mfma_f32_16x16x32_bf16 v[12:15], v[212:215], v[224:227], v[12:15]\n"
      "v_add_f32 v75, 1.0, v75\n"
      "v_rcp_f32 v72, v72\n"
      "v_mfma_f32_16x16x32_bf16 v[12:15], v[216:219], v[240:243], v[12:15]\n"
      "buffer_load_dwordx4 v[208:211], v80, s[60:63], s76 offen offset:0 sc1\n"
      "v_mfma_f32_16x16x32_bf16 v[16:19], v[212:215], v[228:231], v[16:19]\n"
      "v_rcp_f32 v73, v73\n"
      "v_mfma_f32_16x16x32_bf16 v[16:19], v[216:219], v[244:247], v[16:19]\n"
      "v_rcp_f32 v74, v74\n"
      "v_mfma_f32_16x16x32_bf16 v[20:23], v[212:215], v[232:235], v[20:23]\n"
      "s_waitcnt vmcnt(29)\n"
      "v_rcp_f32 v75, v75\n"
      "v_mul_f32 v72, v72, v52\n"
      "v_mfma_f32_16x16x32_bf16 v[20:23], v[216:219], v[248:251], v[20:23]\n"
      "buffer_load_dwordx4 v[212:215], v80, s[60:63], s76 offen offset:1024 sc1\n"
      "v_mfma_f32_16x16x32_bf16 v[20:23], v[96:99], v[68:71], v[20:23]\n"
      "v_mul_f32 v73, v73, v53\n"
      "v_mul_f32 v74, v74, v54\n"
      "v_mul_f32 v75, v75, v55\n"
      "v_mfma_f32_16x16x32_bf16 v[16:19], v[96:99], v[64:67], v[16:19]\n"
      "v_cvt_pk_bf16_f32 v78, v72, v73\n"
      "v_cvt_pk_bf16_f32 v79, v74, v75\n"
      "v_mfma_f32_16x16x32_bf16 v[12:15], v[96:99], v[60:63], v[12:15]\n"
      "s_waitcnt vmcnt(27)\n"
      "ds_write_b64 v86, v[78:79] offset:44032\n"
      "v_mfma_f32_16x16x32_bf16 v[8:11], v[96:99], v[56:59], v[8:11]\n"
      "buffer_load_dwordx4 v[216:219], v80, s[60:63], s76 offen offset:2048 sc1\n"
      "v_mfma_f32_16x16x32_bf16 v[24:27], v[104:107], a[0:3], 0\n"
      "v_mfma_f32_16x16x32_bf16 v[24:27], v[100:103], a[16:19], v[24:27]\n"
      "v_mfma_f32_16x16x32_bf16 v[28:31], v[104:107], a[4:7], 0\n"
      "v_mfma_f32_16x16x32_bf16 v[28:31], v[100:103], a[20:23], v[28:31]\n"
      "buffer_load_dwordx4 v[96:99], v80, s[60:63], s76 offen offset:3072 sc1\n"
      "v_mfma_f32_16x16x32_bf16 v[32:35], v[104:107], a[8:11], 0\n"
      "s_add_u32 s11, s79, 1171456\n"
      "s_add_u32 s76, s77, 12288\n"
      "v_mul_f32 v8, v8, v92\n"
      "v_mul_f32 v9, v9, v92\n"
      "v_mfma_f32_16x16x32_bf16 v[32:35], v[100:103], a[24:27], v[32:35]\n"
      "v_mul_f32 v10, v10, v92\n"
      "v_mul_f32 v11, v11, v92\n"
      "v_mfma_f32_16x16x32_bf16 v[36:39], v[104:107], a[12:15], 0\n"
      "s_waitcnt vmcnt(27)\n"
      "v_exp_f32 v72, v8\n"
      "v_mfma_f32_16x16x32_bf16 v[36:39], v[100:103], a[28:31], v[36:39]\n"
      "buffer_load_dwordx4 v[104:107], v80, s[60:63], s76 offen offset:0 sc1\n"
      "v_mfma_f32_16x16x32_bf16 v[36:39], v[108:111], a[44:47], v[36:39]\n"
      "v_exp_f32 v73, v9\n"
      "v_mfma_f32_16x16x32_bf16 v[36:39], v[112:115], a[60:63], v[36:39]\n"
      "v_exp_f32 v74, v10\n"
      "v_mfma_f32_16x16x32_bf16 v[32:35], v[108:111], a[40:43], v[32:35]\n"
      "v_exp_f32 v75, v11\n"
      "v_add_f32 v72, 1.0, v72\n"
      "v_mfma_f32_16x16x32_bf16 v[32:35], v[112:115], a[56:59], v[32:35]\n"
      "buffer_load_dwordx4 v[100:103], v80, s[60:63], s76 offen offset:1024 sc1\n"
      "v_mfma_f32_16x16x32_bf16 v[28:31], v[108:111], a[36:39], v[28:31]\n"
      "v_add_f32 v73, 1.0, v73\n"
      "v_add_f32 v74, 1.0, v74\n"
      "v_add_f32 v75, 1.0, v75\n"
      "v_mfma_f32_16x16x32_bf16 v[28:31], v[112:115], a[52:55], v[28:31]\n"
      "v_rcp_f32 v72, v72\n"
      "v_mfma_f32_16x16x32_bf16 v[24:27], v[108:111], a[32:35], v[24:27]\n"
      "s_waitcnt vmcnt(27)\n"
      "v_rcp_f32 v73, v73\n"
      "v_mfma_f32_16x16x32_bf16 v[24:27], v[112:115], a[48:51], v[24:27]\n"
      "buffer_load_dwordx4 v[108:111], v80, s[60:63], s76 offen offset:2048 sc1\n"
      "v_mfma_f32_16x16x32_bf16 v[24:27], v[120:123], a[64:67], v[24:27]\n"
      "v_rcp_f32 v74, v74\n"
      "v_mfma_f32_16x16x32_bf16 v[24:27], v[116:119], a[80:83], v[24:27]\n"
      "v_rcp_f32 v75, v75\n"
      "v_mfma_f32_16x16x32_bf16 v[28:31], v[120:123], a[68:71], v[28:31]\n"
      "v_mul_f32 v72, v72, v8\n"
      "v_mul_f32 v73, v73, v9\n"
      "v_mul_f32 v74, v74, v10\n"
      "v_mfma_f32_16x16x32_bf16 v[28:31], v[116:119], a[84:87], v[28:31]\n"
      "buffer_load_dwordx4 v[112:115], v80, s[60:63], s76 offen offset:3072 sc1\n"
      "v_mfma_f32_16x16x32_bf16 v[32:35], v[120:123], a[72:75], v[32:35]\n"
      "s_add_u32 s76, s77, 16384\n"
      "v_mul_f32 v75, v75, v11\n"
      "v_cvt_pk_bf16_f32 v76, v72, v73\n"
      "v_cvt_pk_bf16_f32 v77, v74, v75\n"
      "v_mfma_f32_16x16x32_bf16 v[32:35], v[116:119], a[88:91], v[32:35]\n"
      "ds_write_b64 v86, v[76:77] offset:49152\n"
      "v_mfma_f32_16x16x32_bf16 v[36:39], v[120:123], a[76:79], v[36:39]\n"
      "s_waitcnt vmcnt(27)\n"
      "v_mul_f32 v12, v12, v93\n"
      "v_mul_f32 v13, v13, v93\n"
      "v_mul_f32 v14, v14, v93\n"
      "v_mfma_f32_16x16x32_bf16 v[36:39], v[116:119], a[92:95], v[36:39]\n"
      "buffer_load_dwordx4 v[120:123], v80, s[60:63], s76 offen offset:0 sc1\n"
      "v_mfma_f32_16x16x32_bf16 v[36:39], v[124:127], a[108:111], v[36:39]\n"
      "v_mul_f32 v15, v15, v93\n"
      "v_exp_f32 v72, v12\n"
      "v_mfma_f32_16x16x32_bf16 v[36:39], v[128:131], a[124:127], v[36:39]\n"
      "v_exp_f32 v73, v13\n"
      "v_mfma_f32_16x16x32_bf16 v[32:35], v[124:127], a[104:107], v[32:35]\n"
      "v_exp_f32 v74, v14\n"
      "v_mfma_f32_16x16x32_bf16 v[32:35], v[128:131], a[120:123], v[32:35]\n"
      "buffer_load_dwordx4 v[116:119], v80, s[60:63], s76 offen offset:1024 sc1\n"
      "v_mfma_f32_16x16x32_bf16 v[28:31], v[124:127], a[100:103], v[28:31]\n"
      "v_exp_f32 v75, v15\n"
      "v_add_f32 v72, 1.0, v72\n"
      "v_mfma_f32_16x16x32_bf16 v[28:31], v[128:131], a[116:119], v[28:31]\n"
      "v_add_f32 v73, 1.0, v73\n"
      "v_add_f32 v74, 1.0, v74\n"
      "v_mfma_f32_16x16x32_bf16 v[24:27], v[124:127], a[96:99], v[24:27]\n"
      "s_waitcnt vmcnt(27)\n"
      "v_add_f32 v75, 1.0, v75\n"
      "v_rcp_f32 v72, v72\n"
      "v_mfma_f32_16x16x32_bf16 v[24:27], v[128:131], a[112:115], v[24:27]\n"
      "buffer_load_dwordx4 v[124:127], v80, s[60:63], s76 offen offset:2048 sc1\n"
      "v_mfma_f32_16x16x32_bf16 v[24:27], v[136:139], a[128:131], v[24:27]\n"
      "s_mov_b32 s77, s11\n"
      "s_sub_u32 s76, s77, 4096\n"
      "v_rcp_f32 v73, v73\n"
      "v_mfma_f32_16x16x32_bf16 v[24:27], v[132:135], a[144:147], v[24:27]\n"
      "v_rcp_f32 v74, v74\n"
      "v_mfma_f32_16x16x32_bf16 v[28:31], v[136:139], a[132:135], v[28:31]\n"
      "v_rcp_f32 v75, v75\n"
      "v_mul_f32 v72, v72, v12\n"
      "v_mfma_f32_16x16x32_bf16 v[28:31], v[132:135], a[148:151], v[28:31]\n"
      "buffer_load_dwordx4 v[128:131], v80, s[60:63], s76 offen offset:0 sc1\n"
      "v_mfma_f32_16x16x32_bf16 v[32:35], v[136:139], a[136:139], v[32:35]\n"
      "v_mul_f32 v73, v73, v13\n"
      "v_mul_f32 v74, v74, v14\n"
      "v_mul_f32 v75, v75, v15\n"
      "v_mfma_f32_16x16x32_bf16 v[32:35], v[132:135], a[152:155], v[32:35]\n"
      "v_cvt_pk_bf16_f32 v78, v72, v73\n"
      "v_cvt_pk_bf16_f32 v79, v74, v75\n"
      "v_mfma_f32_16x16x32_bf16 v[36:39], v[136:139], a[140:143], v[36:39]\n"
      "s_waitcnt vmcnt(27)\n"
      "ds_write_b64 v86, v[78:79] offset:50176\n"
      "v_mfma_f32_16x16x32_bf16 v[36:39], v[132:135], a[156:159], v[36:39]\n"
      "buffer_load_dwordx4 v[136:139], v80, s[60:63], s76 offen offset:1024 sc1\n"
      "v_mfma_f32_16x16x32_bf16 v[36:39], v[140:143], a[172:175], v[36:39]\n"
      "v_mul_f32 v16, v16, v94\n"
      "v_mul_f32 v17, v17, v94\n"
      "v_mul_f32 v18, v18, v94\n"
      "v_mfma_f32_16x16x32_bf16 v[36:39], v[144:147], a[188:191], v[36:39]\n"
      "v_mul_f32 v19, v19, v94\n"
      "v_mfma_f32_16x16x32_bf16 v[32:35], v[140:143], a[168:171], v[32:35]\n"
      "v_exp_f32 v72, v16\n"
      "v_mfma_f32_16x16x32_bf16 v[32:35], v[144:147], a[184:187], v[32:35]\n"
      "buffer_load_dwordx4 v[132:135], v80, s[60:63], s76 offen offset:2048 sc1\n"
      "v_mfma_f32_16x16x32_bf16 v[28:31], v[140:143], a[164:167], v[28:31]\n"
      "v_exp_f32 v73, v17\n"
      "v_mfma_f32_16x16x32_bf16 v[28:31], v[144:147], a[180:183], v[28:31]\n"
      "v_exp_f32 v74, v18\n"
      "v_mfma_f32_16x16x32_bf16 v[24:27], v[140:143], a[160:163], v[24:27]\n"
      "s_waitcnt vmcnt(27)\n"
      "v_exp_f32 v75, v19\n"
      "v_add_f32 v72, 1.0, v72\n"
      "v_mfma_f32_16x16x32_bf16 v[24:27], v[144:147], a[176:179], v[24:27]\n"
      "buffer_load_dwordx4 v[140:143], v80, s[60:63], s76 offen offset:3072 sc1\n"
      "v_mfma_f32_16x16x32_bf16 v[24:27], v[152:155], a[192:195], v[24:27]\n"
      "s_add_u32 s76, s77, s4\n"
      "v_add_f32 v73, 1.0, v73\n"
      "v_add_f32 v74, 1.0, v74\n"
      "v_add_f32 v75, 1.0, v75\n"
      "v_mfma_f32_16x16x32_bf16 v[24:27], v[148:151], a[208:211], v[24:27]\n"
      "v_rcp_f32 v72, v72\n"
      "v_mfma_f32_16x16x32_bf16 v[28:31], v[152:155], a[196:199], v[28:31]\n"
      "v_rcp_f32 v73, v73\n"
      "v_mfma_f32_16x16x32_bf16 v[28:31], v[148:151], a[212:215], v[28:31]\n"
      "buffer_load_dwordx4 v[144:147], v80, s[60:63], s76 offen offset:0 sc1\n"
      "v_mfma_f32_16x16x32_bf16 v[32:35], v[152:155], a[200:203], v[32:35]\n"
      "v_rcp_f32 v74, v74\n"
      "v_mfma_f32_16x16x32_bf16 v[32:35], v[148:151], a[216:219], v[32:35]\n"
      "v_rcp_f32 v75, v75\n"
      "v_mfma_f32_16x16x32_bf16 v[36:39], v[152:155], a[204:207], v[36:39]\n"
      "s_waitcnt vmcnt(27)\n"
      "v_mul_f32 v72, v72, v16\n"
      "v_mul_f32 v73, v73, v17\n"
      "v_mul_f32 v74, v74, v18\n"
      "v_mfma_f32_16x16x32_bf16 v[36:39], v[148:151], a[220:223], v[36:39]\n"
      "buffer_load_dwordx4 v[152:155], v80, s[60:63], s76 offen offset:1024 sc1\n"
      "v_mfma_f32_16x16x32_bf16 v[36:39], v[156:159], a[236:239], v[36:39]\n"
      "s_add_u32 s76, s77, s5\n"
      "v_mul_f32 v75, v75, v19\n"
      "v_cvt_pk_bf16_f32 v76, v72, v73\n"
      "v_cvt_pk_bf16_f32 v77, v74, v75\n"
      "v_mfma_f32_16x16x32_bf16 v[36:39], v[160:163], a[252:255], v[36:39]\n"
      "ds_write_b64 v86, v[76:77] offset:51200\n"
      "v_mfma_f32_16x16x32_bf16 v[32:35], v[156:159], a[232:235], v[32:35]\n"
      "v_mul_f32 v20, v20, v95\n"
      "v_mul_f32 v21, v21, v95\n"
      "v_mul_f32 v22, v22, v95\n"
      "v_mfma_f32_16x16x32_bf16 v[32:35], v[160:163], a[248:251], v[32:35]\n"
      "buffer_load_dwordx4 v[148:151], v80, s[60:63], s76 offen offset:0 sc1\n"
      "v_mfma_f32_16x16x32_bf16 v[28:31], v[156:159], a[228:231], v[28:31]\n"
      "v_mul_f32 v23, v23, v95\n"
      "v_exp_f32 v72, v20\n"
      "v_mfma_f32_16x16x32_bf16 v[28:31], v[160:163], a[244:247], v[28:31]\n"
      "v_exp_f32 v73, v21\n"
      "v_mfma_f32_16x16x32_bf16 v[24:27], v[156:159], a[224:227], v[24:27]\n"
      "s_waitcnt vmcnt(27)\n"
      "v_exp_f32 v74, v22\n"
      "v_mfma_f32_16x16x32_bf16 v[24:27], v[160:163], a[240:243], v[24:27]\n"
      "buffer_load_dwordx4 v[156:159], v80, s[60:63], s76 offen offset:1024 sc1\n"
      "v_mfma_f32_16x16x32_bf16 v[24:27], v[168:171], v[220:223], v[24:27]\n"
      "s_add_u32 s76, s77, s6\n"
      "v_exp_f32 v75, v23\n"
      "v_add_f32 v72, 1.0, v72\n"
      "v_mfma_f32_16x16x32_bf16 v[24:27], v[164:167], v[236:239], v[24:27]\n"
      "v_add_f32 v73, 1.0, v73\n"
      "v_add_f32 v74, 1.0, v74\n"
      "v_mfma_f32_16x16x32_bf16 v[28:31], v[168:171], v[224:227], v[28:31]\n"
      "v_add_f32 v75, 1.0, v75\n"
      "v_rcp_f32 v72, v72\n"
      "v_mfma_f32_16x16x32_bf16 v[28:31], v[164:167], v[240:243], v[28:31]\n"
      "buffer_load_dwordx4 v[160:163], v80, s[60:63], s76 offen offset:0 sc1\n"
      "v_mfma_f32_16x16x32_bf16 v[32:35], v[168:171], v[228:231], v[32:35]\n"
      "v_rcp_f32 v73, v73\n"
      "v_mfma_f32_16x16x32_bf16 v[32:35], v[164:167], v[244:247], v[32:35]\n"
      "v_rcp_f32 v74, v74\n"
      "v_mfma_f32_16x16x32_bf16 v[36:39], v[168:171], v[232:235], v[36:39]\n"
      "s_waitcnt vmcnt(28)\n"
      "v_rcp_f32 v75, v75\n"
      "v_mul_f32 v72, v72, v20\n"
      "v_mfma_f32_16x16x32_bf16 v[36:39], v[164:167], v[248:251], v[36:39]\n"
      "buffer_load_dwordx4 v[168:171], v80, s[60:63], s76 offen offset:1024 sc1\n"
      "v_mfma_f32_16x16x32_bf16 v[36:39], v[172:175], v[68:71], v[36:39]\n"
      "s_add_u32 s76, s77, s7\n"
      "v_mul_f32 v73, v73, v21\n"
      "v_mul_f32 v74, v74, v22\n"
      "v_mul_f32 v75, v75, v23\n"
      "v_mfma_f32_16x16x32_bf16 v[32:35], v[172:175], v[64:67], v[32:35]\n"
      "v_cvt_pk_bf16_f32 v78, v72, v73\n"
      "v_cvt_pk_bf16_f32 v79, v74, v75\n"
      "v_mfma_f32_16x16x32_bf16 v[28:31], v[172:175], v[60:63], v[28:31]\n"
      "s_waitcnt vmcnt(27)\n"
      "ds_write_b64 v86, v[78:79] offset:52224\n"
      "v_mfma_f32_16x16x32_bf16 v[24:27], v[172:175], v[56:59], v[24:27]\n"
      "buffer_load_dwordx4 v[164:167], v80, s[60:63], s76 offen offset:0 sc1\n"
      "v_mfma_f32_16x16x32_bf16 v[40:43], v[176:179], a[0:3], 0\n"
      "s_lshr_b32 s11, s4, 11\n"
      "v_mfma_f32_16x16x32_bf16 v[40:43], v[184:187], a[16:19], v[40:43]\n"
      "v_mfma_f32_16x16x32_bf16 v[44:47], v[176:179], a[4:7], 0\n"
      "v_mfma_f32_16x16x32_bf16 v[44:47], v[184:187], a[20:23], v[44:47]\n"
      "buffer_load_dwordx4 v[172:175], v80, s[60:63], s76 offen offset:1024 sc1\n"
      "s_nop 0\n"
      "v_mul_f32 v24, v24, v92\n"
      "s_waitcnt lgkmcnt(0)\n"
      "s_barrier\n"
      "v_mfma_f32_16x16x32_bf16 v[48:51], v[176:179], a[8:11], 0\n"
      "s_add_u32 s11, s11, 1\n"
      "s_add_u32 s76, s77, s8\n"
      "s_add_u32 m0, s13, 10240\n"
      "v_mul_f32 v25, v25, v92\n"
      "v_mul_f32 v26, v26, v92\n"
      "v_mfma_f32_16x16x32_bf16 v[48:51], v[184:187], a[24:27], v[48:51]\n"
      "s_add_u32 s14, s12, 10240\n"
      "v_mul_f32 v27, v27, v92\n"
      "v_exp_f32 v72, v24\n"
      "v_mfma_f32_16x16x32_bf16 v[52:55], v[176:179], a[12:15], 0\n"
      "buffer_load_dwordx4 v80, s[60:63], s14 offen sc1 lds\n"
      "v_exp_f32 v73, v25\n"
      "v_mfma_f32_16x16x32_bf16 v[52:55], v[184:187], a[28:31], v[52:55]\n"
      "buffer_load_dwordx4 v[176:179], v80, s[60:63], s76 offen offset:0 sc1\n"
      "s_waitcnt vmcnt(30)\n"
      "v_mfma_f32_16x16x32_bf16 v[52:55], v[180:183], a[44:47], v[52:55]\n"
      "s_add_u32 s11, s34, s11\n"
      "ds_read_b128 a[0:3], v80 offset:0\n"
      "ds_read_b128 a[4:7], v80 offset:1024\n"
      "v_exp_f32 v74, v26\n"
      "s_waitcnt vmcnt(29)\n"
      "v_mfma_f32_16x16x32_bf16 v[52:55], v[188:191], a[60:63], v[52:55]\n"
      "ds_read_b128 a[8:11], v80 offset:2048\n"
      "ds_read_b128 a[12:15], v80 offset:3072\n"
      "v_mfma_f32_16x16x32_bf16 v[48:51], v[180:183], a[40:43], v[48:51]\n"
      "v_exp_f32 v75, v27\n"
      "v_add_f32 v72, 1.0, v72\n"
      "v_add_f32 v73, 1.0, v73\n"
      "v_mfma_f32_16x16x32_bf16 v[48:51], v[188:191], a[56:59], v[48:51]\n"
      "buffer_load_dwordx4 v[184:187], v80, s[60:63], s76 offen offset:1024 sc1\n"
      "v_add_f32 v74, 1.0, v74\n"
      "v_mfma_f32_16x16x32_bf16 v[44:47], v[180:183], a[36:39], v[44:47]\n"
      "s_and_b32 s11, s11, 7\n"
      "s_add_u32 s76, s77, s9\n"
      "s_add_u32 m0, s13, 11264\n"
      "v_add_f32 v75, 1.0, v75\n"
      "v_mfma_f32_16x16x32_bf16 v[44:47], v[188:191], a[52:55], v[44:47]\n"
      "s_add_u32 s14, s12, 11264\n"
      "ds_read_b128 a[16:19], v80 offset:4096\n"
      "ds_read_b128 a[20:23], v80 offset:5120\n"
      "ds_read_b128 a[24:27], v80 offset:6144\n"
      "v_mfma_f32_16x16x32_bf16 v[40:43], v[180:183], a[32:35], v[40:43]\n"
      "buffer_load_dwordx4 v80, s[60:63], s14 offen sc1 lds\n"
      "v_rcp_f32 v72, v72\n"
      "v_mfma_f32_16x16x32_bf16 v[40:43], v[188:191], a[48:51], v[40:43]\n"
      "buffer_load_dwordx4 v[180:183], v80, s[60:63], s76 offen offset:0 sc1\n"
      "ds_read_b128 a[28:31], v80 offset:7168\n"
      "s_waitcnt vmcnt(31)\n"
      "v_mfma_f32_16x16x32_bf16 v[40:43], v[192:195], a[64:67], v[40:43]\n"
      "ds_read_b128 a[32:35], v80 offset:8192\n"
      "ds_read_b128 a[36:39], v80 offset:9216\n"
      "v_rcp_f32 v73, v73\n"
      "s_waitcnt vmcnt(30)\n"
      "v_mfma_f32_16x16x32_bf16 v[40:43], v[200:203], a[80:83], v[40:43]\n"
      "ds_read_b128 a[40:43], v80 offset:10240\n"
      "ds_read_b128 a[44:47], v80 offset:11264\n"
      "v_mfma_f32_16x16x32_bf16 v[44:47], v[192:195], a[68:71], v[44:47]\n"
      "v_rcp_f32 v74, v74\n"
      "v_rcp_f32 v75, v75\n"
      "v_mfma_f32_16x16x32_bf16 v[44:47], v[200:203], a[84:87], v[44:47]\n"
      "v_mul_f32 v72, v72, v24\n"
      "v_mul_f32 v73, v73, v25\n"
      "v_mul_f32 v74, v74, v26\n"
      "v_mfma_f32_16x16x32_bf16 v[48:51], v[192:195], a[72:75], v[48:51]\n"
      "s_add_u32 m0, s13, 12288\n"
      "ds_read_b128 a[48:51], v80 offset:12288\n"
      "ds_read_b128 a[52:55], v80 offset:13312\n"
      "v_mul_f32 v75, v75, v27\n"
      "v_cvt_pk_bf16_f32 v76, v72, v73\n"
      "v_mfma_f32_16x16x32_bf16 v[48:51], v[200:203], a[88:91], v[48:51]\n"
      "s_add_u32 s14, s12, 12288\n"
      "ds_read_b128 a[56:59], v80 offset:14336\n"
      "ds_read_b128 a[60:63], v80 offset:15360\n"
      "v_cvt_pk_bf16_f32 v77, v74, v75\n"
      "v_mfma_f32_16x16x32_bf16 v[52:55], v[192:195], a[76:79], v[52:55]\n"
      "s_waitcnt vmcnt(28)\n"
      "ds_write_b64 v86, v[76:77] offset:57344\n"
      "v_mul_f32 v28, v28, v93\n"
      "v_mfma_f32_16x16x32_bf16 v[52:55], v[200:203], a[92:95], v[52:55]\n"
      "buffer_load_dwordx4 v80, s[60:63], s14 offen sc1 lds\n"
      "v_mul_f32 v29, v29, v93\n"
      "v_mfma_f32_16x16x32_bf16 v[52:55], v[196:199], a[108:111], v[52:55]\n"
      "ds_read_b128 a[64:67], v80 offset:16384\n"
      "ds_read_b128 a[68:71], v80 offset:17408\n"
      "v_mul_f32 v30, v30, v93\n"
      "v_mul_f32 v31, v31, v93\n"
      "v_mfma_f32_16x16x32_bf16 v[52:55], v[204:207], a[124:127], v[52:55]\n"
      "ds_read_b128 a[72:75], v80 offset:18432\n"
      "ds_read_b128 a[76:79], v80 offset:19456\n"
      "v_mfma_f32_16x16x32_bf16 v[48:51], v[196:199], a[104:107], v[48:51]\n"
      "v_exp_f32 v72, v28\n"
      "v_exp_f32 v73, v29\n"
      "v_mfma_f32_16x16x32_bf16 v[48:51], v[204:207], a[120:123], v[48:51]\n"
      "v_exp_f32 v74, v30\n"
      "v_mfma_f32_16x16x32_bf16 v[44:47], v[196:199], a[100:103], v[44:47]\n"
      "s_add_u32 m0, s13, 13312\n"
      "ds_read_b128 a[80:83], v80 offset:20480\n"
      "ds_read_b128 a[84:87], v80 offset:21504\n"
      "v_exp_f32 v75, v31\n"
      "v_mfma_f32_16x16x32_bf16 v[44:47], v[204:207], a[116:119], v[44:47]\n"
      "ds_read_b128 a[88:91], v80 offset:22528\n"
      "ds_read_b128 a[92:95], v80 offset:23552\n"
      "v_add_f32 v72, 1.0, v72\n"
      "v_mfma_f32_16x16x32_bf16 v[40:43], v[196:199], a[96:99], v[40:43]\n"
      "s_add_u32 s14, s12, 13312\n"
      "v_add_f32 v73, 1.0, v73\n"
      "v_add_f32 v74, 1.0, v74\n"
      "v_add_f32 v75, 1.0, v75\n"
      "v_mfma_f32_16x16x32_bf16 v[40:43], v[204:207], a[112:115], v[40:43]\n"
      "s_waitcnt vmcnt(27)\n"
      "v_rcp_f32 v72, v72\n"
      "v_mfma_f32_16x16x32_bf16 v[40:43], v[208:211], a[128:131], v[40:43]\n"
      "buffer_load_dwordx4 v80, s[60:63], s14 offen sc1 lds\n"
      "v_rcp_f32 v73, v73\n"
      "v_mfma_f32_16x16x32_bf16 v[40:43], v[212:215], a[144:147], v[40:43]\n"
      "ds_read_b128 a[96:99], v80 offset:24576\n"
      "ds_read_b128 a[100:103], v80 offset:25600\n"
      "v_mfma_f32_16x16x32_bf16 v[44:47], v[208:211], a[132:135], v[44:47]\n"
      "ds_read_b128 a[104:107], v80 offset:26624\n"
      "ds_read_b128 a[108:111], v80 offset:27648\n"
      "v_rcp_f32 v74, v74\n"
      "v_mfma_f32_16x16x32_bf16 v[44:47], v[212:215], a[148:151], v[44:47]\n"
      "v_rcp_f32 v75, v75\n"
      "v_mul_f32 v72, v72, v28\n"
      "v_mfma_f32_16x16x32_bf16 v[48:51], v[208:211], a[136:139], v[48:51]\n"
      "ds_read_b128 a[112:115], v80 offset:28672\n"
      "ds_read_b128 a[116:119], v80 offset:29696\n"
      "v_mul_f32 v73, v73, v29\n"
      "v_mul_f32 v74, v74, v30\n"
      "v_mfma_f32_16x16x32_bf16 v[48:51], v[212:215], a[152:155], v[48:51]\n"
      "s_add_u32 m0, s13, 14336\n"
      "ds_read_b128 a[120:123], v80 offset:30720\n"
      "ds_read_b128 a[124:127], v80 offset:31744\n"
      "v_mul_f32 v75, v75, v31\n"
      "v_mfma_f32_16x16x32_bf16 v[52:55], v[208:211], a[140:143], v[52:55]\n"
      "s_add_u32 s14, s12, 14336\n"
      "v_cvt_pk_bf16_f32 v78, v72, v73\n"
      "v_cvt_pk_bf16_f32 v79, v74, v75\n"
      "v_mfma_f32_16x16x32_bf16 v[52:55], v[212:215], a[156:159], v[52:55]\n"
      "s_waitcnt vmcnt(26)\n"
      "ds_write_b64 v86, v[78:79] offset:58368\n"
      "v_mfma_f32_16x16x32_bf16 v[52:55], v[216:219], a[172:175], v[52:55]\n"
      "buffer_load_dwordx4 v80, s[60:63], s14 offen sc1 lds\n"
      "v_mul_f32 v32, v32, v94\n"
      "v_mul_f32 v33, v33, v94\n"
      "v_mfma_f32_16x16x32_bf16 v[52:55], v[96:99], a[188:191], v[52:55]\n"
      "ds_read_b128 a[128:131], v80 offset:32768\n"
      "ds_read_b128 a[132:135], v80 offset:33792\n"
      "v_mul_f32 v34, v34, v94\n"
      "v_mfma_f32_16x16x32_bf16 v[48:51], v[216:219], a[168:171], v[48:51]\n"
      "ds_read_b128 a[136:139], v80 offset:34816\n"
      "ds_read_b128 a[140:143], v80 offset:35840\n"
      "v_mul_f32 v35, v35, v94\n"
      "v_mfma_f32_16x16x32_bf16 v[48:51], v[96:99], a[184:187], v[48:51]\n"
      "v_exp_f32 v72, v32\n"
      "v_mfma_f32_16x16x32_bf16 v[44:47], v[216:219], a[164:167], v[44:47]\n"
      "ds_read_b128 a[144:147], v80 offset:36864\n"
      "ds_read_b128 a[148:151], v80 offset:37888\n"
      "v_exp_f32 v73, v33\n"
      "v_mfma_f32_16x16x32_bf16 v[44:47], v[96:99], a[180:183], v[44:47]\n"
      "s_add_u32 m0, s13, 15360\n"
      "ds_read_b128 a[152:155], v80 offset:38912\n"
      "ds_read_b128 a[156:159], v80 offset:39936\n"
      "v_mfma_f32_16x16x32_bf16 v[40:43], v[216:219], a[160:163], v[40:43]\n"
      "s_waitcnt vmcnt(25)\n"
      "v_exp_f32 v74, v34\n"
      "v_exp_f32 v75, v35\n"
      "v_mfma_f32_16x16x32_bf16 v[40:43], v[96:99], a[176:179], v[40:43]\n"
      "s_add_u32 s14, s12, 15360\n"
      "v_add_f32 v72, 1.0, v72\n"
      "v_add_f32 v73, 1.0, v73\n"
      "v_add_f32 v74, 1.0, v74\n"
      "v_mfma_f32_16x16x32_bf16 v[40:43], v[104:107], a[192:195], v[40:43]\n"
      "s_mul_i32 s11, s11, 77824\n"
      "ds_read_b128 a[160:163], v80 offset:40960\n"
      "ds_read_b128 a[164:167], v80 offset:41984\n"
      "v_add_f32 v75, 1.0, v75\n"
      "v_mfma_f32_16x16x32_bf16 v[40:43], v[100:103], a[208:211], v[40:43]\n"
      "buffer_load_dwordx4 v80, s[60:63], s14 offen sc1 lds\n"
      "v_mfma_f32_16x16x32_bf16 v[44:47], v[104:107], a[196:199], v[44:47]\n"
      "ds_read_b128 a[168:171], v80 offset:43008\n"
      "ds_read_b128 a[172:175], v80 offset:44032\n"
      "v_rcp_f32 v72, v72\n"
      "v_mfma_f32_16x16x32_bf16 v[44:47], v[100:103], a[212:215], v[44:47]\n"
      "buffer_load_dwordx4 v[96:99], v80, s[60:63], s76 offen offset:1024 sc1\n"
      "v_mfma_f32_16x16x32_bf16 v[48:51], v[104:107], a[200:203], v[48:51]\n"
      "s_add_u32 s11, s11, s79\n"
      "s_add_u32 s76, s77, s10\n"
      "ds_read_b128 a[176:179], v80 offset:45056\n"
      "v_rcp_f32 v73, v73\n"
      "v_mfma_f32_16x16x32_bf16 v[48:51], v[100:103], a[216:219], v[48:51]\n"
      "s_waitcnt vmcnt(25)\n"
      "ds_read_b128 a[180:183], v80 offset:46080\n"
      "ds_read_b128 a[184:187], v80 offset:47104\n"
      "v_mfma_f32_16x16x32_bf16 v[52:55], v[104:107], a[204:207], v[52:55]\n"
      "s_add_u32 m0, s13, 16384\n"
      "ds_read_b128 a[188:191], v80 offset:48128\n"
      "v_rcp_f32 v74, v74\n"
      "v_mfma_f32_16x16x32_bf16 v[52:55], v[100:103], a[220:223], v[52:55]\n"
      "buffer_load_dwordx4 v[104:107], v80, s[60:63], s76 offen offset:0 sc1\n"
      "v_mfma_f32_16x16x32_bf16 v[52:55], v[108:111], a[236:239], v[52:55]\n"
      "s_add_u32 s11, s11, 548864\n"
      "s_add_u32 s14, s12, 16384\n"
      "ds_read_b128 a[192:195], v80 offset:49152\n"
      "v_rcp_f32 v75, v75\n"
      "v_mfma_f32_16x16x32_bf16 v[52:55], v[112:115], a[252:255], v[52:55]\n"
      "buffer_load_dwordx4 v80, s[60:63], s14 offen sc1 lds\n"
      "v_mul_f32 v72, v72, v32\n"
      "v_mfma_f32_16x16x32_bf16 v[48:51], v[108:111], a[232:235], v[48:51]\n"
      "ds_read_b128 a[196:199], v80 offset:50176\n"
      "ds_read_b128 a[200:203], v80 offset:51200\n"
      "v_mul_f32 v73, v73, v33\n"
      "v_mul_f32 v74, v74, v34\n"
      "v_mfma_f32_16x16x32_bf16 v[48:51], v[112:115], a[248:251], v[48:51]\n"
      "buffer_load_dwordx4 v[100:103], v80, s[60:63], s76 offen offset:1024 sc1\n"
      "ds_read_b128 a[204:207], v80 offset:52224\n"
      "v_mfma_f32_16x16x32_bf16 v[44:47], v[108:111], a[228:231], v[44:47]\n"
      "s_mov_b32 s77, s11\n"
      "s_sub_u32 s76, s77, 4096\n"
      "ds_read_b128 a[208:211], v80 offset:53248\n"
      "v_mul_f32 v75, v75, v35\n"
      "v_cvt_pk_bf16_f32 v76, v72, v73\n"
      "v_mfma_f32_16x16x32_bf16 v[44:47], v[112:115], a[244:247], v[44:47]\n"
      "s_waitcnt vmcnt(26)\n"
      "ds_read_b128 a[212:215], v80 offset:54272\n"
      "ds_read_b128 a[216:219], v80 offset:55296\n"
      "v_cvt_pk_bf16_f32 v77, v74, v75\n"
      "v_mfma_f32_16x16x32_bf16 v[40:43], v[108:111], a[224:227], v[40:43]\n"
      "s_add_u32 m0, s13, 17408\n"
      "ds_read_b128 a[220:223], v80 offset:56320\n"
      "ds_write_b64 v86, v[76:77] offset:59392\n"
      "v_mfma_f32_16x16x32_bf16 v[40:43], v[112:115], a[240:243], v[40:43]\n"
      "buffer_load_dwordx4 v[108:111], v80, s[60:63], s76 offen offset:0 sc1\n"
      "v_mul_f32 v36, v36, v95\n"
      "v_mfma_f32_16x16x32_bf16 v[40:43], v[120:123], v[220:223], v[40:43]\n"
      "s_add_u32 s14, s12, 17408\n"
      "v_mul_f32 v37, v37, v95\n"
      "v_mul_f32 v38, v38, v95\n"
      "v_mul_f32 v39, v39, v95\n"
      "v_mfma_f32_16x16x32_bf16 v[40:43], v[116:119], v[236:239], v[40:43]\n"
      "v_exp_f32 v72, v36\n"
      "v_mfma_f32_16x16x32_bf16 v[44:47], v[120:123], v[224:227], v[44:47]\n"
      "buffer_load_dwordx4 v80, s[60:63], s14 offen sc1 lds\n"
      "v_exp_f32 v73, v37\n"
      "v_mfma_f32_16x16x32_bf16 v[44:47], v[116:119], v[240:243], v[44:47]\n"
      "buffer_load_dwordx4 v[112:115], v80, s[60:63], s76 offen offset:1024 sc1\n"
      "v_mfma_f32_16x16x32_bf16 v[48:51], v[120:123], v[228:231], v[48:51]\n"
      "v_exp_f32 v74, v38\n"
      "v_exp_f32 v75, v39\n"
      "v_mfma_f32_16x16x32_bf16 v[48:51], v[116:119], v[244:247], v[48:51]\n"
      "v_add_f32 v72, 1.0, v72\n"
      "v_add_f32 v73, 1.0, v73\n"
      "v_add_f32 v74, 1.0, v74\n"
      "v_mfma_f32_16x16x32_bf16 v[52:55], v[120:123], v[232:235], v[52:55]\n"
      "s_waitcnt vmcnt(28)\n"
      "v_add_f32 v75, 1.0, v75\n"
      "v_rcp_f32 v72, v72\n"
      "v_mfma_f32_16x16x32_bf16 v[52:55], v[116:119], v[248:251], v[52:55]\n"
      "buffer_load_dwordx4 v[120:123], v80, s[60:63], s76 offen offset:2048 sc1\n"
      "v_mfma_f32_16x16x32_bf16 v[52:55], v[124:127], v[68:71], v[52:55]\n"
      "v_rcp_f32 v73, v73\n"
      "v_rcp_f32 v74, v74\n"
      "v_mfma_f32_16x16x32_bf16 v[48:51], v[124:127], v[64:67], v[48:51]\n"
      "v_rcp_f32 v75, v75\n"
      "v_mul_f32 v72, v72, v36\n"
      "v_mfma_f32_16x16x32_bf16 v[44:47], v[124:127], v[60:63], v[44:47]\n"
      "s_waitcnt vmcnt(27) lgkmcnt(15)\n"
      "v_mul_f32 v73, v73, v37\n"
      "v_mul_f32 v74, v74, v38\n"
      "v_mul_f32 v75, v75, v39\n"
      "v_cvt_pk_bf16_f32 v78, v72, v73\n"
      "v_mfma_f32_16x16x32_bf16 v[40:43], v[124:127], v[56:59], v[40:43]\n"
      "buffer_load_dwordx4 v[116:119], v80, s[60:63], s76 offen offset:3072 sc1\n"
      "v_cvt_pk_bf16_f32 v79, v74, v75\n"
      "v_mfma_f32_16x16x32_bf16 v[8:11], v[128:131], a[0:3], 0\n"
      "s_add_u32 s76, s77, s4\n"
      "ds_write_b64 v86, v[78:79] offset:60416\n"
      "v_mfma_f32_16x16x32_bf16 v[8:11], v[136:139], a[16:19], v[8:11]\n"
      "v_mfma_f32_16x16x32_bf16 v[12:15], v[128:131], a[4:7], 0\n"
      "v_mfma_f32_16x16x32_bf16 v[12:15], v[136:139], a[20:23], v[12:15]\n"
      "buffer_load_dwordx4 v[124:127], v80, s[60:63], s76 offen offset:0 sc1\n"
      "v_mul_f32 v40, v40, v92\n"
      "v_mfma_f32_16x16x32_bf16 v[16:19], v[128:131], a[8:11], 0\n"
      "v_mul_f32 v41, v41, v92\n"
      "v_mul_f32 v42, v42, v92\n"
      "v_mul_f32 v43, v43, v92\n"
      "v_mfma_f32_16x16x32_bf16 v[16:19], v[136:139], a[24:27], v[16:19]\n"
      "v_exp_f32 v72, v40\n"
      "v_exp_f32 v73, v41\n"
      "s_waitcnt lgkmcnt(0)\n"
      "s_barrier\n"
      "v_mfma_f32_16x16x32_bf16 v[20:23], v[128:131], a[12:15], 0\n"
      "s_waitcnt vmcnt(27)\n"
      "ds_read_b128 a[224:227], v80 offset:57344\n"
      "v_exp_f32 v74, v42\n"
      "v_mfma_f32_16x16x32_bf16 v[20:23], v[136:139], a[28:31], v[20:23]\n"
      "buffer_load_dwordx4 v[128:131], v80, s[60:63], s76 offen offset:1024 sc1\n"
      "v_mfma_f32_16x16x32_bf16 v[20:23], v[132:135], a[44:47], v[20:23]\n"
      "s_add_u32 s76, s77, s5\n"
      "ds_read_b128 a[228:231], v80 offset:58368\n"
      "v_exp_f32 v75, v43\n"
      "v_add_f32 v72, 1.0, v72\n"
      "v_mfma_f32_16x16x32_bf16 v[20:23], v[140:143], a[60:63], v[20:23]\n"
      "v_add_f32 v73, 1.0, v73\n"
      "v_add_f32 v74, 1.0, v74\n"
      "v_add_f32 v75, 1.0, v75\n"
      "v_mfma_f32_16x16x32_bf16 v[16:19], v[132:135], a[40:43], v[16:19]\n"
      "v_rcp_f32 v72, v72\n"
      "v_rcp_f32 v73, v73\n"
      "v_mfma_f32_16x16x32_bf16 v[16:19], v[140:143], a[56:59], v[16:19]\n"
      "buffer_load_dwordx4 v[136:139], v80, s[60:63], s76 offen offset:0 sc1\n"
      "v_mfma_f32_16x16x32_bf16 v[12:15], v[132:135], a[36:39], v[12:15]\n"
      "ds_read_b128 a[232:235], v80 offset:59392\n"
      "v_rcp_f32 v74, v74\n"
      "v_mfma_f32_16x16x32_bf16 v[12:15], v[140:143], a[52:55], v[12:15]\n"
      "v_rcp_f32 v75, v75\n"
      "v_mul_f32 v72, v72, v40\n"
      "v_mul_f32 v73, v73, v41\n"
      "v_mfma_f32_16x16x32_bf16 v[8:11], v[132:135], a[32:35], v[8:11]\n"
      "s_waitcnt vmcnt(27)\n"
      "v_mul_f32 v74, v74, v42\n"
      "v_mul_f32 v75, v75, v43\n"
      "v_cvt_pk_bf16_f32 v76, v72, v73\n"
      "v_cvt_pk_bf16_f32 v77, v74, v75\n"
      "v_mfma_f32_16x16x32_bf16 v[8:11], v[140:143], a[48:51], v[8:11]\n"
      "buffer_load_dwordx4 v[132:135], v80, s[60:63], s76 offen offset:1024 sc1\n"
      "v_mfma_f32_16x16x32_bf16 v[8:11], v[144:147], a[64:67], v[8:11]\n"
      "s_add_u32 s76, s77, s6\n"
      "ds_read_b128 a[236:239], v80 offset:60416\n"
      "ds_write_b64 v87, v[76:77] offset:0\n"
      "v_mfma_f32_16x16x32_bf16 v[8:11], v[152:155], a[80:83], v[8:11]\n"
      "v_mul_f32 v44, v44, v93\n"
      "v_mul_f32 v45, v45, v93\n"
      "v_mul_f32 v46, v46, v93\n"
      "v_mul_f32 v47, v47, v93\n"
      "v_mfma_f32_16x16x32_bf16 v[12:15], v[144:147], a[68:71], v[12:15]\n"
      "v_exp_f32 v72, v44\n"
      "v_exp_f32 v73, v45\n"
      "v_mfma_f32_16x16x32_bf16 v[12:15], v[152:155], a[84:87], v[12:15]\n"
      "buffer_load_dwordx4 v[140:143], v80, s[60:63], s76 offen offset:0 sc1\n"
      "v_mfma_f32_16x16x32_bf16 v[16:19], v[144:147], a[72:75], v[16:19]\n"
      "ds_read_b128 a[240:243], v80 offset:61440\n"
      "v_exp_f32 v74, v46\n"
      "v_mfma_f32_16x16x32_bf16 v[16:19], v[152:155], a[88:91], v[16:19]\n"
      "v_exp_f32 v75, v47\n"
      "v_add_f32 v72, 1.0, v72\n"
      "v_add_f32 v73, 1.0, v73\n"
      "v_mfma_f32_16x16x32_bf16 v[20:23], v[144:147], a[76:79], v[20:23]\n"
      "s_waitcnt vmcnt(27)\n"
      "v_add_f32 v74, 1.0, v74\n"
      "v_add_f32 v75, 1.0, v75\n"
      "v_rcp_f32 v72, v72\n"
      "v_mfma_f32_16x16x32_bf16 v[20:23], v[152:155], a[92:95], v[20:23]\n"
      "buffer_load_dwordx4 v[144:147], v80, s[60:63], s76 offen offset:1024 sc1\n"
      "v_mfma_f32_16x16x32_bf16 v[20:23], v[148:151], a[108:111], v[20:23]\n"
      "s_add_u32 s76, s77, s7\n"
      "ds_read_b128 a[244:247], v80 offset:62464\n"
      "v_rcp_f32 v73, v73\n"
      "v_mfma_f32_16x16x32_bf16 v[20:23], v[156:159], a[124:127], v[20:23]\n"
      "v_rcp_f32 v74, v74\n"
      "v_rcp_f32 v75, v75\n"
      "v_mfma_f32_16x16x32_bf16 v[16:19], v[148:151], a[104:107], v[16:19]\n"
      "v_mul_f32 v72, v72, v44\n"
      "v_mul_f32 v73, v73, v45\n"
      "v_mul_f32 v74, v74, v46\n"
      "v_mul_f32 v75, v75, v47\n"
      "v_mfma_f32_16x16x32_bf16 v[16:19], v[156:159], a[120:123], v[16:19]\n"
      "buffer_load_dwordx4 v[152:155], v80, s[60:63], s76 offen offset:0 sc1\n"
      "v_cvt_pk_bf16_f32 v78, v72, v73\n"
      "v_mfma_f32_16x16x32_bf16 v[12:15], v[148:151], a[100:103], v[12:15]\n"
      "s_lshr_b32 s11, s5, 11\n"
      "ds_read_b128 a[248:251], v80 offset:63488\n"
      "v_cvt_pk_bf16_f32 v79, v74, v75\n"
      "v_mfma_f32_16x16x32_bf16 v[12:15], v[156:159], a[116:119], v[12:15]\n"
      "ds_write_b64 v87, v[78:79] offset:1024\n"
      "v_mul_f32 v48, v48, v94\n"
      "v_mfma_f32_16x16x32_bf16 v[8:11], v[148:151], a[96:99], v[8:11]\n"
      "s_waitcnt vmcnt(27)\n"
      "v_mul_f32 v49, v49, v94\n"
      "v_mul_f32 v50, v50, v94\n"
      "v_mul_f32 v51, v51, v94\n"
      "v_mfma_f32_16x16x32_bf16 v[8:11], v[156:159], a[112:115], v[8:11]\n"
      "buffer_load_dwordx4 v[148:151], v80, s[60:63], s76 offen offset:1024 sc1\n"
      "v_mfma_f32_16x16x32_bf16 v[8:11], v[160:163], a[128:131], v[8:11]\n"
      "s_add_u32 s11, s11, 1\n"
      "s_add_u32 s76, s77, s8\n"
      "ds_read_b128 a[252:255], v80 offset:64512\n"
      "v_exp_f32 v72, v48\n"
      "v_mfma_f32_16x16x32_bf16 v[8:11], v[168:171], a[144:147], v[8:11]\n"
      "v_exp_f32 v73, v49\n"
      "v_exp_f32 v74, v50\n"
      "v_mfma_f32_16x16x32_bf16 v[12:15], v[160:163], a[132:135], v[12:15]\n"
      "v_exp_f32 v75, v51\n"
      "v_add_f32 v72, 1.0, v72\n"
      "v_add_f32 v73, 1.0, v73\n"
      "v_mfma_f32_16x16x32_bf16 v[12:15], v[168:171], a[148:151], v[12:15]\n"
      "buffer_load_dwordx4 v[156:159], v80, s[60:63], s76 offen offset:0 sc1\n"
      "v_add_f32 v74, 1.0, v74\n"
      "v_mfma_f32_16x16x32_bf16 v[16:19], v[160:163], a[136:139], v[16:19]\n"
      "s_add_u32 s11, s34, s11\n"
      "v_add_f32 v75, 1.0, v75\n"
      "v_rcp_f32 v72, v72\n"
      "v_mfma_f32_16x16x32_bf16 v[16:19], v[168:171], a[152:155], v[16:19]\n"
      "v_rcp_f32 v73, v73\n"
      "v_rcp_f32 v74, v74\n"
      "v_mfma_f32_16x16x32_bf16 v[20:23], v[160:163], a[140:143], v[20:23]\n"
      "s_waitcnt vmcnt(27)\n"
      "v_rcp_f32 v75, v75\n"
      "v_mul_f32 v72, v72, v48\n"
      "v_mul_f32 v73, v73, v49\n"
      "v_mfma_f32_16x16x32_bf16 v[20:23], v[168:171], a[156:159], v[20:23]\n"
      "buffer_load_dwordx4 v[160:163], v80, s[60:63], s76 offen offset:1024 sc1\n"
      "v_mul_f32 v74, v74, v50\n"
      "v_mfma_f32_16x16x32_bf16 v[20:23], v[164:167], a[172:175], v[20:23]\n"
      "s_and_b32 s11, s11, 7\n"
      "s_add_u32 s76, s77, s9\n"
      "v_mul_f32 v75, v75, v51\n"
      "v_cvt_pk_bf16_f32 v76, v72, v73\n"
      "v_cvt_pk_bf16_f32 v77, v74, v75\n"
      "v_mfma_f32_16x16x32_bf16 v[20:23], v[172:175], a[188:191], v[20:23]\n"
      "ds_write_b64 v87, v[76:77] offset:2048\n"
      "v_mul_f32 v52, v52, v95\n"
      "v_mfma_f32_16x16x32_bf16 v[16:19], v[164:167], a[168:171], v[16:19]\n"
      "v_mul_f32 v53, v53, v95\n"
      "v_mul_f32 v54, v54, v95\n"
      "v_mul_f32 v55, v55, v95\n"
      "v_mfma_f32_16x16x32_bf16 v[16:19], v[172:175], a[184:187], v[16:19]\n"
      "buffer_load_dwordx4 v[168:171], v80, s[60:63], s76 offen offset:0 sc1\n"
      "v_mfma_f32_16x16x32_bf16 v[12:15], v[164:167], a[164:167], v[12:15]\n"
      "s_mul_i32 s11, s11, 77824\n"
      "v_exp_f32 v72, v52\n"
      "v_exp_f32 v73, v53\n"
      "v_mfma_f32_16x16x32_bf16 v[12:15], v[172:175], a[180:183], v[12:15]\n"
      "v_exp_f32 v74, v54\n"
      "v_exp_f32 v75, v55\n"
      "v_mfma_f32_16x16x32_bf16 v[8:11], v[164:167], a[160:163], v[8:11]\n"
      "s_waitcnt vmcnt(26)\n"
      "v_add_f32 v72, 1.0, v72\n"
      "v_add_f32 v73, 1.0, v73\n"
      "v_add_f32 v74, 1.0, v74\n"
      "v_add_f32 v75, 1.0, v75\n"
      "v_mfma_f32_16x16x32_bf16 v[8:11], v[172:175], a[176:179], v[8:11]\n"
      "buffer_load_dwordx4 v[164:167], v80, s[60:63], s76 offen offset:1024 sc1\n"
      "v_mfma_f32_16x16x32_bf16 v[8:11], v[176:179], a[192:195], v[8:11]\n"
      "s_add_u32 s11, s11, s79\n"
      "s_add_u32 s76, s77, s10\n"
      "v_rcp_f32 v72, v72\n"
      "v_mfma_f32_16x16x32_bf16 v[8:11], v[184:187], a[208:211], v[8:11]\n"
      "s_mov_b32 s22, s28\n"
      "v_rcp_f32 v73, v73\n"
      "v_rcp_f32 v74, v74\n"
      "v_mfma_f32_16x16x32_bf16 v[12:15], v[176:179], a[196:199], v[12:15]\n"
      "v_rcp_f32 v75, v75\n"
      "v_mul_f32 v72, v72, v52\n"
      "v_mul_f32 v73, v73, v53\n"
      "v_mfma_f32_16x16x32_bf16 v[12:15], v[184:187], a[212:215], v[12:15]\n"
      "buffer_load_dwordx4 v[172:175], v80, s[60:63], s76 offen offset:0 sc1\n"
      "v_mul_f32 v74, v74, v54\n"
      "v_mfma_f32_16x16x32_bf16 v[16:19], v[176:179], a[200:203], v[16:19]\n"
      "s_add_u32 s11, s11, 548864\n"
      "v_mul_f32 v75, v75, v55\n"
      "v_cvt_pk_bf16_f32 v78, v72, v73\n"
      "v_cvt_pk_bf16_f32 v79, v74, v75\n"
      "v_mfma_f32_16x16x32_bf16 v[16:19], v[184:187], a[216:219], v[16:19]\n"
      "ds_write_b64 v87, v[78:79] offset:3072\n"
      "v_mfma_f32_16x16x32_bf16 v[20:23], v[176:179], a[204:207], v[20:23]\n"
      "s_waitcnt vmcnt(21) lgkmcnt(2)\n"
      "v_mfma_f32_16x16x32_bf16 v[20:23], v[184:187], a[220:223], v[20:23]\n"
      "buffer_load_dwordx4 v[176:179], v80, s[60:63], s76 offen offset:1024 sc1\n"
      "s_waitcnt lgkmcnt(0)\n"
      "s_barrier\n"
      "v_mfma_f32_16x16x32_bf16 v[20:23], v[180:183], a[236:239], v[20:23]\n"
      "s_mov_b32 s77, s11\n"
      "s_sub_u32 s76, s77, 4096\n"
      "ds_read_b128 v[220:223], v85 offset:0\n"
      "v_mfma_f32_16x16x32_bf16 v[20:23], v[96:99], a[252:255], v[20:23]\n"
      "ds_read_b128 v[224:227], v85 offset:1024\n"
      "ds_read_b128 v[228:231], v85 offset:2048\n"
      "v_mfma_f32_16x16x32_bf16 v[16:19], v[180:183], a[232:235], v[16:19]\n"
      "ds_read_b128 v[232:235], v85 offset:3072\n"
      "ds_read_b128 v[236:239], v85 offset:4096\n"
      "v_mfma_f32_16x16x32_bf16 v[16:19], v[96:99], a[248:251], v[16:19]\n"
      "buffer_load_dwordx4 v[184:187], v80, s[60:63], s76 offen offset:0 sc1\n"
      "v_mfma_f32_16x16x32_bf16 v[12:15], v[180:183], a[228:231], v[12:15]\n"
      "ds_read_b128 v[240:243], v85 offset:5120\n"
      "ds_read_b128 v[244:247], v85 offset:6144\n"
      "v_mfma_f32_16x16x32_bf16 v[12:15], v[96:99], a[244:247], v[12:15]\n"
      "ds_read_b128 v[248:251], v85 offset:7168\n"
      "v_and_b32 v5, 7, v1\n"
      "v_mfma_f32_16x16x32_bf16 v[8:11], v[180:183], a[224:227], v[8:11]\n"
      "s_waitcnt vmcnt(20) lgkmcnt(3)\n"
      "v_lshlrev_b32 v90, 4, v5\n"
      "v_sub_u32 v5, s34, v1\n"
      "v_mfma_f32_16x16x32_bf16 v[8:11], v[96:99], a[240:243], v[8:11]\n"
      "buffer_load_dwordx4 v[180:183], v80, s[60:63], s76 offen offset:1024 sc1\n"
      "v_mfma_f32_16x16x32_bf16 v[8:11], v[104:107], v[220:223], v[8:11]\n"
      "v_and_b32 v5, 7, v5\n"
      "v_lshlrev_b32 v5, 10, v5\n"
      "v_mfma_f32_16x16x32_bf16 v[8:11], v[100:103], v[236:239], v[8:11]\n"
      "v_lshrrev_b32 v2, 5, v1\n"
      "v_lshl_add_u32 v4, v2, 10, v90\n"
      "v_mfma_f32_16x16x32_bf16 v[12:15], v[104:107], v[224:227], v[12:15]\n"
      "s_waitcnt lgkmcnt(2)\n"
      "v_mfma_f32_16x16x32_bf16 v[12:15], v[100:103], v[240:243], v[12:15]\n"
      "buffer_load_dwordx4 v[96:99], v80, s[60:63], s76 offen offset:2048 sc1\n"
      "v_mfma_f32_16x16x32_bf16 v[16:19], v[104:107], v[228:231], v[16:19]\n"
      "v_lshl_add_u32 v90, v2, 8, v90\n"
      "v_bfe_u32 v2, v1, 3, 2\n"
      "s_waitcnt lgkmcnt(1)\n"
      "v_mfma_f32_16x16x32_bf16 v[16:19], v[100:103], v[244:247], v[16:19]\n"
      "v_mfma_f32_16x16x32_bf16 v[20:23], v[104:107], v[232:235], v[20:23]\n"
      "s_waitcnt vmcnt(19)\n"
      "v_lshl_add_u32 v4, v2, 13, v4\n"
      "s_waitcnt lgkmcnt(0)\n"
      "v_mfma_f32_16x16x32_bf16 v[20:23], v[100:103], v[248:251], v[20:23]\n"
      "buffer_load_dwordx4 v[104:107], v80, s[60:63], s76 offen offset:3072 sc1\n"
      "v_mfma_f32_16x16x32_bf16 v[24:27], v[108:111], a[0:3], 0\n"
      "s_add_u32 s76, s77, s4\n"
      "v_mfma_f32_16x16x32_bf16 v[24:27], v[112:115], a[16:19], v[24:27]\n"
      "v_cvt_pk_bf16_f32 v72, v8, v9\n"
      "v_cvt_pk_bf16_f32 v73, v10, v11\n"
      "v_mfma_f32_16x16x32_bf16 v[28:31], v[108:111], a[4:7], 0\n"
      "v_cvt_pk_bf16_f32 v74, v12, v13\n"
      "v_cvt_pk_bf16_f32 v75, v14, v15\n"
      "v_mfma_f32_16x16x32_bf16 v[28:31], v[112:115], a[20:23], v[28:31]\n"
      "buffer_load_dwordx4 v[100:103], v80, s[60:63], s76 offen offset:0 sc1\n"
      "v_mfma_f32_16x16x32_bf16 v[32:35], v[108:111], a[8:11], 0\n"
      "v_permlane16_swap_b32 v72, v74\n"
      "v_mfma_f32_16x16x32_bf16 v[32:35], v[112:115], a[24:27], v[32:35]\n"
      "s_waitcnt vmcnt(19)\n"
      "v_permlane16_swap_b32 v73, v75\n"
      "v_mfma_f32_16x16x32_bf16 v[36:39], v[108:111], a[12:15], 0\n"
      "buffer_store_dwordx4 v[72:75], v4, s[60:63], s22 offen sc1\n"
      "v_mfma_f32_16x16x32_bf16 v[36:39], v[112:115], a[28:31], v[36:39]\n"
      "buffer_load_dwordx4 v[108:111], v80, s[60:63], s76 offen offset:1024 sc1\n"
      "v_mfma_f32_16x16x32_bf16 v[36:39], v[120:123], a[44:47], v[36:39]\n"
      "s_add_u32 s76, s77, s5\n"
      "v_cvt_pk_bf16_f32 v76, v16, v17\n"
      "v_cvt_pk_bf16_f32 v77, v18, v19\n"
      "v_mfma_f32_16x16x32_bf16 v[36:39], v[116:119], a[60:63], v[36:39]\n"
      "v_cvt_pk_bf16_f32 v78, v20, v21\n"
      "v_cvt_pk_bf16_f32 v79, v22, v23\n"
      "v_mfma_f32_16x16x32_bf16 v[32:35], v[120:123], a[40:43], v[32:35]\n"
      "v_permlane16_swap_b32 v76, v78\n"
      "v_mfma_f32_16x16x32_bf16 v[32:35], v[116:119], a[56:59], v[32:35]\n"
      "buffer_load_dwordx4 v[112:115], v80, s[60:63], s76 offen offset:0 sc1\n"
      "v_mfma_f32_16x16x32_bf16 v[28:31], v[120:123], a[36:39], v[28:31]\n"
      "s_waitcnt vmcnt(20)\n"
      "v_permlane16_swap_b32 v77, v79\n"
      "v_mfma_f32_16x16x32_bf16 v[28:31], v[116:119], a[52:55], v[28:31]\n"
      "s_add_u32 s22, s22, 32768\n"
      "v_mfma_f32_16x16x32_bf16 v[24:27], v[120:123], a[32:35], v[24:27]\n"
      "buffer_store_dwordx4 v[76:79], v4, s[60:63], s22 offen sc1\n"
      "v_mfma_f32_16x16x32_bf16 v[24:27], v[116:119], a[48:51], v[24:27]\n"
      "buffer_load_dwordx4 v[120:123], v80, s[60:63], s76 offen offset:1024 sc1\n"
      "v_mfma_f32_16x16x32_bf16 v[24:27], v[124:127], a[64:67], v[24:27]\n"
      "s_add_u32 s76, s77, s6\n"
      "v_mfma_f32_16x16x32_bf16 v[24:27], v[128:131], a[80:83], v[24:27]\n"
      "v_mfma_f32_16x16x32_bf16 v[28:31], v[124:127], a[68:71], v[28:31]\n"
      "v_mfma_f32_16x16x32_bf16 v[28:31], v[128:131], a[84:87], v[28:31]\n"
      "buffer_load_dwordx4 v[116:119], v80, s[60:63], s76 offen offset:0 sc1\n"
      "v_mfma_f32_16x16x32_bf16 v[32:35], v[124:127], a[72:75], v[32:35]\n"
      "v_and_b32 v2, 0x1f7, v86\n"
      "v_bfe_u32 v3, v1, 4, 1\n"
      "v_mfma_f32_16x16x32_bf16 v[32:35], v[128:131], a[88:91], v[32:35]\n"
      "s_waitcnt vmcnt(21)\n"
      "v_mul_u32_u24 v3, 18432, v3\n"
      "v_add_u32 v2, v2, v3\n"
      "v_mfma_f32_16x16x32_bf16 v[36:39], v[124:127], a[76:79], v[36:39]\n"
      "buffer_load_dwordx4 v[188:191], v2, s[60:63], s25 offen sc1\n"
      "v_mfma_f32_16x16x32_bf16 v[36:39], v[128:131], a[92:95], v[36:39]\n"
      "buffer_load_dwordx4 v[124:127], v80, s[60:63], s76 offen offset:1024 sc1\n"
      "v_mfma_f32_16x16x32_bf16 v[36:39], v[136:139], a[108:111], v[36:39]\n"
      "s_add_u32 s76, s77, s7\n"
      "s_add_u32 s25, s25, 36864\n"
      "v_mfma_f32_16x16x32_bf16 v[36:39], v[132:135], a[124:127], v[36:39]\n"
      "buffer_load_dwordx4 v[192:195], v2, s[60:63], s25 offen sc1\n"
      "v_mfma_f32_16x16x32_bf16 v[32:35], v[136:139], a[104:107], v[32:35]\n"
      "v_mfma_f32_16x16x32_bf16 v[32:35], v[132:135], a[120:123], v[32:35]\n"
      "buffer_load_dwordx4 v[128:131], v80, s[60:63], s76 offen offset:0 sc1\n"
      "v_mfma_f32_16x16x32_bf16 v[28:31], v[136:139], a[100:103], v[28:31]\n"
      "s_lshr_b32 s11, s6, 11\n"
      "v_mfma_f32_16x16x32_bf16 v[28:31], v[132:135], a[116:119], v[28:31]\n"
      "v_mfma_f32_16x16x32_bf16 v[24:27], v[136:139], a[96:99], v[24:27]\n"
      "s_waitcnt vmcnt(23)\n"
      "v_mfma_f32_16x16x32_bf16 v[24:27], v[132:135], a[112:115], v[24:27]\n"
      "buffer_load_dwordx4 v[136:139], v80, s[60:63], s76 offen offset:1024 sc1\n"
      "v_mfma_f32_16x16x32_bf16 v[24:27], v[140:143], a[128:131], v[24:27]\n"
      "s_add_u32 s11, s11, 1\n"
      "s_add_u32 s76, s77, s8\n"
      "v_mfma_f32_16x16x32_bf16 v[24:27], v[144:147], a[144:147], v[24:27]\n"
      "v_mfma_f32_16x16x32_bf16 v[28:31], v[140:143], a[132:135], v[28:31]\n"
      "v_mfma_f32_16x16x32_bf16 v[28:31], v[144:147], a[148:151], v[28:31]\n"
      "buffer_load_dwordx4 v[132:135], v80, s[60:63], s76 offen offset:0 sc1\n"
      "v_mfma_f32_16x16x32_bf16 v[32:35], v[140:143], a[136:139], v[32:35]\n"
      "s_add_u32 s11, s34, s11\n"
      "v_mfma_f32_16x16x32_bf16 v[32:35], v[144:147], a[152:155], v[32:35]\n"
      "v_mfma_f32_16x16x32_bf16 v[36:39], v[140:143], a[140:143], v[36:39]\n"
      "s_waitcnt vmcnt(23)\n"
      "v_mfma_f32_16x16x32_bf16 v[36:39], v[144:147], a[156:159], v[36:39]\n"
      "buffer_load_dwordx4 v[140:143], v80, s[60:63], s76 offen offset:1024 sc1\n"
      "v_mfma_f32_16x16x32_bf16 v[36:39], v[152:155], a[172:175], v[36:39]\n"
      "s_and_b32 s11, s11, 7\n"
      "s_add_u32 s76, s77, s9\n"
      "v_mfma_f32_16x16x32_bf16 v[36:39], v[148:151], a[188:191], v[36:39]\n"
      "v_mfma_f32_16x16x32_bf16 v[32:35], v[152:155], a[168:171], v[32:35]\n"
      "v_mfma_f32_16x16x32_bf16 v[32:35], v[148:151], a[184:187], v[32:35]\n"
      "buffer_load_dwordx4 v[144:147], v80, s[60:63], s76 offen offset:0 sc1\n"
      "v_mfma_f32_16x16x32_bf16 v[28:31], v[152:155], a[164:167], v[28:31]\n"
      "s_mul_i32 s11, s11, 77824\n"
      "s_lshr_b32 s71, s34, 1\n"
      "v_mfma_f32_16x16x32_bf16 v[28:31], v[148:151], a[180:183], v[28:31]\n"
      "s_waitcnt vmcnt(23)\n"
      "v_mfma_f32_16x16x32_bf16 v[24:27], v[152:155], a[160:163], v[24:27]\n"
      "s_lshl2_add_u32 s73, s35, s71\n"
      "v_mfma_f32_16x16x32_bf16 v[24:27], v[148:151], a[176:179], v[24:27]\n"
      "buffer_load_dwordx4 v[152:155], v80, s[60:63], s76 offen offset:1024 sc1\n"
      "v_mfma_f32_16x16x32_bf16 v[24:27], v[156:159], a[192:195], v[24:27]\n"
      "s_add_u32 s11, s11, s79\n"
      "s_add_u32 s76, s77, s10\n"
      "s_lshr_b32 s22, s4, 11\n"
      "v_mfma_f32_16x16x32_bf16 v[24:27], v[160:163], a[208:211], v[24:27]\n"
      "s_add_u32 s22, s22, 1\n"
      "s_add_u32 s22, s34, s22\n"
      "s_and_b32 s22, s22, 7\n"
      "v_mfma_f32_16x16x32_bf16 v[28:31], v[156:159], a[196:199], v[28:31]\n"
      "s_lshl_b32 s22, s22, 16\n"
      "s_add_u32 s22, s22, s26\n"
      "s_mul_i32 s73, s73, 18432\n"
      "v_mfma_f32_16x16x32_bf16 v[28:31], v[160:163], a[212:215], v[28:31]\n"
      "buffer_load_dwordx4 v[148:151], v80, s[60:63], s76 offen offset:0 sc1\n"
      "v_mfma_f32_16x16x32_bf16 v[32:35], v[156:159], a[200:203], v[32:35]\n"
      "s_add_u32 s11, s11, 548864\n"
      "s_lshl_b32 s70, s64, 9\n"
      "v_mfma_f32_16x16x32_bf16 v[32:35], v[160:163], a[216:219], v[32:35]\n"
      "v_mfma_f32_16x16x32_bf16 v[36:39], v[156:159], a[204:207], v[36:39]\n"
      "s_waitcnt vmcnt(23)\n"
      "v_mfma_f32_16x16x32_bf16 v[36:39], v[160:163], a[220:223], v[36:39]\n"
      "buffer_load_dwordx4 v[156:159], v80, s[60:63], s76 offen offset:1024 sc1\n"
      "v_mfma_f32_16x16x32_bf16 v[36:39], v[168:171], a[236:239], v[36:39]\n"
      "s_mov_b32 s77, s11\n"
      "s_sub_u32 s76, s77, 4096\n"
      "s_add_u32 s73, s73, s70\n"
      "v_mfma_f32_16x16x32_bf16 v[36:39], v[164:167], a[252:255], v[36:39]\n"
      "v_mfma_f32_16x16x32_bf16 v[32:35], v[168:171], a[232:235], v[32:35]\n"
      "v_mfma_f32_16x16x32_bf16 v[32:35], v[164:167], a[248:251], v[32:35]\n"
      "buffer_load_dwordx4 v[160:163], v80, s[60:63], s76 offen offset:0 sc1\n"
      "v_mfma_f32_16x16x32_bf16 v[28:31], v[168:171], a[228:231], v[28:31]\n"
      "s_and_b32 s70, s34, 1\n"
      "v_mfma_f32_16x16x32_bf16 v[28:31], v[164:167], a[244:247], v[28:31]\n"
      "s_waitcnt vmcnt(23)\n"
      "v_mfma_f32_16x16x32_bf16 v[24:27], v[168:171], a[224:227], v[24:27]\n"
      "s_lshl_b32 s70, s70, 7\n"
      "v_mfma_f32_16x16x32_bf16 v[24:27], v[164:167], a[240:243], v[24:27]\n"
      "buffer_load_dwordx4 v[168:171], v80, s[60:63], s76 offen offset:1024 sc1\n"
      "v_mfma_f32_16x16x32_bf16 v[24:27], v[172:175], v[220:223], v[24:27]\n"
      "v_mfma_f32_16x16x32_bf16 v[24:27], v[176:179], v[236:239], v[24:27]\n"
      "s_add_u32 s73, s73, s70\n"
      "v_mfma_f32_16x16x32_bf16 v[28:31], v[172:175], v[224:227], v[28:31]\n"
      "v_mfma_f32_16x16x32_bf16 v[28:31], v[176:179], v[240:243], v[28:31]\n"
      "buffer_load_dwordx4 v[164:167], v80, s[60:63], s76 offen offset:2048 sc1\n"
      "v_mfma_f32_16x16x32_bf16 v[32:35], v[172:175], v[228:231], v[32:35]\n"
      "s_add_u32 s25, s73, 2899968\n"
      "v_mfma_f32_16x16x32_bf16 v[32:35], v[176:179], v[244:247], v[32:35]\n"
      "v_mfma_f32_16x16x32_bf16 v[36:39], v[172:175], v[232:235], v[36:39]\n"
      "s_waitcnt vmcnt(23)\n"
      "v_mfma_f32_16x16x32_bf16 v[36:39], v[176:179], v[248:251], v[36:39]\n"
      "buffer_load_dwordx4 v[172:175], v80, s[60:63], s76 offen offset:3072 sc1\n"
      "v_mfma_f32_16x16x32_bf16 v[8:11], v[184:187], a[0:3], 0\n"
      "s_add_u32 s76, s77, s4\n"
      "s_and_b32 s70, s64, 1\n"
      "v_mfma_f32_16x16x32_bf16 v[8:11], v[180:183], a[16:19], v[8:11]\n"
      "v_cvt_pk_bf16_f32 v72, v24, v25\n"
      "v_cvt_pk_bf16_f32 v73, v26, v27\n"
      "v_mfma_f32_16x16x32_bf16 v[12:15], v[184:187], a[4:7], 0\n"
      "v_cvt_pk_bf16_f32 v74, v28, v29\n"
      "v_cvt_pk_bf16_f32 v75, v30, v31\n"
      "v_mfma_f32_16x16x32_bf16 v[12:15], v[180:183], a[20:23], v[12:15]\n"
      "buffer_load_dwordx4 v[176:179], v80, s[60:63], s76 offen offset:0 sc1\n"
      "v_mfma_f32_16x16x32_bf16 v[16:19], v[184:187], a[8:11], 0\n"
      "buffer_store_dwordx4 v[72:75], v80, s[60:63], s22 offen offset:0 sc1\n"
      "v_mfma_f32_16x16x32_bf16 v[16:19], v[180:183], a[24:27], v[16:19]\n"
      "s_lshl1_add_u32 s70, s35, s70\n"
      "v_cvt_pk_bf16_f32 v76, v32, v33\n"
      "v_cvt_pk_bf16_f32 v77, v34, v35\n"
      "v_mfma_f32_16x16x32_bf16 v[20:23], v[184:187], a[12:15], 0\n"
      "s_lshl_b32 s70, s70, 5\n"
      "v_cvt_pk_bf16_f32 v78, v36, v37\n"
      "v_cvt_pk_bf16_f32 v79, v38, v39\n"
      "v_mfma_f32_16x16x32_bf16 v[20:23], v[180:183], a[28:31], v[20:23]\n"
      "buffer_load_dwordx4 v[184:187], v80, s[60:63], s76 offen offset:1024 sc1\n"
      "s_waitcnt vmcnt(26)\n"
      "v_mfma_f32_16x16x32_bf16 v[20:23], v[96:99], a[44:47], v[20:23]\n"
      "s_add_u32 s76, s77, s5\n"
      "s_waitcnt vmcnt(25)\n"
      "v_mfma_f32_16x16x32_bf16 v[20:23], v[104:107], a[60:63], v[20:23]\n"
      "buffer_store_dwordx4 v[76:79], v80, s[60:63], s22 offen offset:1024 sc1\n"
      "v_mfma_f32_16x16x32_bf16 v[16:19], v[96:99], a[40:43], v[16:19]\n"
      "s_lshr_b32 s71, s64, 1\n"
      "v_mfma_f32_16x16x32_bf16 v[16:19], v[104:107], a[56:59], v[16:19]\n"
      "buffer_load_dwordx4 v[180:183], v80, s[60:63], s76 offen offset:0 sc1\n"
      "v_mfma_f32_16x16x32_bf16 v[12:15], v[96:99], a[36:39], v[12:15]\n"
      "s_add_u32 s70, s70, s71\n"
      "v_mfma_f32_16x16x32_bf16 v[12:15], v[104:107], a[52:55], v[12:15]\n"
      "v_mfma_f32_16x16x32_bf16 v[8:11], v[96:99], a[32:35], v[8:11]\n"
      "s_waitcnt vmcnt(24)\n"
      "v_mfma_f32_16x16x32_bf16 v[8:11], v[104:107], a[48:51], v[8:11]\n"
      "buffer_load_dwordx4 v[96:99], v80, s[60:63], s76 offen offset:1024 sc1\n"
      "v_mfma_f32_16x16x32_bf16 v[8:11], v[100:103], a[64:67], v[8:11]\n"
      "s_add_u32 s76, s77, s6\n"
      "s_mul_i32 s70, s70, 1152\n"
      "v_mfma_f32_16x16x32_bf16 v[8:11], v[108:111], a[80:83], v[8:11]\n"
      "v_mfma_f32_16x16x32_bf16 v[12:15], v[100:103], a[68:71], v[12:15]\n"
      "v_mfma_f32_16x16x32_bf16 v[12:15], v[108:111], a[84:87], v[12:15]\n"
      "buffer_load_dwordx4 v[104:107], v80, s[60:63], s76 offen offset:0 sc1\n"
      "v_mfma_f32_16x16x32_bf16 v[16:19], v[100:103], a[72:75], v[16:19]\n"
      "s_lshl_b32 s71, s34, 7\n"
      "v_mfma_f32_16x16x32_bf16 v[16:19], v[108:111], a[88:91], v[16:19]\n"
      "s_waitcnt vmcnt(23)\n"
      "v_mfma_f32_16x16x32_bf16 v[20:23], v[100:103], a[76:79], v[20:23]\n"
      "s_add_u32 s20, s70, s71\n"
      "v_mfma_f32_16x16x32_bf16 v[20:23], v[108:111], a[92:95], v[20:23]\n"
      "buffer_load_dwordx4 v[100:103], v80, s[60:63], s76 offen offset:1024 sc1\n"
      "v_mfma_f32_16x16x32_bf16 v[20:23], v[112:115], a[108:111], v[20:23]\n"
      "s_add_u32 s76, s77, s7\n"
      "s_waitcnt vmcnt(22)\n"
      "v_permlane16_swap_b32 v188, v190\n"
      "v_mfma_f32_16x16x32_bf16 v[20:23], v[120:123], a[124:127], v[20:23]\n"
      "s_add_u32 s18, s20, 18432\n"
      "v_permlane16_swap_b32 v189, v191\n"
      "v_mfma_f32_16x16x32_bf16 v[16:19], v[112:115], a[104:107], v[16:19]\n"
      "v_mfma_f32_16x16x32_bf16 v[16:19], v[120:123], a[120:123], v[16:19]\n"
      "buffer_load_dwordx4 v[108:111], v80, s[60:63], s76 offen offset:0 sc1\n"
      "v_mfma_f32_16x16x32_bf16 v[12:15], v[112:115], a[100:103], v[12:15]\n"
      "s_lshr_b32 s11, s7, 11\n"
      "s_waitcnt vmcnt(21)\n"
      "v_permlane16_swap_b32 v192, v194\n"
      "v_mfma_f32_16x16x32_bf16 v[12:15], v[120:123], a[116:119], v[12:15]\n"
      "s_and_b32 s70, s34, 3\n"
      "v_mfma_f32_16x16x32_bf16 v[8:11], v[112:115], a[96:99], v[8:11]\n"
      "v_permlane16_swap_b32 v193, v195\n"
      "v_mfma_f32_16x16x32_bf16 v[8:11], v[120:123], a[112:115], v[8:11]\n"
      "buffer_load_dwordx4 v[112:115], v80, s[60:63], s76 offen offset:1024 sc1\n"
      "v_mfma_f32_16x16x32_bf16 v[8:11], v[116:119], a[128:131], v[8:11]\n"
      "s_add_u32 s11, s11, 1\n"
      "s_add_u32 s76, s77, s8\n"
      "v_lshlrev_b32 v56, 16, v188\n"
      "v_mfma_f32_16x16x32_bf16 v[8:11], v[124:127], a[144:147], v[8:11]\n"
      "s_lshl2_add_u32 s70, s35, s70\n"
      "v_and_b32 v57, s16, v188\n"
      "v_mfma_f32_16x16x32_bf16 v[12:15], v[116:119], a[132:135], v[12:15]\n"
      "v_mul_f32 v56, s78, v56\n"
      "v_mfma_f32_16x16x32_bf16 v[12:15], v[124:127], a[148:151], v[12:15]\n"
      "buffer_load_dwordx4 v[120:123], v80, s[60:63], s76 offen offset:0 sc1\n"
      "v_mfma_f32_16x16x32_bf16 v[16:19], v[116:119], a[136:139], v[16:19]\n"
      "s_add_u32 s11, s34, s11\n"
      "v_mul_f32 v57, s78, v57\n"
      "s_lshl2_add_u32 s70, s70, s64\n"
      "v_mfma_f32_16x16x32_bf16 v[16:19], v[124:127], a[152:155], v[16:19]\n"
      "s_waitcnt vmcnt(21)\n"
      "v_lshlrev_b32 v58, 16, v189\n"
      "v_mfma_f32_16x16x32_bf16 v[20:23], v[116:119], a[140:143], v[20:23]\n"
      "s_lshr_b32 s71, s34, 2\n"
      "v_and_b32 v59, s16, v189\n"
      "v_mfma_f32_16x16x32_bf16 v[20:23], v[124:127], a[156:159], v[20:23]\n"
      "buffer_load_dwordx4 v[116:119], v80, s[60:63], s76 offen offset:1024 sc1\n"
      "v_mfma_f32_16x16x32_bf16 v[20:23], v[128:131], a[172:175], v[20:23]\n"
      "s_and_b32 s11, s11, 7\n"
      "s_add_u32 s76, s77, s9\n"
      "v_mul_f32 v58, s78, v58\n"
      "v_mfma_f32_16x16x32_bf16 v[20:23], v[136:139], a[188:191], v[20:23]\n"
      "s_lshl2_add_u32 s70, s70, s71\n"
      "v_mul_f32 v59, s78, v59\n"
      "v_lshlrev_b32 v60, 16, v190\n"
      "v_mfma_f32_16x16x32_bf16 v[16:19], v[128:131], a[168:171], v[16:19]\n"
      "v_and_b32 v61, s16, v190\n"
      "v_mfma_f32_16x16x32_bf16 v[16:19], v[136:139], a[184:187], v[16:19]\n"
      "buffer_load_dwordx4 v[124:127], v80, s[60:63], s76 offen offset:0 sc1\n"
      "v_mfma_f32_16x16x32_bf16 v[12:15], v[128:131], a[164:167], v[12:15]\n"
      "s_mul_i32 s11, s11, 77824\n"
      "v_mul_f32 v60, s78, v60\n"
      "s_mul_i32 s70, s70, 1152\n"
      "v_mfma_f32_16x16x32_bf16 v[12:15], v[136:139], a[180:183], v[12:15]\n"
      "v_mul_f32 v61, s78, v61\n"
      "v_mfma_f32_16x16x32_bf16 v[8:11], v[128:131], a[160:163], v[8:11]\n"
      "s_waitcnt vmcnt(21)\n"
      "v_lshlrev_b32 v62, 16, v191\n"
      "v_mfma_f32_16x16x32_bf16 v[8:11], v[136:139], a[176:179], v[8:11]\n"
      "buffer_load_dwordx4 v[128:131], v80, s[60:63], s76 offen offset:1024 sc1\n"
      "v_mfma_f32_16x16x32_bf16 v[8:11], v[132:135], a[192:195], v[8:11]\n"
      "s_add_u32 s11, s11, s79\n"
      "s_add_u32 s76, s77, s10\n"
      "s_lshr_b32 s22, s5, 11\n"
      "v_mfma_f32_16x16x32_bf16 v[8:11], v[140:143], a[208:211], v[8:11]\n"
      "s_add_u32 s22, s22, 1\n"
      "s_add_u32 s22, s34, s22\n"
      "s_and_b32 s22, s22, 7\n"
      "v_mfma_f32_16x16x32_bf16 v[12:15], v[132:135], a[196:199], v[12:15]\n"
      "s_lshl_b32 s22, s22, 16\n"
      "s_add_u32 s22, s22, s26\n"
      "v_and_b32 v63, s16, v191\n"
      "v_mfma_f32_16x16x32_bf16 v[12:15], v[140:143], a[212:215], v[12:15]\n"
      "buffer_load_dwordx4 v[136:139], v80, s[60:63], s76 offen offset:0 sc1\n"
      "v_mfma_f32_16x16x32_bf16 v[16:19], v[132:135], a[200:203], v[16:19]\n"
      "s_add_u32 s11, s11, 548864\n"
      "v_mul_f32 v62, s78, v62\n"
      "v_mul_f32 v63, s78, v63\n"
      "v_mfma_f32_16x16x32_bf16 v[16:19], v[140:143], a[216:219], v[16:19]\n"
      "s_add_u32 s21, s70, 1024\n"
      "v_lshlrev_b32 v64, 16, v192\n"
      "v_and_b32 v65, s16, v192\n"
      "v_mfma_f32_16x16x32_bf16 v[20:23], v[132:135], a[204:207], v[20:23]\n"
      "s_lshl_b32 s70, s64, 1\n"
      "v_mul_f32 v64, s78, v64\n"
      "v_mul_f32 v65, s78, v65\n"
      "v_mfma_f32_16x16x32_bf16 v[20:23], v[140:143], a[220:223], v[20:23]\n"
      "buffer_load_dwordx4 v[132:135], v80, s[60:63], s76 offen offset:1024 sc1\n"
      "s_waitcnt vmcnt(23)\n"
      "v_mfma_f32_16x16x32_bf16 v[20:23], v[144:147], a[236:239], v[20:23]\n"
      "s_mov_b32 s77, s11\n"
      "s_sub_u32 s76, s77, 4096\n"
      "v_lshlrev_b32 v66, 16, v193\n"
      "s_waitcnt vmcnt(22)\n"
      "v_mfma_f32_16x16x32_bf16 v[20:23], v[152:155], a[252:255], v[20:23]\n"
      "v_and_b32 v67, s16, v193\n"
      "v_lshrrev_b32 v91, 5, v1\n"
      "v_mfma_f32_16x16x32_bf16 v[16:19], v[144:147], a[232:235], v[16:19]\n"
      "v_mul_f32 v66, s78, v66\n"
      "v_add_u32 v91, s70, v91\n"
      "v_mfma_f32_16x16x32_bf16 v[16:19], v[152:155], a[248:251], v[16:19]\n"
      "buffer_load_dwordx4 v[140:143], v80, s[60:63], s76 offen offset:0 sc1\n"
      "v_mfma_f32_16x16x32_bf16 v[12:15], v[144:147], a[228:231], v[12:15]\n"
      "v_mul_f32 v67, s78, v67\n"
      "v_lshlrev_b32 v68, 16, v194\n"
      "v_mfma_f32_16x16x32_bf16 v[12:15], v[152:155], a[244:247], v[12:15]\n"
      "v_and_b32 v69, s16, v194\n"
      "v_and_b32 v87, 7, v1\n"
      "v_mfma_f32_16x16x32_bf16 v[8:11], v[144:147], a[224:227], v[8:11]\n"
      "s_waitcnt vmcnt(21)\n"
      "v_mul_f32 v68, s78, v68\n"
      "v_mfma_f32_16x16x32_bf16 v[8:11], v[152:155], a[240:243], v[8:11]\n"
      "buffer_load_dwordx4 v[144:147], v80, s[60:63], s76 offen offset:1024 sc1\n"
      "v_mfma_f32_16x16x32_bf16 v[8:11], v[148:151], v[220:223], v[8:11]\n"
      "v_mul_f32 v69, s78, v69\n"
      "v_lshlrev_b32 v84, 7, v87\n"
      "v_mfma_f32_16x16x32_bf16 v[8:11], v[156:159], v[236:239], v[8:11]\n"
      "v_lshlrev_b32 v70, 16, v195\n"
      "v_mfma_f32_16x16x32_bf16 v[12:15], v[148:151], v[224:227], v[12:15]\n"
      "v_and_b32 v71, s16, v195\n"
      "v_mfma_f32_16x16x32_bf16 v[12:15], v[156:159], v[240:243], v[12:15]\n"
      "buffer_load_dwordx4 v[152:155], v80, s[60:63], s76 offen offset:2048 sc1\n"
      "v_mfma_f32_16x16x32_bf16 v[16:19], v[148:151], v[228:231], v[16:19]\n"
      "v_mul_f32 v70, s78, v70\n"
      "v_mul_f32 v71, s78, v71\n"
      "v_mfma_f32_16x16x32_bf16 v[16:19], v[156:159], v[244:247], v[16:19]\n"
      "v_lshl_add_u32 v84, v91, 4, v84\n"
      "v_mfma_f32_16x16x32_bf16 v[20:23], v[148:151], v[232:235], v[20:23]\n"
      "s_waitcnt vmcnt(21)\n"
      "v_add_u32 v84, 86016, v84\n"
      "v_mfma_f32_16x16x32_bf16 v[20:23], v[156:159], v[248:251], v[20:23]\n"
      "buffer_load_dwordx4 v[148:151], v80, s[60:63], s76 offen offset:3072 sc1\n"
      "v_mfma_f32_16x16x32_bf16 v[24:27], v[160:163], a[0:3], 0\n"
      "s_add_u32 s76, s77, s4\n"
      "v_mfma_f32_16x16x32_bf16 v[24:27], v[168:171], a[16:19], v[24:27]\n"
      "v_cvt_pk_bf16_f32 v72, v8, v9\n"
      "v_cvt_pk_bf16_f32 v73, v10, v11\n"
      "v_mfma_f32_16x16x32_bf16 v[28:31], v[160:163], a[4:7], 0\n"
      "v_cvt_pk_bf16_f32 v74, v12, v13\n"
      "v_cvt_pk_bf16_f32 v75, v14, v15\n"
      "v_mfma_f32_16x16x32_bf16 v[28:31], v[168:171], a[20:23], v[28:31]\n"
      "buffer_load_dwordx4 v[156:159], v80, s[60:63], s76 offen offset:0 sc1\n"
      "v_mfma_f32_16x16x32_bf16 v[32:35], v[160:163], a[8:11], 0\n"
      "buffer_store_dwordx4 v[72:75], v80, s[60:63], s22 offen offset:0 sc1\n"
      "v_mfma_f32_16x16x32_bf16 v[32:35], v[168:171], a[24:27], v[32:35]\n"
      "v_cvt_pk_bf16_f32 v76, v16, v17\n"
      "v_cvt_pk_bf16_f32 v77, v18, v19\n"
      "v_mfma_f32_16x16x32_bf16 v[36:39], v[160:163], a[12:15], 0\n"
      "s_waitcnt vmcnt(22)\n"
      "v_cvt_pk_bf16_f32 v78, v20, v21\n"
      "v_cvt_pk_bf16_f32 v79, v22, v23\n"
      "v_mfma_f32_16x16x32_bf16 v[36:39], v[168:171], a[28:31], v[36:39]\n"
      "buffer_load_dwordx4 v[160:163], v80, s[60:63], s76 offen offset:1024 sc1\n"
      "v_mfma_f32_16x16x32_bf16 v[36:39], v[164:167], a[44:47], v[36:39]\n"
      "s_add_u32 s76, s77, s5\n"
      "v_xor_b32 v91, v91, v87\n"
      "v_and_b32 v81, 31, v1\n"
      "v_mfma_f32_16x16x32_bf16 v[36:39], v[172:175], a[60:63], v[36:39]\n"
      "buffer_store_dwordx4 v[76:79], v80, s[60:63], s22 offen offset:1024 sc1\n"
      "v_mfma_f32_16x16x32_bf16 v[32:35], v[164:167], a[40:43], v[32:35]\n"
      "v_lshlrev_b32 v81, 7, v81\n"
      "v_mfma_f32_16x16x32_bf16 v[32:35], v[172:175], a[56:59], v[32:35]\n"
      "buffer_load_dwordx4 v[168:171], v80, s[60:63], s76 offen offset:0 sc1\n"
      "v_mfma_f32_16x16x32_bf16 v[28:31], v[164:167], a[36:39], v[28:31]\n"
      "v_lshl_add_u32 v81, v91, 4, v81\n"
      "v_mfma_f32_16x16x32_bf16 v[28:31], v[172:175], a[52:55], v[28:31]\n"
      "v_mfma_f32_16x16x32_bf16 v[24:27], v[164:167], a[32:35], v[24:27]\n"
      "s_waitcnt vmcnt(22)\n"
      "v_add_u32 v81, 73728, v81\n"
      "v_mfma_f32_16x16x32_bf16 v[24:27], v[172:175], a[48:51], v[24:27]\n"
      "buffer_load_dwordx4 v[164:167], v80, s[60:63], s76 offen offset:1024 sc1\n"
      "v_mfma_f32_16x16x32_bf16 v[24:27], v[176:179], a[64:67], v[24:27]\n"
      "s_add_u32 s76, s77, s6\n"
      "v_mfma_f32_16x16x32_bf16 v[24:27], v[184:187], a[80:83], v[24:27]\n"
      "v_lshrrev_b32 v91, 3, v1\n"
      "v_mfma_f32_16x16x32_bf16 v[28:31], v[176:179], a[68:71], v[28:31]\n"
      "v_mfma_f32_16x16x32_bf16 v[28:31], v[184:187], a[84:87], v[28:31]\n"
      "buffer_load_dwordx4 v[172:175], v80, s[60:63], s76 offen offset:0 sc1\n"
      "v_mfma_f32_16x16x32_bf16 v[32:35], v[176:179], a[72:75], v[32:35]\n"
      "v_mul_u32_u24 v83, 2304, v91\n"
      "v_mfma_f32_16x16x32_bf16 v[32:35], v[184:187], a[88:91], v[32:35]\n"
      "v_mfma_f32_16x16x32_bf16 v[36:39], v[176:179], a[76:79], v[36:39]\n"
      "s_waitcnt vmcnt(21)\n"
      "v_mfma_f32_16x16x32_bf16 v[36:39], v[184:187], a[92:95], v[36:39]\n"
      "buffer_load_dwordx4 v[176:179], v80, s[60:63], s76 offen offset:1024 sc1\n"
      "v_mfma_f32_16x16x32_bf16 v[36:39], v[180:183], a[108:111], v[36:39]\n"
      "s_add_u32 s76, s77, s7\n"
      "v_lshl_add_u32 v83, v87, 4, v83\n"
      "v_mfma_f32_16x16x32_bf16 v[36:39], v[96:99], a[124:127], v[36:39]\n"
      "v_mfma_f32_16x16x32_bf16 v[32:35], v[180:183], a[104:107], v[32:35]\n"
      "v_mfma_f32_16x16x32_bf16 v[32:35], v[96:99], a[120:123], v[32:35]\n"
      "buffer_load_dwordx4 v[184:187], v80, s[60:63], s76 offen offset:0 sc1\n"
      "v_mfma_f32_16x16x32_bf16 v[28:31], v[180:183], a[100:103], v[28:31]\n"
      "s_lshr_b32 s11, s8, 11\n"
      "v_xor_b32 v87, v87, v91\n"
      "v_mfma_f32_16x16x32_bf16 v[28:31], v[96:99], a[116:119], v[28:31]\n"
      "v_mfma_f32_16x16x32_bf16 v[24:27], v[180:183], a[96:99], v[24:27]\n"
      "s_waitcnt vmcnt(21)\n"
      "v_lshlrev_b32 v91, 7, v91\n"
      "v_mfma_f32_16x16x32_bf16 v[24:27], v[96:99], a[112:115], v[24:27]\n"
      "buffer_load_dwordx4 v[180:183], v80, s[60:63], s76 offen offset:1024 sc1\n"
      "v_mfma_f32_16x16x32_bf16 v[24:27], v[104:107], a[128:131], v[24:27]\n"
      "s_add_u32 s11, s11, 1\n"
      "s_add_u32 s76, s77, s8\n"
      "v_mfma_f32_16x16x32_bf16 v[24:27], v[100:103], a[144:147], v[24:27]\n"
      "v_lshl_add_u32 v91, v87, 4, v91\n"
      "v_mfma_f32_16x16x32_bf16 v[28:31], v[104:107], a[132:135], v[28:31]\n"
      "v_mfma_f32_16x16x32_bf16 v[28:31], v[100:103], a[148:151], v[28:31]\n"
      "buffer_load_dwordx4 v[96:99], v80, s[60:63], s76 offen offset:0 sc1\n"
      "v_mfma_f32_16x16x32_bf16 v[32:35], v[104:107], a[136:139], v[32:35]\n"
      "s_add_u32 s11, s34, s11\n"
      "s_lshl_b32 s70, s64, 11\n"
      "v_mfma_f32_16x16x32_bf16 v[32:35], v[100:103], a[152:155], v[32:35]\n"
      "v_mfma_f32_16x16x32_bf16 v[36:39], v[104:107], a[140:143], v[36:39]\n"
      "s_waitcnt vmcnt(21)\n"
      "v_mfma_f32_16x16x32_bf16 v[36:39], v[100:103], a[156:159], v[36:39]\n"
      "buffer_load_dwordx4 v[104:107], v80, s[60:63], s76 offen offset:1024 sc1\n"
      "v_mfma_f32_16x16x32_bf16 v[36:39], v[108:111], a[172:175], v[36:39]\n"
      "s_and_b32 s11, s11, 7\n"
      "s_add_u32 s76, s77, s9\n"
      "s_add_u32 s70, s70, 73728\n"
      "v_mfma_f32_16x16x32_bf16 v[36:39], v[112:115], a[188:191], v[36:39]\n"
      "v_mfma_f32_16x16x32_bf16 v[32:35], v[108:111], a[168:171], v[32:35]\n"
      "v_mfma_f32_16x16x32_bf16 v[32:35], v[112:115], a[184:187], v[32:35]\n"
      "buffer_load_dwordx4 v[100:103], v80, s[60:63], s76 offen offset:0 sc1\n"
      "v_mfma_f32_16x16x32_bf16 v[28:31], v[108:111], a[164:167], v[28:31]\n"
      "s_mul_i32 s11, s11, 77824\n"
      "v_add_u32 v82, s70, v91\n"
      "v_mfma_f32_16x16x32_bf16 v[28:31], v[112:115], a[180:183], v[28:31]\n"
      "s_waitcnt vmcnt(21)\n"
      "v_mfma_f32_16x16x32_bf16 v[24:27], v[108:111], a[160:163], v[24:27]\n"
      "s_lshl_b32 s70, s64, 8\n"
      "v_mfma_f32_16x16x32_bf16 v[24:27], v[112:115], a[176:179], v[24:27]\n"
      "buffer_load_dwordx4 v[108:111], v80, s[60:63], s76 offen offset:1024 sc1\n"
      "v_mfma_f32_16x16x32_bf16 v[24:27], v[120:123], a[192:195], v[24:27]\n"
      "s_add_u32 s11, s11, s79\n"
      "s_add_u32 s76, s77, s10\n"
      "s_lshr_b32 s22, s6, 11\n"
      "v_mfma_f32_16x16x32_bf16 v[24:27], v[116:119], a[208:211], v[24:27]\n"
      "s_add_u32 s22, s22, 1\n"
      "s_add_u32 s22, s34, s22\n"
      "s_and_b32 s22, s22, 7\n"
      "v_mfma_f32_16x16x32_bf16 v[28:31], v[120:123], a[196:199], v[28:31]\n"
      "s_lshl_b32 s22, s22, 16\n"
      "s_add_u32 s22, s22, s26\n"
      "s_add_u32 s70, s70, 86016\n"
      "v_mfma_f32_16x16x32_bf16 v[28:31], v[116:119], a[212:215], v[28:31]\n"
      "buffer_load_dwordx4 v[112:115], v80, s[60:63], s76 offen offset:0 sc1\n"
      "v_mfma_f32_16x16x32_bf16 v[32:35], v[120:123], a[200:203], v[32:35]\n"
      "s_add_u32 s11, s11, 548864\n"
      "v_add_u32 v85, s70, v80\n"
      "v_mfma_f32_16x16x32_bf16 v[32:35], v[116:119], a[216:219], v[32:35]\n"
      "v_mfma_f32_16x16x32_bf16 v[36:39], v[120:123], a[204:207], v[36:39]\n"
      "s_waitcnt vmcnt(21)\n"
      "v_mfma_f32_16x16x32_bf16 v[36:39], v[116:119], a[220:223], v[36:39]\n"
      "buffer_load_dwordx4 v[120:123], v80, s[60:63], s76 offen offset:1024 sc1\n"
      "v_mfma_f32_16x16x32_bf16 v[36:39], v[124:127], a[236:239], v[36:39]\n"
      "s_mov_b32 s77, s11\n"
      "s_sub_u32 s76, s77, 4096\n"
      "v_mfma_f32_16x16x32_bf16 v[36:39], v[128:131], a[252:255], v[36:39]\n"
      "v_mfma_f32_16x16x32_bf16 v[32:35], v[124:127], a[232:235], v[32:35]\n"
      "v_mfma_f32_16x16x32_bf16 v[32:35], v[128:131], a[248:251], v[32:35]\n"
      "buffer_load_dwordx4 v[116:119], v80, s[60:63], s76 offen offset:0 sc1\n"
      "v_mfma_f32_16x16x32_bf16 v[28:31], v[124:127], a[228:231], v[28:31]\n"
      "v_mfma_f32_16x16x32_bf16 v[28:31], v[128:131], a[244:247], v[28:31]\n"
      "v_mfma_f32_16x16x32_bf16 v[24:27], v[124:127], a[224:227], v[24:27]\n"
      "s_waitcnt vmcnt(21)\n"
      "v_mfma_f32_16x16x32_bf16 v[24:27], v[128:131], a[240:243], v[24:27]\n"
      "buffer_load_dwordx4 v[124:127], v80, s[60:63], s76 offen offset:1024 sc1\n"
      "v_mfma_f32_16x16x32_bf16 v[24:27], v[136:139], v[220:223], v[24:27]\n"
      "v_mfma_f32_16x16x32_bf16 v[24:27], v[132:135], v[236:239], v[24:27]\n"
      "v_mfma_f32_16x16x32_bf16 v[28:31], v[136:139], v[224:227], v[28:31]\n"
      "v_mov_b32 v6, 3\n"
      "s_waitcnt vmcnt(14)\n"
      "v_mfma_f32_16x16x32_bf16 v[28:31], v[132:135], v[240:243], v[28:31]\n"
      "buffer_load_dwordx4 v[128:131], v80, s[60:63], s76 offen offset:2048 sc1\n"
      "v_mfma_f32_16x16x32_bf16 v[32:35], v[136:139], v[228:231], v[32:35]\n"
      "s_mov_b64 exec, 1\n"
      "buffer_store_dword v6, off, s[60:63], s30 sc1\n"
      "s_mov_b64 exec, -1\n"
      "v_mfma_f32_16x16x32_bf16 v[32:35], v[132:135], v[244:247], v[32:35]\n"
      "v_mfma_f32_16x16x32_bf16 v[36:39], v[136:139], v[232:235], v[36:39]\n"
      "v_mfma_f32_16x16x32_bf16 v[36:39], v[132:135], v[248:251], v[36:39]\n"
      "buffer_load_dwordx4 v[136:139], v80, s[60:63], s76 offen offset:3072 sc1\n"
      "v_mfma_f32_16x16x32_bf16 v[8:11], v[140:143], a[0:3], 0\n"
      "s_add_u32 s76, s77, s4\n"
      "v_mfma_f32_16x16x32_bf16 v[8:11], v[144:147], a[16:19], v[8:11]\n"
      "v_cvt_pk_bf16_f32 v72, v24, v25\n"
      "v_cvt_pk_bf16_f32 v73, v26, v27\n"
      "v_mfma_f32_16x16x32_bf16 v[12:15], v[140:143], a[4:7], 0\n"
      "v_cvt_pk_bf16_f32 v74, v28, v29\n"
      "v_cvt_pk_bf16_f32 v75, v30, v31\n"
      "v_mfma_f32_16x16x32_bf16 v[12:15], v[144:147], a[20:23], v[12:15]\n"
      "buffer_load_dwordx4 v[132:135], v80, s[60:63], s76 offen offset:0 sc1\n"
      "v_mfma_f32_16x16x32_bf16 v[16:19], v[140:143], a[8:11], 0\n"
      "buffer_store_dwordx4 v[72:75], v80, s[60:63], s22 offen offset:0 sc1\n"
      "v_mfma_f32_16x16x32_bf16 v[16:19], v[144:147], a[24:27], v[16:19]\n"
      "v_cvt_pk_bf16_f32 v76, v32, v33\n"
      "v_cvt_pk_bf16_f32 v77, v34, v35\n"
      "v_mfma_f32_16x16x32_bf16 v[20:23], v[140:143], a[12:15], 0\n"
      "v_cvt_pk_bf16_f32 v78, v36, v37\n"
      "v_cvt_pk_bf16_f32 v79, v38, v39\n"
      "v_mfma_f32_16x16x32_bf16 v[20:23], v[144:147], a[28:31], v[20:23]\n"
      "buffer_load_dwordx4 v[140:143], v80, s[60:63], s76 offen offset:1024 sc1\n"
      "v_mfma_f32_16x16x32_bf16 v[20:23], v[152:155], a[44:47], v[20:23]\n"
      "s_add_u32 s76, s77, s5\n"
      "v_mfma_f32_16x16x32_bf16 v[20:23], v[148:151], a[60:63], v[20:23]\n"
      "buffer_store_dwordx4 v[76:79], v80, s[60:63], s22 offen offset:1024 sc1\n"
      "v_mfma_f32_16x16x32_bf16 v[16:19], v[152:155], a[40:43], v[16:19]\n"
      "v_mfma_f32_16x16x32_bf16 v[16:19], v[148:151], a[56:59], v[16:19]\n"
      "buffer_load_dwordx4 v[144:147], v80, s[60:63], s76 offen offset:0 sc1\n"
      "v_mfma_f32_16x16x32_bf16 v[12:15], v[152:155], a[36:39], v[12:15]\n"
      "v_mfma_f32_16x16x32_bf16 v[12:15], v[148:151], a[52:55], v[12:15]\n"
      "v_mfma_f32_16x16x32_bf16 v[8:11], v[152:155], a[32:35], v[8:11]\n"
      "v_mfma_f32_16x16x32_bf16 v[8:11], v[148:151], a[48:51], v[8:11]\n"
      "buffer_load_dwordx4 v[152:155], v80, s[60:63], s76 offen offset:1024 sc1\n"
      "v_mfma_f32_16x16x32_bf16 v[8:11], v[156:159], a[64:67], v[8:11]\n"
      "s_add_u32 s76, s77, s6\n"
      "v_mfma_f32_16x16x32_bf16 v[8:11], v[160:163], a[80:83], v[8:11]\n"
      "v_mfma_f32_16x16x32_bf16 v[12:15], v[156:159], a[68:71], v[12:15]\n"
      "v_mfma_f32_16x16x32_bf16 v[12:15], v[160:163], a[84:87], v[12:15]\n"
      "buffer_load_dwordx4 v[148:151], v80, s[60:63], s76 offen offset:0 sc1\n"
      "v_mfma_f32_16x16x32_bf16 v[16:19], v[156:159], a[72:75], v[16:19]\n"
      "v_mfma_f32_16x16x32_bf16 v[16:19], v[160:163], a[88:91], v[16:19]\n"
      "v_mfma_f32_16x16x32_bf16 v[20:23], v[156:159], a[76:79], v[20:23]\n"
      "s_waitcnt vmcnt(22)\n"
      "v_mfma_f32_16x16x32_bf16 v[20:23], v[160:163], a[92:95], v[20:23]\n"
      "buffer_load_dwordx4 v[156:159], v80, s[60:63], s76 offen offset:1024 sc1\n"
      "v_mfma_f32_16x16x32_bf16 v[20:23], v[168:171], a[108:111], v[20:23]\n"
      "s_add_u32 s76, s77, s7\n"
      "v_mfma_f32_16x16x32_bf16 v[20:23], v[164:167], a[124:127], v[20:23]\n"
      "v_mfma_f32_16x16x32_bf16 v[16:19], v[168:171], a[104:107], v[16:19]\n"
      "v_mfma_f32_16x16x32_bf16 v[16:19], v[164:167], a[120:123], v[16:19]\n"
      "buffer_load_dwordx4 v[160:163], v80, s[60:63], s76 offen offset:0 sc1\n"
      "v_mfma_f32_16x16x32_bf16 v[12:15], v[168:171], a[100:103], v[12:15]\n"
      "s_lshr_b32 s11, s9, 11\n"
      "v_mfma_f32_16x16x32_bf16 v[12:15], v[164:167], a[116:119], v[12:15]\n"
      "v_mfma_f32_16x16x32_bf16 v[8:11], v[168:171], a[96:99], v[8:11]\n"
      "s_waitcnt vmcnt(22)\n"
      "v_mfma_f32_16x16x32_bf16 v[8:11], v[164:167], a[112:115], v[8:11]\n"
      "buffer_load_dwordx4 v[168:171], v80, s[60:63], s76 offen offset:1024 sc1\n"
      "v_mfma_f32_16x16x32_bf16 v[8:11], v[172:175], a[128:131], v[8:11]\n"
      "s_add_u32 s11, s11, 1\n"
      "s_add_u32 s76, s77, s8\n"
      "v_mfma_f32_16x16x32_bf16 v[8:11], v[176:179], a[144:147], v[8:11]\n"
      "v_mfma_f32_16x16x32_bf16 v[12:15], v[172:175], a[132:135], v[12:15]\n"
      "v_mfma_f32_16x16x32_bf16 v[12:15], v[176:179], a[148:151], v[12:15]\n"
      "buffer_load_dwordx4 v[164:167], v80, s[60:63], s76 offen offset:0 sc1\n"
      "v_mfma_f32_16x16x32_bf16 v[16:19], v[172:175], a[136:139], v[16:19]\n"
      "s_add_u32 s11, s34, s11\n"
      "v_mfma_f32_16x16x32_bf16 v[16:19], v[176:179], a[152:155], v[16:19]\n"
      "v_mfma_f32_16x16x32_bf16 v[20:23], v[172:175], a[140:143], v[20:23]\n"
      "s_waitcnt vmcnt(22)\n"
      "v_mfma_f32_16x16x32_bf16 v[20:23], v[176:179], a[156:159], v[20:23]\n"
      "buffer_load_dwordx4 v[172:175], v80, s[60:63], s76 offen offset:1024 sc1\n"
      "v_mfma_f32_16x16x32_bf16 v[20:23], v[184:187], a[172:175], v[20:23]\n"
      "s_and_b32 s11, s11, 7\n"
      "s_add_u32 s76, s77, s9\n"
      "v_mfma_f32_16x16x32_bf16 v[20:23], v[180:183], a[188:191], v[20:23]\n"
      "v_mfma_f32_16x16x32_bf16 v[16:19], v[184:187], a[168:171], v[16:19]\n"
      "v_mfma_f32_16x16x32_bf16 v[16:19], v[180:183], a[184:187], v[16:19]\n"
      "buffer_load_dwordx4 v[176:179], v80, s[60:63], s76 offen offset:0 sc1\n"
      "v_mfma_f32_16x16x32_bf16 v[12:15], v[184:187], a[164:167], v[12:15]\n"
      "s_mul_i32 s11, s11, 77824\n"
      "v_mfma_f32_16x16x32_bf16 v[12:15], v[180:183], a[180:183], v[12:15]\n"
      "v_mfma_f32_16x16x32_bf16 v[8:11], v[184:187], a[160:163], v[8:11]\n"
      "s_waitcnt vmcnt(22)\n"
      "v_mfma_f32_16x16x32_bf16 v[8:11], v[180:183], a[176:179], v[8:11]\n"
      "buffer_load_dwordx4 v[184:187], v80, s[60:63], s76 offen offset:1024 sc1\n"
      "v_mfma_f32_16x16x32_bf16 v[8:11], v[96:99], a[192:195], v[8:11]\n"
      "s_add_u32 s11, s11, s79\n"
      "s_add_u32 s76, s77, s10\n"
      "s_lshr_b32 s22, s7, 11\n"
      "v_mfma_f32_16x16x32_bf16 v[8:11], v[104:107], a[208:211], v[8:11]\n"
      "s_add_u32 s22, s22, 1\n"
      "s_add_u32 s22, s34, s22\n"
      "s_and_b32 s22, s22, 7\n"
      "v_mfma_f32_16x16x32_bf16 v[12:15], v[96:99], a[196:199], v[12:15]\n"
      "s_lshl_b32 s22, s22, 16\n"
      "s_add_u32 s22, s22, s26\n"
      "v_mfma_f32_16x16x32_bf16 v[12:15], v[104:107], a[212:215], v[12:15]\n"
      "buffer_load_dwordx4 v[180:183], v80, s[60:63], s76 offen offset:0 sc1\n"
      "v_mfma_f32_16x16x32_bf16 v[16:19], v[96:99], a[200:203], v[16:19]\n"
      "s_add_u32 s11, s11, 548864\n"
      "v_mfma_f32_16x16x32_bf16 v[16:19], v[104:107], a[216:219], v[16:19]\n"
      "v_mfma_f32_16x16x32_bf16 v[20:23], v[96:99], a[204:207], v[20:23]\n"
      "s_waitcnt vmcnt(22)\n"
      "v_mfma_f32_16x16x32_bf16 v[20:23], v[104:107], a[220:223], v[20:23]\n"
      "buffer_load_dwordx4 v[96:99], v80, s[60:63], s76 offen offset:1024 sc1\n"
      "v_mfma_f32_16x16x32_bf16 v[20:23], v[100:103], a[236:239], v[20:23]\n"
      "s_mov_b32 s77, s11\n"
      "s_sub_u32 s76, s77, 4096\n"
      "v_mfma_f32_16x16x32_bf16 v[20:23], v[108:111], a[252:255], v[20:23]\n"
      "v_mfma_f32_16x16x32_bf16 v[16:19], v[100:103], a[232:235], v[16:19]\n"
      "v_mfma_f32_16x16x32_bf16 v[16:19], v[108:111], a[248:251], v[16:19]\n"
      "buffer_load_dwordx4 v[104:107], v80, s[60:63], s76 offen offset:0 sc1\n"
      "v_mfma_f32_16x16x32_bf16 v[12:15], v[100:103], a[228:231], v[12:15]\n"
      "v_mfma_f32_16x16x32_bf16 v[12:15], v[108:111], a[244:247], v[12:15]\n"
      "v_mfma_f32_16x16x32_bf16 v[8:11], v[100:103], a[224:227], v[8:11]\n"
      "s_waitcnt vmcnt(22)\n"
      "v_mfma_f32_16x16x32_bf16 v[8:11], v[108:111], a[240:243], v[8:11]\n"
      "buffer_load_dwordx4 v[100:103], v80, s[60:63], s76 offen offset:1024 sc1\n"
      "v_mfma_f32_16x16x32_bf16 v[8:11], v[112:115], v[220:223], v[8:11]\n"
      "v_mfma_f32_16x16x32_bf16 v[8:11], v[120:123], v[236:239], v[8:11]\n"
      "v_mfma_f32_16x16x32_bf16 v[12:15], v[112:115], v[224:227], v[12:15]\n"
      "v_mfma_f32_16x16x32_bf16 v[12:15], v[120:123], v[240:243], v[12:15]\n"
      "buffer_load_dwordx4 v[108:111], v80, s[60:63], s76 offen offset:2048 sc1\n"
      "v_mfma_f32_16x16x32_bf16 v[16:19], v[112:115], v[228:231], v[16:19]\n"
      "v_mfma_f32_16x16x32_bf16 v[16:19], v[120:123], v[244:247], v[16:19]\n"
      "v_mfma_f32_16x16x32_bf16 v[20:23], v[112:115], v[232:235], v[20:23]\n"
      "s_waitcnt vmcnt(22)\n"
      "v_mfma_f32_16x16x32_bf16 v[20:23], v[120:123], v[248:251], v[20:23]\n"
      "buffer_load_dwordx4 v[112:115], v80, s[60:63], s76 offen offset:3072 sc1\n"
      "v_mfma_f32_16x16x32_bf16 v[24:27], v[116:119], a[0:3], 0\n"
      "s_add_u32 s76, s77, s4\n"
      "v_mfma_f32_16x16x32_bf16 v[24:27], v[124:127], a[16:19], v[24:27]\n"
      "v_cvt_pk_bf16_f32 v72, v8, v9\n"
      "v_cvt_pk_bf16_f32 v73, v10, v11\n"
      "v_mfma_f32_16x16x32_bf16 v[28:31], v[116:119], a[4:7], 0\n"
      "v_cvt_pk_bf16_f32 v74, v12, v13\n"
      "v_cvt_pk_bf16_f32 v75, v14, v15\n"
      "v_mfma_f32_16x16x32_bf16 v[28:31], v[124:127], a[20:23], v[28:31]\n"
      "buffer_load_dwordx4 v[120:123], v80, s[60:63], s76 offen offset:0 sc1\n"
      "v_mfma_f32_16x16x32_bf16 v[32:35], v[116:119], a[8:11], 0\n"
      "buffer_store_dwordx4 v[72:75], v80, s[60:63], s22 offen offset:0 sc1\n"
      "v_mfma_f32_16x16x32_bf16 v[32:35], v[124:127], a[24:27], v[32:35]\n"
      "v_cvt_pk_bf16_f32 v76, v16, v17\n"
      "v_cvt_pk_bf16_f32 v77, v18, v19\n"
      "v_mfma_f32_16x16x32_bf16 v[36:39], v[116:119], a[12:15], 0\n"
      "s_waitcnt vmcnt(22)\n"
      "v_cvt_pk_bf16_f32 v78, v20, v21\n"
      "v_cvt_pk_bf16_f32 v79, v22, v23\n"
      "v_mfma_f32_16x16x32_bf16 v[36:39], v[124:127], a[28:31], v[36:39]\n"
      "buffer_load_dwordx4 v[116:119], v80, s[60:63], s76 offen offset:1024 sc1\n"
      "v_mfma_f32_16x16x32_bf16 v[36:39], v[128:131], a[44:47], v[36:39]\n"
      "s_add_u32 s76, s77, s5\n"
      "v_mfma_f32_16x16x32_bf16 v[36:39], v[136:139], a[60:63], v[36:39]\n"
      "buffer_store_dwordx4 v[76:79], v80, s[60:63], s22 offen offset:1024 sc1\n"
      "v_mfma_f32_16x16x32_bf16 v[32:35], v[128:131], a[40:43], v[32:35]\n"
      "v_mfma_f32_16x16x32_bf16 v[32:35], v[136:139], a[56:59], v[32:35]\n"
      "buffer_load_dwordx4 v[124:127], v80, s[60:63], s76 offen offset:0 sc1\n"
      "v_mfma_f32_16x16x32_bf16 v[28:31], v[128:131], a[36:39], v[28:31]\n"
      "v_mfma_f32_16x16x32_bf16 v[28:31], v[136:139], a[52:55], v[28:31]\n"
      "v_mfma_f32_16x16x32_bf16 v[24:27], v[128:131], a[32:35], v[24:27]\n"
      "s_waitcnt vmcnt(22)\n"
      "v_mfma_f32_16x16x32_bf16 v[24:27], v[136:139], a[48:51], v[24:27]\n"
      "buffer_load_dwordx4 v[128:131], v80, s[60:63], s76 offen offset:1024 sc1\n"
      "v_mfma_f32_16x16x32_bf16 v[24:27], v[132:135], a[64:67], v[24:27]\n"
      "s_add_u32 s76, s77, s6\n"
      "v_mfma_f32_16x16x32_bf16 v[24:27], v[140:143], a[80:83], v[24:27]\n"
      "v_mfma_f32_16x16x32_bf16 v[28:31], v[132:135], a[68:71], v[28:31]\n"
      "v_mfma_f32_16x16x32_bf16 v[28:31], v[140:143], a[84:87], v[28:31]\n"
      "buffer_load_dwordx4 v[136:139], v80, s[60:63], s76 offen offset:0 sc1\n"
      "v_mfma_f32_16x16x32_bf16 v[32:35], v[132:135], a[72:75], v[32:35]\n"
      "v_mfma_f32_16x16x32_bf16 v[32:35], v[140:143], a[88:91], v[32:35]\n"
      "v_mfma_f32_16x16x32_bf16 v[36:39], v[132:135], a[76:79], v[36:39]\n"
      "s_waitcnt vmcnt(21)\n"
      "v_mfma_f32_16x16x32_bf16 v[36:39], v[140:143], a[92:95], v[36:39]\n"
      "buffer_load_dwordx4 v[132:135], v80, s[60:63], s76 offen offset:1024 sc1\n"
      "v_mfma_f32_16x16x32_bf16 v[36:39], v[144:147], a[108:111], v[36:39]\n"
      "s_add_u32 s76, s77, s7\n"
      "v_mfma_f32_16x16x32_bf16 v[36:39], v[152:155], a[124:127], v[36:39]\n"
      "v_mfma_f32_16x16x32_bf16 v[32:35], v[144:147], a[104:107], v[32:35]\n"
      "v_mfma_f32_16x16x32_bf16 v[32:35], v[152:155], a[120:123], v[32:35]\n"
      "buffer_load_dwordx4 v[140:143], v80, s[60:63], s76 offen offset:0 sc1\n"
      "v_mfma_f32_16x16x32_bf16 v[28:31], v[144:147], a[100:103], v[28:31]\n"
      "s_lshr_b32 s11, s10, 11\n"
      "v_mfma_f32_16x16x32_bf16 v[28:31], v[152:155], a[116:119], v[28:31]\n"
      "s_waitcnt vmcnt(21)\n"
      "v_mfma_f32_16x16x32_bf16 v[24:27], v[144:147], a[96:99], v[24:27]\n"
      "s_mov_b64 exec, 255\n"
      "buffer_load_dword v7, v5, s[60:63], s31 offen sc1\n"
      "s_mov_b64 exec, -1\n"
      "v_mfma_f32_16x16x32_bf16 v[24:27], v[152:155], a[112:115], v[24:27]\n"
      "buffer_load_dwordx4 v[144:147], v80, s[60:63], s76 offen offset:1024 sc1\n"
      "v_mfma_f32_16x16x32_bf16 v[24:27], v[148:151], a[128:131], v[24:27]\n"
      "s_add_u32 s11, s11, 1\n"
      "s_add_u32 s76, s77, s8\n"
      "v_mfma_f32_16x16x32_bf16 v[24:27], v[156:159], a[144:147], v[24:27]\n"
      "v_mfma_f32_16x16x32_bf16 v[28:31], v[148:151], a[132:135], v[28:31]\n"
      "v_mfma_f32_16x16x32_bf16 v[28:31], v[156:159], a[148:151], v[28:31]\n"
      "buffer_load_dwordx4 v[152:155], v80, s[60:63], s76 offen offset:0 sc1\n"
      "v_mfma_f32_16x16x32_bf16 v[32:35], v[148:151], a[136:139], v[32:35]\n"
      "s_add_u32 s11, s34, s11\n"
      "v_mfma_f32_16x16x32_bf16 v[32:35], v[156:159], a[152:155], v[32:35]\n"
      "v_mfma_f32_16x16x32_bf16 v[36:39], v[148:151], a[140:143], v[36:39]\n"
      "s_waitcnt vmcnt(22)\n"
      "v_mfma_f32_16x16x32_bf16 v[36:39], v[156:159], a[156:159], v[36:39]\n"
      "buffer_load_dwordx4 v[148:151], v80, s[60:63], s76 offen offset:1024 sc1\n"
      "v_mfma_f32_16x16x32_bf16 v[36:39], v[160:163], a[172:175], v[36:39]\n"
      "s_and_b32 s11, s11, 7\n"
      "s_add_u32 s76, s77, s9\n"
      "v_mfma_f32_16x16x32_bf16 v[36:39], v[168:171], a[188:191], v[36:39]\n"
      "v_mfma_f32_16x16x32_bf16 v[32:35], v[160:163], a[168:171], v[32:35]\n"
      "v_mfma_f32_16x16x32_bf16 v[32:35], v[168:171], a[184:187], v[32:35]\n"
      "buffer_load_dwordx4 v[156:159], v80, s[60:63], s76 offen offset:0 sc1\n"
      "v_mfma_f32_16x16x32_bf16 v[28:31], v[160:163], a[164:167], v[28:31]\n"
      "s_mul_i32 s11, s11, 77824\n"
      "v_mfma_f32_16x16x32_bf16 v[28:31], v[168:171], a[180:183], v[28:31]\n"
      "v_mfma_f32_16x16x32_bf16 v[24:27], v[160:163], a[160:163], v[24:27]\n"
      "s_waitcnt vmcnt(22)\n"
      "v_mfma_f32_16x16x32_bf16 v[24:27], v[168:171], a[176:179], v[24:27]\n"
      "buffer_load_dwordx4 v[160:163], v80, s[60:63], s76 offen offset:1024 sc1\n"
      "v_mfma_f32_16x16x32_bf16 v[24:27], v[164:167], a[192:195], v[24:27]\n"
      "s_add_u32 s11, s11, s79\n"
      "s_add_u32 s76, s77, s10\n"
      "s_lshr_b32 s22, s8, 11\n"
      "v_mfma_f32_16x16x32_bf16 v[24:27], v[172:175], a[208:211], v[24:27]\n"
      "s_add_u32 s22, s22, 1\n"
      "s_add_u32 s22, s34, s22\n"
      "s_and_b32 s22, s22, 7\n"
      "v_mfma_f32_16x16x32_bf16 v[28:31], v[164:167], a[196:199], v[28:31]\n"
      "s_lshl_b32 s22, s22, 16\n"
      "s_add_u32 s22, s22, s26\n"
      "v_mfma_f32_16x16x32_bf16 v[28:31], v[172:175], a[212:215], v[28:31]\n"
      "buffer_load_dwordx4 v[168:171], v80, s[60:63], s76 offen offset:0 sc1\n"
      "v_mfma_f32_16x16x32_bf16 v[32:35], v[164:167], a[200:203], v[32:35]\n"
      "s_add_u32 s11, s11, 548864\n"
      "v_mfma_f32_16x16x32_bf16 v[32:35], v[172:175], a[216:219], v[32:35]\n"
      "v_mfma_f32_16x16x32_bf16 v[36:39], v[164:167], a[204:207], v[36:39]\n"
      "s_waitcnt vmcnt(22)\n"
      "v_mfma_f32_16x16x32_bf16 v[36:39], v[172:175], a[220:223], v[36:39]\n"
      "buffer_load_dwordx4 v[164:167], v80, s[60:63], s76 offen offset:1024 sc1\n"
      "v_mfma_f32_16x16x32_bf16 v[36:39], v[176:179], a[236:239], v[36:39]\n"
      "s_mov_b32 s77, s11\n"
      "s_sub_u32 s76, s77, 4096\n"
      "v_mfma_f32_16x16x32_bf16 v[36:39], v[184:187], a[252:255], v[36:39]\n"
      "v_mfma_f32_16x16x32_bf16 v[32:35], v[176:179], a[232:235], v[32:35]\n"
      "v_mfma_f32_16x16x32_bf16 v[32:35], v[184:187], a[248:251], v[32:35]\n"
      "buffer_load_dwordx4 v[172:175], v80, s[60:63], s76 offen offset:0 sc1\n"
      "v_mfma_f32_16x16x32_bf16 v[28:31], v[176:179], a[228:231], v[28:31]\n"
      "v_mfma_f32_16x16x32_bf16 v[28:31], v[184:187], a[244:247], v[28:31]\n"
      "v_mfma_f32_16x16x32_bf16 v[24:27], v[176:179], a[224:227], v[24:27]\n"
      "s_waitcnt vmcnt(22)\n"
      "v_mfma_f32_16x16x32_bf16 v[24:27], v[184:187], a[240:243], v[24:27]\n"
      "buffer_load_dwordx4 v[176:179], v80, s[60:63], s76 offen offset:1024 sc1\n"
      "v_mfma_f32_16x16x32_bf16 v[24:27], v[180:183], v[220:223], v[24:27]\n"
      "v_mfma_f32_16x16x32_bf16 v[24:27], v[96:99], v[236:239], v[24:27]\n"
      "v_mfma_f32_16x16x32_bf16 v[28:31], v[180:183], v[224:227], v[28:31]\n"
      "s_waitcnt vmcnt(9)\n"
      "RC26_%=:\n"
      "v_cmp_le_u32 vcc, 3, v7\n"
      "s_nop 1\n"
      "s_andn2_b32 s69, 255, vcc_lo\n"
      "s_cbranch_scc0 RO27_%=\n"
      "s_cmp_eq_u32 s87, 0\n"
      "s_cbranch_scc1 RO27_%=\n"
      "s_sub_u32 s87, s87, 1\n"
      "s_mov_b64 exec, 255\n"
      "buffer_load_dword v7, v5, s[60:63], s31 offen sc1\n"
      "s_mov_b64 exec, -1\n"
      "s_waitcnt vmcnt(0)\n"
      "s_branch RC26_%=\n"
      "RO27_%=:\n"
      "v_mov_b32 v6, 5\n"
      "v_mfma_f32_16x16x32_bf16 v[28:31], v[96:99], v[240:243], v[28:31]\n"
      "buffer_load_dwordx4 v[184:187], v80, s[60:63], s76 offen offset:2048 sc1\n"
      "v_mfma_f32_16x16x32_bf16 v[32:35], v[180:183], v[228:231], v[32:35]\n"
      "buffer_load_dwordx4 v[188:191], v80, s[60:63], s29 offen offset:0 sc1\n"
      "v_mfma_f32_16x16x32_bf16 v[32:35], v[96:99], v[244:247], v[32:35]\n"
      "buffer_load_dwordx4 v[192:195], v80, s[60:63], s29 offen offset:1024 sc1\n"
      "s_mov_b64 exec, 1\n"
      "buffer_store_dword v6, off, s[60:63], s30 sc1\n"
      "s_mov_b64 exec, -1\n"
      "v_mfma_f32_16x16x32_bf16 v[36:39], v[180:183], v[232:235], v[36:39]\n"
      "buffer_load_dwordx4 v[196:199], v90, s[60:63], s25 offen sc1\n"
      "v_mfma_f32_16x16x32_bf16 v[36:39], v[96:99], v[248:251], v[36:39]\n"
      "buffer_load_dwordx4 v[180:183], v80, s[60:63], s76 offen offset:3072 sc1\n"
      "v_mfma_f32_16x16x32_bf16 v[8:11], v[104:107], a[0:3], 0\n"
      "s_add_u32 s76, s77, s4\n"
      "s_lshr_b32 s24, s4, 11\n"
      "s_add_u32 s24, s24, 1\n"
      "s_sub_u32 s24, s34, s24\n"
      "v_mfma_f32_16x16x32_bf16 v[8:11], v[100:103], a[16:19], v[8:11]\n"
      "s_and_b32 s24, s24, 7\n"
      "v_cvt_pk_bf16_f32 v72, v24, v25\n"
      "v_cvt_pk_bf16_f32 v73, v26, v27\n"
      "v_mfma_f32_16x16x32_bf16 v[12:15], v[104:107], a[4:7], 0\n"
      "s_lshl_b32 s24, s24, 13\n"
      "v_cvt_pk_bf16_f32 v74, v28, v29\n"
      "v_cvt_pk_bf16_f32 v75, v30, v31\n"
      "s_add_u32 s24, s24, s27\n"
      "v_mfma_f32_16x16x32_bf16 v[12:15], v[100:103], a[20:23], v[12:15]\n"
      "buffer_load_dwordx4 v[96:99], v80, s[60:63], s76 offen offset:0 sc1\n"
      "v_mfma_f32_16x16x32_bf16 v[16:19], v[104:107], a[8:11], 0\n"
      "buffer_store_dwordx4 v[72:75], v80, s[60:63], s22 offen offset:0 sc1\n"
      "v_mfma_f32_16x16x32_bf16 v[16:19], v[100:103], a[24:27], v[16:19]\n"
      "v_cvt_pk_bf16_f32 v76, v32, v33\n"
      "v_cvt_pk_bf16_f32 v77, v34, v35\n"
      "v_mfma_f32_16x16x32_bf16 v[20:23], v[104:107], a[12:15], 0\n"
      "v_cvt_pk_bf16_f32 v78, v36, v37\n"
      "v_cvt_pk_bf16_f32 v79, v38, v39\n"
      "v_mfma_f32_16x16x32_bf16 v[20:23], v[100:103], a[28:31], v[20:23]\n"
      "buffer_load_dwordx4 v[104:107], v80, s[60:63], s76 offen offset:1024 sc1\n"
      "v_mfma_f32_16x16x32_bf16 v[20:23], v[108:111], a[44:47], v[20:23]\n"
      "s_add_u32 s76, s77, s5\n"
      "v_mfma_f32_16x16x32_bf16 v[20:23], v[112:115], a[60:63], v[20:23]\n"
      "buffer_store_dwordx4 v[76:79], v80, s[60:63], s22 offen offset:1024 sc1\n"
      "v_mfma_f32_16x16x32_bf16 v[16:19], v[108:111], a[40:43], v[16:19]\n"
      "buffer_load_dwordx4 v[40:43], v80, s[60:63], s24 offen offset:0 sc1\n"
      "v_mfma_f32_16x16x32_bf16 v[16:19], v[112:115], a[56:59], v[16:19]\n"
      "buffer_load_dwordx4 v[100:103], v80, s[60:63], s76 offen offset:0 sc1\n"
      "v_mfma_f32_16x16x32_bf16 v[12:15], v[108:111], a[36:39], v[12:15]\n"
      "buffer_load_dwordx4 v[44:47], v80, s[60:63], s24 offen offset:1024 sc1\n"
      "v_mfma_f32_16x16x32_bf16 v[12:15], v[112:115], a[52:55], v[12:15]\n"
      "s_lshr_b32 s24, s5, 11\n"
      "s_add_u32 s24, s24, 1\n"
      "s_sub_u32 s24, s34, s24\n"
      "v_mfma_f32_16x16x32_bf16 v[8:11], v[108:111], a[32:35], v[8:11]\n"
      "s_and_b32 s24, s24, 7\n"
      "s_lshl_b32 s24, s24, 13\n"
      "s_add_u32 s24, s24, s27\n"
      "v_mfma_f32_16x16x32_bf16 v[8:11], v[112:115], a[48:51], v[8:11]\n"
      "buffer_load_dwordx4 v[108:111], v80, s[60:63], s76 offen offset:1024 sc1\n"
      "v_mfma_f32_16x16x32_bf16 v[8:11], v[120:123], a[64:67], v[8:11]\n"
      "s_add_u32 s76, s77, s6\n"
      "v_mfma_f32_16x16x32_bf16 v[8:11], v[116:119], a[80:83], v[8:11]\n"
      "buffer_load_dwordx4 v[48:51], v80, s[60:63], s24 offen offset:0 sc1\n"
      "v_mfma_f32_16x16x32_bf16 v[12:15], v[120:123], a[68:71], v[12:15]\n"
      "buffer_load_dwordx4 v[52:55], v80, s[60:63], s24 offen offset:1024 sc1\n"
      "v_mfma_f32_16x16x32_bf16 v[12:15], v[116:119], a[84:87], v[12:15]\n"
      "buffer_load_dwordx4 v[112:115], v80, s[60:63], s76 offen offset:0 sc1\n"
      "v_mfma_f32_16x16x32_bf16 v[16:19], v[120:123], a[72:75], v[16:19]\n"
      "v_mfma_f32_16x16x32_bf16 v[16:19], v[116:119], a[88:91], v[16:19]\n"
      "v_mfma_f32_16x16x32_bf16 v[20:23], v[120:123], a[76:79], v[20:23]\n"
      "v_mfma_f32_16x16x32_bf16 v[20:23], v[116:119], a[92:95], v[20:23]\n"
      "buffer_load_dwordx4 v[120:123], v80, s[60:63], s76 offen offset:1024 sc1\n"
      "v_mfma_f32_16x16x32_bf16 v[20:23], v[124:127], a[108:111], v[20:23]\n"
      "s_add_u32 s76, s77, s7\n"
      "v_mfma_f32_16x16x32_bf16 v[20:23], v[128:131], a[124:127], v[20:23]\n"
      "v_mfma_f32_16x16x32_bf16 v[16:19], v[124:127], a[104:107], v[16:19]\n"
      "v_mfma_f32_16x16x32_bf16 v[16:19], v[128:131], a[120:123], v[16:19]\n"
      "buffer_load_dwordx4 v[116:119], v80, s[60:63], s76 offen offset:0 sc1\n"
      "v_mfma_f32_16x16x32_bf16 v[12:15], v[124:127], a[100:103], v[12:15]\n"
      "s_mov_b32 s11, s34\n"
      "v_mfma_f32_16x16x32_bf16 v[12:15], v[128:131], a[116:119], v[12:15]\n"
      "v_mfma_f32_16x16x32_bf16 v[8:11], v[124:127], a[96:99], v[8:11]\n"
      "v_mfma_f32_16x16x32_bf16 v[8:11], v[128:131], a[112:115], v[8:11]\n"
      "buffer_load_dwordx4 v[124:127], v80, s[60:63], s76 offen offset:1024 sc1\n"
      "v_mfma_f32_16x16x32_bf16 v[8:11], v[136:139], a[128:131], v[8:11]\n"
      "s_mul_i32 s11, s11, 77824\n"
      "s_add_u32 s76, s77, s8\n"
      "v_mfma_f32_16x16x32_bf16 v[8:11], v[132:135], a[144:147], v[8:11]\n"
      "v_mfma_f32_16x16x32_bf16 v[12:15], v[136:139], a[132:135], v[12:15]\n"
      "s_waitcnt vmcnt(18)\n"
      "v_lshlrev_b32 v204, 16, v188\n"
      "v_and_b32 v205, s16, v188\n"
      "v_mfma_f32_16x16x32_bf16 v[12:15], v[132:135], a[148:151], v[12:15]\n"
      "buffer_load_dwordx4 v[128:131], v80, s[60:63], s76 offen offset:0 sc1\n"
      "v_mfma_f32_16x16x32_bf16 v[16:19], v[136:139], a[136:139], v[16:19]\n"
      "s_add_u32 s11, s11, s79\n"
      "v_lshlrev_b32 v206, 16, v189\n"
      "v_and_b32 v207, s16, v189\n"
      "v_mfma_f32_16x16x32_bf16 v[16:19], v[132:135], a[152:155], v[16:19]\n"
      "v_lshlrev_b32 v208, 16, v190\n"
      "v_and_b32 v209, s16, v190\n"
      "v_mfma_f32_16x16x32_bf16 v[20:23], v[136:139], a[140:143], v[20:23]\n"
      "v_lshlrev_b32 v210, 16, v191\n"
      "v_and_b32 v211, s16, v191\n"
      "v_mfma_f32_16x16x32_bf16 v[20:23], v[132:135], a[156:159], v[20:23]\n"
      "buffer_load_dwordx4 v[136:139], v80, s[60:63], s76 offen offset:1024 sc1\n"
      "v_mfma_f32_16x16x32_bf16 v[20:23], v[140:143], a[172:175], v[20:23]\n"
      "s_add_u32 s11, s11, 548864\n"
      "s_add_u32 s76, s77, s9\n"
      "s_waitcnt vmcnt(19)\n"
      "v_lshlrev_b32 v212, 16, v192\n"
      "v_mfma_f32_16x16x32_bf16 v[20:23], v[144:147], a[188:191], v[20:23]\n"
      "v_and_b32 v213, s16, v192\n"
      "v_lshlrev_b32 v214, 16, v193\n"
      "v_mfma_f32_16x16x32_bf16 v[16:19], v[140:143], a[168:171], v[16:19]\n"
      "v_and_b32 v215, s16, v193\n"
      "v_lshlrev_b32 v216, 16, v194\n"
      "v_mfma_f32_16x16x32_bf16 v[16:19], v[144:147], a[184:187], v[16:19]\n"
      "buffer_load_dwordx4 v[132:135], v80, s[60:63], s76 offen offset:0 sc1\n"
      "v_mfma_f32_16x16x32_bf16 v[12:15], v[140:143], a[164:167], v[12:15]\n"
      "v_and_b32 v217, s16, v194\n"
      "v_lshlrev_b32 v218, 16, v195\n"
      "v_mfma_f32_16x16x32_bf16 v[12:15], v[144:147], a[180:183], v[12:15]\n"
      "v_and_b32 v219, s16, v195\n"
      "v_mfma_f32_16x16x32_bf16 v[8:11], v[140:143], a[160:163], v[8:11]\n"
      "v_permlane32_swap_b32 v204, v212\n"
      "v_mfma_f32_16x16x32_bf16 v[8:11], v[144:147], a[176:179], v[8:11]\n"
      "buffer_load_dwordx4 v[140:143], v80, s[60:63], s76 offen offset:1024 sc1\n"
      "v_mfma_f32_16x16x32_bf16 v[8:11], v[152:155], a[192:195], v[8:11]\n"
      "s_add_u32 s76, s77, s10\n"
      "s_lshr_b32 s22, s9, 11\n"
      "s_add_u32 s22, s22, 1\n"
      "v_mfma_f32_16x16x32_bf16 v[8:11], v[148:151], a[208:211], v[8:11]\n"
      "s_add_u32 s22, s34, s22\n"
      "s_and_b32 s22, s22, 7\n"
      "s_lshl_b32 s22, s22, 16\n"
      "v_mfma_f32_16x16x32_bf16 v[12:15], v[152:155], a[196:199], v[12:15]\n"
      "s_add_u32 s22, s22, s26\n"
      "v_permlane32_swap_b32 v205, v213\n"
      "v_mfma_f32_16x16x32_bf16 v[12:15], v[148:151], a[212:215], v[12:15]\n"
      "buffer_load_dwordx4 v[144:147], v80, s[60:63], s76 offen offset:0 sc1\n"
      "v_mfma_f32_16x16x32_bf16 v[16:19], v[152:155], a[200:203], v[16:19]\n"
      "v_permlane32_swap_b32 v206, v214\n"
      "v_mfma_f32_16x16x32_bf16 v[16:19], v[148:151], a[216:219], v[16:19]\n"
      "v_permlane32_swap_b32 v207, v215\n"
      "v_mfma_f32_16x16x32_bf16 v[20:23], v[152:155], a[204:207], v[20:23]\n"
      "v_permlane32_swap_b32 v208, v216\n"
      "v_mfma_f32_16x16x32_bf16 v[20:23], v[148:151], a[220:223], v[20:23]\n"
      "buffer_load_dwordx4 v[152:155], v80, s[60:63], s76 offen offset:1024 sc1\n"
      "v_mfma_f32_16x16x32_bf16 v[20:23], v[156:159], a[236:239], v[20:23]\n"
      "v_permlane32_swap_b32 v209, v217\n"
      "v_mfma_f32_16x16x32_bf16 v[20:23], v[160:163], a[252:255], v[20:23]\n"
      "v_permlane32_swap_b32 v210, v218\n"
      "v_mfma_f32_16x16x32_bf16 v[16:19], v[156:159], a[232:235], v[16:19]\n"
      "v_permlane32_swap_b32 v211, v219\n"
      "v_mfma_f32_16x16x32_bf16 v[16:19], v[160:163], a[248:251], v[16:19]\n"
      "v_add_f32 v204, v204, v212\n"
      "v_add_f32 v205, v205, v213\n"
      "v_mfma_f32_16x16x32_bf16 v[12:15], v[156:159], a[228:231], v[12:15]\n"
      "v_add_f32 v206, v206, v214\n"
      "v_add_f32 v207, v207, v215\n"
      "v_mfma_f32_16x16x32_bf16 v[12:15], v[160:163], a[244:247], v[12:15]\n"
      "v_add_f32 v208, v208, v216\n"
      "v_add_f32 v209, v209, v217\n"
      "v_mfma_f32_16x16x32_bf16 v[8:11], v[156:159], a[224:227], v[8:11]\n"
      "v_add_f32 v210, v210, v218\n"
      "v_add_f32 v211, v211, v219\n"
      "v_mfma_f32_16x16x32_bf16 v[8:11], v[160:163], a[240:243], v[8:11]\n"
      "v_permlane16_swap_b32 v204, v208\n"
      "v_mfma_f32_16x16x32_bf16 v[8:11], v[168:171], v[220:223], v[8:11]\n"
      "v_permlane16_swap_b32 v205, v209\n"
      "v_mfma_f32_16x16x32_bf16 v[8:11], v[164:167], v[236:239], v[8:11]\n"
      "v_permlane16_swap_b32 v206, v210\n"
      "v_mfma_f32_16x16x32_bf16 v[12:15], v[168:171], v[224:227], v[12:15]\n"
      "v_permlane16_swap_b32 v207, v211\n"
      "v_mfma_f32_16x16x32_bf16 v[12:15], v[164:167], v[240:243], v[12:15]\n"
      "v_add_f32 v204, v204, v208\n"
      "v_add_f32 v205, v205, v209\n"
      "v_mfma_f32_16x16x32_bf16 v[16:19], v[168:171], v[228:231], v[16:19]\n"
      "v_add_f32 v206, v206, v210\n"
      "v_add_f32 v207, v207, v211\n"
      "v_mfma_f32_16x16x32_bf16 v[16:19], v[164:167], v[244:247], v[16:19]\n"
      "v_add_f32_dpp v204, v204, v204 row_ror:8 row_mask:0xf bank_mask:0xf\n"
      "v_add_f32_dpp v205, v205, v205 row_ror:8 row_mask:0xf bank_mask:0xf\n"
      "v_mfma_f32_16x16x32_bf16 v[20:23], v[168:171], v[232:235], v[20:23]\n"
      "v_add_f32_dpp v206, v206, v206 row_ror:8 row_mask:0xf bank_mask:0xf\n"
      "v_add_f32_dpp v207, v207, v207 row_ror:8 row_mask:0xf bank_mask:0xf\n"
      "v_mfma_f32_16x16x32_bf16 v[20:23], v[164:167], v[248:251], v[20:23]\n"
      "v_mov_b32 v208, v204\n"
      "v_mov_b32 v209, v205\n"
      "v_mfma_f32_16x16x32_bf16 v[24:27], v[172:175], a[0:3], 0\n"
      "v_mov_b32 v210, v206\n"
      "v_mov_b32 v211, v207\n"
      "v_mfma_f32_16x16x32_bf16 v[24:27], v[176:179], a[16:19], v[24:27]\n"
      "v_cvt_pk_bf16_f32 v72, v8, v9\n"
      "v_cvt_pk_bf16_f32 v73, v10, v11\n"
      "v_mfma_f32_16x16x32_bf16 v[28:31], v[172:175], a[4:7], 0\n"
      "v_cvt_pk_bf16_f32 v74, v12, v13\n"
      "v_cvt_pk_bf16_f32 v75, v14, v15\n"
      "v_mfma_f32_16x16x32_bf16 v[28:31], v[176:179], a[20:23], v[28:31]\n"
      "buffer_store_dwordx4 v[72:75], v80, s[60:63], s22 offen offset:0 sc1\n"
      "v_mfma_f32_16x16x32_bf16 v[32:35], v[172:175], a[8:11], 0\n"
      "v_cvt_pk_bf16_f32 v76, v16, v17\n"
      "v_cvt_pk_bf16_f32 v77, v18, v19\n"
      "v_mfma_f32_16x16x32_bf16 v[32:35], v[176:179], a[24:27], v[32:35]\n"
      "v_cvt_pk_bf16_f32 v78, v20, v21\n"
      "v_cvt_pk_bf16_f32 v79, v22, v23\n"
      "v_mfma_f32_16x16x32_bf16 v[36:39], v[172:175], a[12:15], 0\n"
      "buffer_store_dwordx4 v[76:79], v80, s[60:63], s22 offen offset:1024 sc1\n"
      "v_mfma_f32_16x16x32_bf16 v[36:39], v[176:179], a[28:31], v[36:39]\n"
      "s_waitcnt vmcnt(22)\n"
      "v_permlane16_swap_b32 v204, v208\n"
      "v_mfma_f32_16x16x32_bf16 v[36:39], v[184:187], a[44:47], v[36:39]\n"
      "v_permlane16_swap_b32 v205, v209\n"
      "v_mfma_f32_16x16x32_bf16 v[36:39], v[180:183], a[60:63], v[36:39]\n"
      "v_permlane16_swap_b32 v206, v210\n"
      "v_mfma_f32_16x16x32_bf16 v[32:35], v[184:187], a[40:43], v[32:35]\n"
      "v_permlane16_swap_b32 v207, v211\n"
      "v_mfma_f32_16x16x32_bf16 v[32:35], v[180:183], a[56:59], v[32:35]\n"
      "v_lshlrev_b32 v212, 16, v196\n"
      "v_and_b32 v213, s16, v196\n"
      "v_mfma_f32_16x16x32_bf16 v[28:31], v[184:187], a[36:39], v[28:31]\n"
      "v_lshlrev_b32 v214, 16, v197\n"
      "v_and_b32 v215, s16, v197\n"
      "v_mfma_f32_16x16x32_bf16 v[28:31], v[180:183], a[52:55], v[28:31]\n"
      "v_lshlrev_b32 v216, 16, v198\n"
      "v_and_b32 v217, s16, v198\n"
      "v_mfma_f32_16x16x32_bf16 v[24:27], v[184:187], a[32:35], v[24:27]\n"
      "v_lshlrev_b32 v218, 16, v199\n"
      "v_and_b32 v219, s16, v199\n"
      "v_mfma_f32_16x16x32_bf16 v[24:27], v[180:183], a[48:51], v[24:27]\n"
      "s_waitcnt vmcnt(19)\n"
      "v_fmac_f32 v212, s17, v204\n"
      "v_fmac_f32 v213, s17, v205\n"
      "v_mfma_f32_16x16x32_bf16 v[24:27], v[96:99], a[64:67], v[24:27]\n"
      "v_fmac_f32 v214, s17, v206\n"
      "v_fmac_f32 v215, s17, v207\n"
      "v_mfma_f32_16x16x32_bf16 v[24:27], v[104:107], a[80:83], v[24:27]\n"
      "v_fmac_f32 v216, s17, v208\n"
      "v_fmac_f32 v217, s17, v209\n"
      "v_mfma_f32_16x16x32_bf16 v[28:31], v[96:99], a[68:71], v[28:31]\n"
      "v_fmac_f32 v218, s17, v210\n"
      "v_fmac_f32 v219, s17, v211\n"
      "v_mfma_f32_16x16x32_bf16 v[28:31], v[104:107], a[84:87], v[28:31]\n"
      "v_cvt_pk_bf16_f32 v204, v212, v213\n"
      "v_cvt_pk_bf16_f32 v205, v214, v215\n"
      "v_mfma_f32_16x16x32_bf16 v[32:35], v[96:99], a[72:75], v[32:35]\n"
      "v_cvt_pk_bf16_f32 v206, v216, v217\n"
      "v_cvt_pk_bf16_f32 v207, v218, v219\n"
      "v_mfma_f32_16x16x32_bf16 v[32:35], v[104:107], a[88:91], v[32:35]\n"
      "s_mov_b32 exec_lo, 0xff\n"
      "s_mov_b32 exec_hi, 0xff\n"
      "ds_write_b128 v84, v[204:207]\n"
      "s_mov_b64 exec, -1\n"
      "v_mfma_f32_16x16x32_bf16 v[36:39], v[96:99], a[76:79], v[36:39]\n"
      "s_waitcnt vmcnt(17)\n"
      "v_lshlrev_b32 v2, 16, v40\n"
      "v_and_b32 v3, s16, v40\n"
      "v_mfma_f32_16x16x32_bf16 v[36:39], v[104:107], a[92:95], v[36:39]\n"
      "s_waitcnt vmcnt(14)\n"
      "v_add_f32 v56, v56, v2\n"
      "v_add_f32 v57, v57, v3\n"
      "v_mfma_f32_16x16x32_bf16 v[36:39], v[100:103], a[108:111], v[36:39]\n"
      "v_lshlrev_b32 v2, 16, v41\n"
      "v_and_b32 v3, s16, v41\n"
      "v_mfma_f32_16x16x32_bf16 v[36:39], v[108:111], a[124:127], v[36:39]\n"
      "v_add_f32 v58, v58, v2\n"
      "v_add_f32 v59, v59, v3\n"
      "v_mfma_f32_16x16x32_bf16 v[32:35], v[100:103], a[104:107], v[32:35]\n"
      "v_lshlrev_b32 v2, 16, v42\n"
      "v_and_b32 v3, s16, v42\n"
      "v_mfma_f32_16x16x32_bf16 v[32:35], v[108:111], a[120:123], v[32:35]\n"
      "v_add_f32 v60, v60, v2\n"
      "v_add_f32 v61, v61, v3\n"
      "v_mfma_f32_16x16x32_bf16 v[28:31], v[100:103], a[100:103], v[28:31]\n"
      "v_lshlrev_b32 v2, 16, v43\n"
      "v_and_b32 v3, s16, v43\n"
      "v_mfma_f32_16x16x32_bf16 v[28:31], v[108:111], a[116:119], v[28:31]\n"
      "v_add_f32 v62, v62, v2\n"
      "v_add_f32 v63, v63, v3\n"
      "v_mfma_f32_16x16x32_bf16 v[24:27], v[100:103], a[96:99], v[24:27]\n"
      "v_lshlrev_b32 v2, 16, v44\n"
      "v_and_b32 v3, s16, v44\n"
      "v_mfma_f32_16x16x32_bf16 v[24:27], v[108:111], a[112:115], v[24:27]\n"
      "s_waitcnt vmcnt(10)\n"
      "v_add_f32 v64, v64, v2\n"
      "v_add_f32 v65, v65, v3\n"
      "v_mfma_f32_16x16x32_bf16 v[24:27], v[112:115], a[128:131], v[24:27]\n"
      "v_lshlrev_b32 v2, 16, v45\n"
      "v_and_b32 v3, s16, v45\n"
      "v_mfma_f32_16x16x32_bf16 v[24:27], v[120:123], a[144:147], v[24:27]\n"
      "v_add_f32 v66, v66, v2\n"
      "v_add_f32 v67, v67, v3\n"
      "v_mfma_f32_16x16x32_bf16 v[28:31], v[112:115], a[132:135], v[28:31]\n"
      "v_lshlrev_b32 v2, 16, v46\n"
      "v_and_b32 v3, s16, v46\n"
      "v_mfma_f32_16x16x32_bf16 v[28:31], v[120:123], a[148:151], v[28:31]\n"
      "v_add_f32 v68, v68, v2\n"
      "v_add_f32 v69, v69, v3\n"
      "v_mfma_f32_16x16x32_bf16 v[32:35], v[112:115], a[136:139], v[32:35]\n"
      "v_lshlrev_b32 v2, 16, v47\n"
      "v_and_b32 v3, s16, v47\n"
      "v_mfma_f32_16x16x32_bf16 v[32:35], v[120:123], a[152:155], v[32:35]\n"
      "v_add_f32 v70, v70, v2\n"
      "v_add_f32 v71, v71, v3\n"
      "v_mfma_f32_16x16x32_bf16 v[36:39], v[112:115], a[140:143], v[36:39]\n"
      "v_lshlrev_b32 v2, 16, v48\n"
      "v_and_b32 v3, s16, v48\n"
      "v_mfma_f32_16x16x32_bf16 v[36:39], v[120:123], a[156:159], v[36:39]\n"
      "s_waitcnt vmcnt(8)\n"
      "v_add_f32 v56, v56, v2\n"
      "v_add_f32 v57, v57, v3\n"
      "v_mfma_f32_16x16x32_bf16 v[36:39], v[116:119], a[172:175], v[36:39]\n"
      "v_lshlrev_b32 v2, 16, v49\n"
      "v_and_b32 v3, s16, v49\n"
      "v_mfma_f32_16x16x32_bf16 v[36:39], v[124:127], a[188:191], v[36:39]\n"
      "v_add_f32 v58, v58, v2\n"
      "v_add_f32 v59, v59, v3\n"
      "v_mfma_f32_16x16x32_bf16 v[32:35], v[116:119], a[168:171], v[32:35]\n"
      "v_lshlrev_b32 v2, 16, v50\n"
      "v_and_b32 v3, s16, v50\n"
      "v_mfma_f32_16x16x32_bf16 v[32:35], v[124:127], a[184:187], v[32:35]\n"
      "v_add_f32 v60, v60, v2\n"
      "v_add_f32 v61, v61, v3\n"
      "v_mfma_f32_16x16x32_bf16 v[28:31], v[116:119], a[164:167], v[28:31]\n"
      "v_lshlrev_b32 v2, 16, v51\n"
      "v_and_b32 v3, s16, v51\n"
      "v_mfma_f32_16x16x32_bf16 v[28:31], v[124:127], a[180:183], v[28:31]\n"
      "v_add_f32 v62, v62, v2\n"
      "v_add_f32 v63, v63, v3\n"
      "v_mfma_f32_16x16x32_bf16 v[24:27], v[116:119], a[160:163], v[24:27]\n"
      "v_lshlrev_b32 v2, 16, v52\n"
      "v_and_b32 v3, s16, v52\n"
      "v_mfma_f32_16x16x32_bf16 v[24:27], v[124:127], a[176:179], v[24:27]\n"
      "s_waitcnt vmcnt(6)\n"
      "v_add_f32 v64, v64, v2\n"
      "v_add_f32 v65, v65, v3\n"
      "v_mfma_f32_16x16x32_bf16 v[24:27], v[128:131], a[192:195], v[24:27]\n"
      "s_lshr_b32 s22, s10, 11\n"
      "s_add_u32 s22, s22, 1\n"
      "s_add_u32 s22, s34, s22\n"
      "v_mfma_f32_16x16x32_bf16 v[24:27], v[136:139], a[208:211], v[24:27]\n"
      "s_and_b32 s22, s22, 7\n"
      "s_lshl_b32 s22, s22, 16\n"
      "s_add_u32 s22, s22, s26\n"
      "v_mfma_f32_16x16x32_bf16 v[28:31], v[128:131], a[196:199], v[28:31]\n"
      "v_lshlrev_b32 v2, 16, v53\n"
      "v_and_b32 v3, s16, v53\n"
      "v_mfma_f32_16x16x32_bf16 v[28:31], v[136:139], a[212:215], v[28:31]\n"
      "s_mov_b64 exec, 24\n"
      "buffer_load_dword v7, v5, s[60:63], s31 offen sc1\n"
      "s_mov_b64 exec, -1\n"
      "v_add_f32 v66, v66, v2\n"
      "v_add_f32 v67, v67, v3\n"
      "v_mfma_f32_16x16x32_bf16 v[32:35], v[128:131], a[200:203], v[32:35]\n"
      "v_lshlrev_b32 v2, 16, v54\n"
      "v_and_b32 v3, s16, v54\n"
      "v_mfma_f32_16x16x32_bf16 v[32:35], v[136:139], a[216:219], v[32:35]\n"
      "v_add_f32 v68, v68, v2\n"
      "v_add_f32 v69, v69, v3\n"
      "v_mfma_f32_16x16x32_bf16 v[36:39], v[128:131], a[204:207], v[36:39]\n"
      "v_lshlrev_b32 v2, 16, v55\n"
      "v_and_b32 v3, s16, v55\n"
      "v_mfma_f32_16x16x32_bf16 v[36:39], v[136:139], a[220:223], v[36:39]\n"
      "s_waitcnt vmcnt(5)\n"
      "ds_read_b128 v[128:131], v88 offset:0\n"
      "v_add_f32 v70, v70, v2\n"
      "v_mfma_f32_16x16x32_bf16 v[36:39], v[132:135], a[236:239], v[36:39]\n"
      "v_add_f32 v71, v71, v3\n"
      "v_mfma_f32_16x16x32_bf16 v[36:39], v[140:143], a[252:255], v[36:39]\n"
      "v_mfma_f32_16x16x32_bf16 v[32:35], v[132:135], a[232:235], v[32:35]\n"
      "v_mfma_f32_16x16x32_bf16 v[32:35], v[140:143], a[248:251], v[32:35]\n"
      "ds_read_b128 v[136:139], v88 offset:1024\n"
      "v_mfma_f32_16x16x32_bf16 v[28:31], v[132:135], a[228:231], v[28:31]\n"
      "v_mfma_f32_16x16x32_bf16 v[28:31], v[140:143], a[244:247], v[28:31]\n"
      "v_mfma_f32_16x16x32_bf16 v[24:27], v[132:135], a[224:227], v[24:27]\n"
      "v_mfma_f32_16x16x32_bf16 v[24:27], v[140:143], a[240:243], v[24:27]\n"
      "s_waitcnt vmcnt(3)\n"
      "ds_read_b128 v[132:135], v88 offset:2048\n"
      "v_mfma_f32_16x16x32_bf16 v[24:27], v[144:147], v[220:223], v[24:27]\n"
      "v_mfma_f32_16x16x32_bf16 v[24:27], v[152:155], v[236:239], v[24:27]\n"
      "v_mfma_f32_16x16x32_bf16 v[28:31], v[144:147], v[224:227], v[28:31]\n"
      "v_mov_b32 v6, 7\n"
      "s_waitcnt vmcnt(1)\n"
      "v_mfma_f32_16x16x32_bf16 v[28:31], v[152:155], v[240:243], v[28:31]\n"
      "ds_read_b128 v[140:143], v88 offset:3072\n"
      "v_mfma_f32_16x16x32_bf16 v[32:35], v[144:147], v[228:231], v[32:35]\n"
      "s_mov_b64 exec, 1\n"
      "buffer_store_dword v6, off, s[60:63], s30 sc1\n"
      "s_mov_b64 exec, -1\n"
      "v_mfma_f32_16x16x32_bf16 v[32:35], v[152:155], v[244:247], v[32:35]\n"
      "v_mfma_f32_16x16x32_bf16 v[36:39], v[144:147], v[232:235], v[36:39]\n"
      "v_mfma_f32_16x16x32_bf16 v[36:39], v[152:155], v[248:251], v[36:39]\n"
      "s_waitcnt lgkmcnt(2)\n"
      "ds_read_b128 v[144:147], v88 offset:4096\n"
      "v_mfma_f32_16x16x32_bf16 v[8:11], v[128:131], a[0:3], 0\n"
      "v_mfma_f32_16x16x32_bf16 v[8:11], v[136:139], a[16:19], v[8:11]\n"
      "v_cvt_pk_bf16_f32 v72, v24, v25\n"
      "v_cvt_pk_bf16_f32 v73, v26, v27\n"
      "v_mfma_f32_16x16x32_bf16 v[12:15], v[128:131], a[4:7], 0\n"
      "v_cvt_pk_bf16_f32 v74, v28, v29\n"
      "v_cvt_pk_bf16_f32 v75, v30, v31\n"
      "v_mfma_f32_16x16x32_bf16 v[12:15], v[136:139], a[20:23], v[12:15]\n"
      "ds_read_b128 v[152:155], v88 offset:5120\n"
      "v_min_u32 v72, -2, v72\n"
      "v_mfma_f32_16x16x32_bf16 v[16:19], v[128:131], a[8:11], 0\n"
      "v_min_u32 v73, -2, v73\n"
      "v_min_u32 v74, -2, v74\n"
      "v_mfma_f32_16x16x32_bf16 v[16:19], v[136:139], a[24:27], v[16:19]\n"
      "v_min_u32 v75, -2, v75\n"
      "v_mfma_f32_16x16x32_bf16 v[20:23], v[128:131], a[12:15], 0\n"
      "buffer_store_dwordx4 v[72:75], v80, s[60:63], s22 offen offset:0 sc1\n"
      "v_mfma_f32_16x16x32_bf16 v[20:23], v[136:139], a[28:31], v[20:23]\n"
      "s_waitcnt lgkmcnt(2)\n"
      "ds_read_b128 v[128:131], v88 offset:6144\n"
      "v_cvt_pk_bf16_f32 v76, v32, v33\n"
      "v_mfma_f32_16x16x32_bf16 v[20:23], v[132:135], a[44:47], v[20:23]\n"
      "v_cvt_pk_bf16_f32 v77, v34, v35\n"
      "v_cvt_pk_bf16_f32 v78, v36, v37\n"
      "v_mfma_f32_16x16x32_bf16 v[20:23], v[140:143], a[60:63], v[20:23]\n"
      "v_cvt_pk_bf16_f32 v79, v38, v39\n"
      "v_min_u32 v76, -2, v76\n"
      "v_mfma_f32_16x16x32_bf16 v[16:19], v[132:135], a[40:43], v[16:19]\n"
      "v_min_u32 v77, -2, v77\n"
      "v_min_u32 v78, -2, v78\n"
      "v_mfma_f32_16x16x32_bf16 v[16:19], v[140:143], a[56:59], v[16:19]\n"
      "ds_read_b128 v[136:139], v88 offset:7168\n"
      "v_min_u32 v79, -2, v79\n"
      "v_mfma_f32_16x16x32_bf16 v[12:15], v[132:135], a[36:39], v[12:15]\n"
      "buffer_store_dwordx4 v[76:79], v80, s[60:63], s22 offen offset:1024 sc1\n"
      "v_mfma_f32_16x16x32_bf16 v[12:15], v[140:143], a[52:55], v[12:15]\n"
      "s_waitcnt vmcnt(3)\n"
      "RC28_%=:\n"
      "v_cmp_le_u32 vcc, 5, v7\n"
      "s_nop 1\n"
      "s_andn2_b32 s69, 24, vcc_lo\n"
      "s_cbranch_scc0 RO29_%=\n"
      "s_cmp_eq_u32 s87, 0\n"
      "s_cbranch_scc1 RO29_%=\n"
      "s_sub_u32 s87, s87, 1\n"
      "s_mov_b64 exec, 24\n"
      "buffer_load_dword v7, v5, s[60:63], s31 offen sc1\n"
      "s_mov_b64 exec, -1\n"
      "s_waitcnt vmcnt(0)\n"
      "s_branch RC28_%=\n"
      "RO29_%=:\n"
      "v_mfma_f32_16x16x32_bf16 v[8:11], v[132:135], a[32:35], v[8:11]\n"
      "s_lshr_b32 s24, s6, 11\n"
      "s_add_u32 s24, s24, 1\n"
      "s_sub_u32 s24, s34, s24\n"
      "v_mfma_f32_16x16x32_bf16 v[8:11], v[140:143], a[48:51], v[8:11]\n"
      "s_and_b32 s24, s24, 7\n"
      "ds_read_b128 v[132:135], v88 offset:8192\n"
      "s_lshl_b32 s24, s24, 13\n"
      "s_waitcnt lgkmcnt(4)\n"
      "v_mfma_f32_16x16x32_bf16 v[8:11], v[144:147], a[64:67], v[8:11]\n"
      "s_add_u32 s24, s24, s27\n"
      "s_waitcnt lgkmcnt(3)\n"
      "v_mfma_f32_16x16x32_bf16 v[8:11], v[152:155], a[80:83], v[8:11]\n"
      "buffer_load_dwordx4 v[160:163], v80, s[60:63], s24 offen offset:0 sc1\n"
      "v_mfma_f32_16x16x32_bf16 v[12:15], v[144:147], a[68:71], v[12:15]\n"
      "buffer_load_dwordx4 v[164:167], v80, s[60:63], s24 offen offset:1024 sc1\n"
      "v_mfma_f32_16x16x32_bf16 v[12:15], v[152:155], a[84:87], v[12:15]\n"
      "s_lshr_b32 s24, s7, 11\n"
      "ds_read_b128 v[140:143], v88 offset:9216\n"
      "s_add_u32 s24, s24, 1\n"
      "v_mfma_f32_16x16x32_bf16 v[16:19], v[144:147], a[72:75], v[16:19]\n"
      "s_sub_u32 s24, s34, s24\n"
      "s_and_b32 s24, s24, 7\n"
      "s_lshl_b32 s24, s24, 13\n"
      "v_mfma_f32_16x16x32_bf16 v[16:19], v[152:155], a[88:91], v[16:19]\n"
      "s_add_u32 s24, s24, s27\n"
      "v_mfma_f32_16x16x32_bf16 v[20:23], v[144:147], a[76:79], v[20:23]\n"
      "buffer_load_dwordx4 v[168:171], v80, s[60:63], s24 offen offset:0 sc1\n"
      "v_mfma_f32_16x16x32_bf16 v[20:23], v[152:155], a[92:95], v[20:23]\n"
      "s_waitcnt lgkmcnt(2)\n"
      "ds_read_b128 v[144:147], v88 offset:10240\n"
      "v_mfma_f32_16x16x32_bf16 v[20:23], v[128:131], a[108:111], v[20:23]\n"
      "buffer_load_dwordx4 v[172:175], v80, s[60:63], s24 offen offset:1024 sc1\n"
      "v_mfma_f32_16x16x32_bf16 v[20:23], v[136:139], a[124:127], v[20:23]\n"
      "v_mfma_f32_16x16x32_bf16 v[16:19], v[128:131], a[104:107], v[16:19]\n"
      "v_mfma_f32_16x16x32_bf16 v[16:19], v[136:139], a[120:123], v[16:19]\n"
      "ds_read_b128 v[152:155], v88 offset:11264\n"
      "v_mfma_f32_16x16x32_bf16 v[12:15], v[128:131], a[100:103], v[12:15]\n"
      "v_mfma_f32_16x16x32_bf16 v[12:15], v[136:139], a[116:119], v[12:15]\n"
      "v_mfma_f32_16x16x32_bf16 v[8:11], v[128:131], a[96:99], v[8:11]\n"
      "v_mfma_f32_16x16x32_bf16 v[8:11], v[136:139], a[112:115], v[8:11]\n"
      "s_waitcnt lgkmcnt(2)\n"
      "ds_read_b128 v[128:131], v88 offset:12288\n"
      "v_mfma_f32_16x16x32_bf16 v[8:11], v[132:135], a[128:131], v[8:11]\n"
      "v_mfma_f32_16x16x32_bf16 v[8:11], v[140:143], a[144:147], v[8:11]\n"
      "v_mfma_f32_16x16x32_bf16 v[12:15], v[132:135], a[132:135], v[12:15]\n"
      "v_mfma_f32_16x16x32_bf16 v[12:15], v[140:143], a[148:151], v[12:15]\n"
      "ds_read_b128 v[136:139], v88 offset:13312\n"
      "v_mfma_f32_16x16x32_bf16 v[16:19], v[132:135], a[136:139], v[16:19]\n"
      "v_mfma_f32_16x16x32_bf16 v[16:19], v[140:143], a[152:155], v[16:19]\n"
      "v_mfma_f32_16x16x32_bf16 v[20:23], v[132:135], a[140:143], v[20:23]\n"
      "v_mfma_f32_16x16x32_bf16 v[20:23], v[140:143], a[156:159], v[20:23]\n"
      "s_waitcnt lgkmcnt(2)\n"
      "ds_read_b128 v[132:135], v88 offset:14336\n"
      "v_mfma_f32_16x16x32_bf16 v[20:23], v[144:147], a[172:175], v[20:23]\n"
      "v_mfma_f32_16x16x32_bf16 v[20:23], v[152:155], a[188:191], v[20:23]\n"
      "v_mfma_f32_16x16x32_bf16 v[16:19], v[144:147], a[168:171], v[16:19]\n"
      "s_mov_b64 exec, 96\n"
      "buffer_load_dword v7, v5, s[60:63], s31 offen sc1\n"
      "s_mov_b64 exec, -1\n"
      "v_mfma_f32_16x16x32_bf16 v[16:19], v[152:155], a[184:187], v[16:19]\n"
      "ds_read_b128 v[140:143], v88 offset:15360\n"
      "v_mfma_f32_16x16x32_bf16 v[12:15], v[144:147], a[164:167], v[12:15]\n"
      "v_mfma_f32_16x16x32_bf16 v[12:15], v[152:155], a[180:183], v[12:15]\n"
      "v_mfma_f32_16x16x32_bf16 v[8:11], v[144:147], a[160:163], v[8:11]\n"
      "v_mfma_f32_16x16x32_bf16 v[8:11], v[152:155], a[176:179], v[8:11]\n"
      "s_waitcnt lgkmcnt(2)\n"
      "ds_read_b128 v[144:147], v88 offset:16384\n"
      "v_mfma_f32_16x16x32_bf16 v[8:11], v[128:131], a[192:195], v[8:11]\n"
      "v_mfma_f32_16x16x32_bf16 v[8:11], v[136:139], a[208:211], v[8:11]\n"
      "v_mfma_f32_16x16x32_bf16 v[12:15], v[128:131], a[196:199], v[12:15]\n"
      "v_mfma_f32_16x16x32_bf16 v[12:15], v[136:139], a[212:215], v[12:15]\n"
      "ds_read_b128 v[152:155], v88 offset:17408\n"
      "v_mfma_f32_16x16x32_bf16 v[16:19], v[128:131], a[200:203], v[16:19]\n"
      "s_waitcnt vmcnt(4)\n"
      "v_lshlrev_b32 v2, 16, v160\n"
      "v_and_b32 v3, s16, v160\n"
      "v_mfma_f32_16x16x32_bf16 v[16:19], v[136:139], a[216:219], v[16:19]\n"
      "v_add_f32 v56, v56, v2\n"
      "v_add_f32 v57, v57, v3\n"
      "v_mfma_f32_16x16x32_bf16 v[20:23], v[128:131], a[204:207], v[20:23]\n"
      "v_lshlrev_b32 v2, 16, v161\n"
      "v_and_b32 v3, s16, v161\n"
      "v_mfma_f32_16x16x32_bf16 v[20:23], v[136:139], a[220:223], v[20:23]\n"
      "s_waitcnt lgkmcnt(2)\n"
      "v_add_f32 v58, v58, v2\n"
      "v_add_f32 v59, v59, v3\n"
      "v_mfma_f32_16x16x32_bf16 v[20:23], v[132:135], a[236:239], v[20:23]\n"
      "v_lshlrev_b32 v2, 16, v162\n"
      "v_and_b32 v3, s16, v162\n"
      "v_mfma_f32_16x16x32_bf16 v[20:23], v[140:143], a[252:255], v[20:23]\n"
      "v_add_f32 v60, v60, v2\n"
      "v_add_f32 v61, v61, v3\n"
      "v_mfma_f32_16x16x32_bf16 v[16:19], v[132:135], a[232:235], v[16:19]\n"
      "v_lshlrev_b32 v2, 16, v163\n"
      "v_and_b32 v3, s16, v163\n"
      "v_mfma_f32_16x16x32_bf16 v[16:19], v[140:143], a[248:251], v[16:19]\n"
      "v_add_f32 v62, v62, v2\n"
      "v_add_f32 v63, v63, v3\n"
      "v_mfma_f32_16x16x32_bf16 v[12:15], v[132:135], a[228:231], v[12:15]\n"
      "s_waitcnt vmcnt(3)\n"
      "v_lshlrev_b32 v2, 16, v164\n"
      "v_and_b32 v3, s16, v164\n"
      "v_mfma_f32_16x16x32_bf16 v[12:15], v[140:143], a[244:247], v[12:15]\n"
      "v_add_f32 v64, v64, v2\n"
      "v_add_f32 v65, v65, v3\n"
      "v_mfma_f32_16x16x32_bf16 v[8:11], v[132:135], a[224:227], v[8:11]\n"
      "v_lshlrev_b32 v2, 16, v165\n"
      "v_and_b32 v3, s16, v165\n"
      "v_mfma_f32_16x16x32_bf16 v[8:11], v[140:143], a[240:243], v[8:11]\n"
      "s_waitcnt lgkmcnt(0)\n"
      "v_add_f32 v66, v66, v2\n"
      "v_add_f32 v67, v67, v3\n"
      "v_mfma_f32_16x16x32_bf16 v[8:11], v[144:147], v[220:223], v[8:11]\n"
      "v_lshlrev_b32 v2, 16, v166\n"
      "v_and_b32 v3, s16, v166\n"
      "v_mfma_f32_16x16x32_bf16 v[8:11], v[152:155], v[236:239], v[8:11]\n"
      "v_add_f32 v68, v68, v2\n"
      "v_add_f32 v69, v69, v3\n"
      "v_mfma_f32_16x16x32_bf16 v[12:15], v[144:147], v[224:227], v[12:15]\n"
      "v_mfma_f32_16x16x32_bf16 v[12:15], v[152:155], v[240:243], v[12:15]\n"
      "v_mfma_f32_16x16x32_bf16 v[16:19], v[144:147], v[228:231], v[16:19]\n"
      "v_mfma_f32_16x16x32_bf16 v[16:19], v[152:155], v[244:247], v[16:19]\n"
      "v_mfma_f32_16x16x32_bf16 v[20:23], v[144:147], v[232:235], v[20:23]\n"
      "v_mfma_f32_16x16x32_bf16 v[20:23], v[152:155], v[248:251], v[20:23]\n"
      "s_lshr_b32 s12, s8, 11\n"
      "s_add_u32 s12, s12, 1\n"
      "s_sub_u32 s12, s34, s12\n"
      "s_and_b32 s12, s12, 7\n"
      "s_lshl_b32 s12, s12, 13\n"
      "s_add_u32 s12, s12, s27\n"
      "s_lshr_b32 s13, s9, 11\n"
      "s_add_u32 s13, s13, 1\n"
      "s_sub_u32 s13, s34, s13\n"
      "s_and_b32 s13, s13, 7\n"
      "s_lshl_b32 s13, s13, 13\n"
      "s_add_u32 s13, s13, s27\n"
      "s_lshr_b32 s14, s10, 11\n"
      "s_add_u32 s14, s14, 1\n"
      "s_sub_u32 s14, s34, s14\n"
      "s_and_b32 s14, s14, 7\n"
      "s_lshl_b32 s14, s14, 13\n"
      "s_add_u32 s14, s14, s27\n"
      "s_waitcnt vmcnt(0)\n"
      "RC30_%=:\n"
      "v_cmp_le_u32 vcc, 7, v7\n"
      "s_nop 1\n"
      "s_andn2_b32 s69, 96, vcc_lo\n"
      "s_cbranch_scc0 RO31_%=\n"
      "s_cmp_eq_u32 s87, 0\n"
      "s_cbranch_scc1 RO31_%=\n"
      "s_sub_u32 s87, s87, 1\n"
      "s_mov_b64 exec, 96\n"
      "buffer_load_dword v7, v5, s[60:63], s31 offen sc1\n"
      "s_mov_b64 exec, -1\n"
      "s_waitcnt vmcnt(0)\n"
      "s_branch RC30_%=\n"
      "RO31_%=:\n"
      "buffer_load_dwordx4 v[176:179], v80, s[60:63], s12 offen offset:0 sc1\n"
      "buffer_load_dwordx4 v[180:183], v80, s[60:63], s12 offen offset:1024 sc1\n"
      "buffer_load_dwordx4 v[96:99], v80, s[60:63], s13 offen offset:0 sc1\n"
      "buffer_load_dwordx4 v[100:103], v80, s[60:63], s13 offen offset:1024 sc1\n"
      "v_add_f32 v56, v56, v8\n"
      "v_add_f32 v57, v57, v9\n"
      "v_add_f32 v58, v58, v10\n"
      "v_add_f32 v59, v59, v11\n"
      "v_add_f32 v60, v60, v12\n"
      "v_add_f32 v61, v61, v13\n"
      "v_add_f32 v62, v62, v14\n"
      "v_add_f32 v63, v63, v15\n"
      "v_add_f32 v64, v64, v16\n"
      "v_add_f32 v65, v65, v17\n"
      "v_add_f32 v66, v66, v18\n"
      "v_add_f32 v67, v67, v19\n"
      "v_add_f32 v68, v68, v20\n"
      "v_add_f32 v69, v69, v21\n"
      "v_add_f32 v70, v70, v22\n"
      "v_add_f32 v71, v71, v23\n"
      "buffer_load_dwordx4 v[104:107], v80, s[60:63], s14 offen offset:0 sc1\n"
      "buffer_load_dwordx4 v[108:111], v80, s[60:63], s14 offen offset:1024 sc1\n"
      "v_lshlrev_b32 v2, 16, v167\n"
      "v_and_b32 v3, s16, v167\n"
      "v_add_f32 v70, v70, v2\n"
      "v_add_f32 v71, v71, v3\n"
      "v_lshlrev_b32 v2, 16, v168\n"
      "v_and_b32 v3, s16, v168\n"
      "v_add_f32 v56, v56, v2\n"
      "v_add_f32 v57, v57, v3\n"
      "v_lshlrev_b32 v2, 16, v169\n"
      "v_and_b32 v3, s16, v169\n"
      "v_add_f32 v58, v58, v2\n"
      "v_add_f32 v59, v59, v3\n"
      "v_lshlrev_b32 v2, 16, v170\n"
      "v_and_b32 v3, s16, v170\n"
      "v_add_f32 v60, v60, v2\n"
      "v_add_f32 v61, v61, v3\n"
      "v_lshlrev_b32 v2, 16, v171\n"
      "v_and_b32 v3, s16, v171\n"
      "v_add_f32 v62, v62, v2\n"
      "v_add_f32 v63, v63, v3\n"
      "v_lshlrev_b32 v2, 16, v172\n"
      "v_and_b32 v3, s16, v172\n"
      "v_add_f32 v64, v64, v2\n"
      "v_add_f32 v65, v65, v3\n"
      "v_lshlrev_b32 v2, 16, v173\n"
      "v_and_b32 v3, s16, v173\n"
      "v_add_f32 v66, v66, v2\n"
      "v_add_f32 v67, v67, v3\n"
      "v_lshlrev_b32 v2, 16, v174\n"
      "v_and_b32 v3, s16, v174\n"
      "v_add_f32 v68, v68, v2\n"
      "v_add_f32 v69, v69, v3\n"
      "v_lshlrev_b32 v2, 16, v175\n"
      "v_and_b32 v3, s16, v175\n"
      "v_add_f32 v70, v70, v2\n"
      "v_add_f32 v71, v71, v3\n"
      "s_waitcnt vmcnt(5)\n"
      "v_lshlrev_b32 v2, 16, v176\n"
      "v_and_b32 v3, s16, v176\n"
      "v_add_f32 v56, v56, v2\n"
      "v_add_f32 v57, v57, v3\n"
      "v_lshlrev_b32 v2, 16, v177\n"
      "v_and_b32 v3, s16, v177\n"
      "v_add_f32 v58, v58, v2\n"
      "v_add_f32 v59, v59, v3\n"
      "v_lshlrev_b32 v2, 16, v178\n"
      "v_and_b32 v3, s16, v178\n"
      "v_add_f32 v60, v60, v2\n"
      "v_add_f32 v61, v61, v3\n"
      "v_lshlrev_b32 v2, 16, v179\n"
      "v_and_b32 v3, s16, v179\n"
      "v_add_f32 v62, v62, v2\n"
      "v_add_f32 v63, v63, v3\n"
      "s_waitcnt vmcnt(4)\n"
      "v_lshlrev_b32 v2, 16, v180\n"
      "v_and_b32 v3, s16, v180\n"
      "v_add_f32 v64, v64, v2\n"
      "v_add_f32 v65, v65, v3\n"
      "v_lshlrev_b32 v2, 16, v181\n"
      "v_and_b32 v3, s16, v181\n"
      "v_add_f32 v66, v66, v2\n"
      "v_add_f32 v67, v67, v3\n"
      "v_lshlrev_b32 v2, 16, v182\n"
      "v_and_b32 v3, s16, v182\n"
      "v_add_f32 v68, v68, v2\n"
      "v_add_f32 v69, v69, v3\n"
      "v_lshlrev_b32 v2, 16, v183\n"
      "v_and_b32 v3, s16, v183\n"
      "v_add_f32 v70, v70, v2\n"
      "v_add_f32 v71, v71, v3\n"
      "s_waitcnt vmcnt(3)\n"
      "v_lshlrev_b32 v2, 16, v96\n"
      "v_and_b32 v3, s16, v96\n"
      "v_add_f32 v56, v56, v2\n"
      "v_add_f32 v57, v57, v3\n"
      "v_lshlrev_b32 v2, 16, v97\n"
      "v_and_b32 v3, s16, v97\n"
      "v_add_f32 v58, v58, v2\n"
      "v_add_f32 v59, v59, v3\n"
      "v_lshlrev_b32 v2, 16, v98\n"
      "v_and_b32 v3, s16, v98\n"
      "v_add_f32 v60, v60, v2\n"
      "v_add_f32 v61, v61, v3\n"
      "v_lshlrev_b32 v2, 16, v99\n"
      "v_and_b32 v3, s16, v99\n"
      "v_add_f32 v62, v62, v2\n"
      "v_add_f32 v63, v63, v3\n"
      "s_waitcnt vmcnt(2)\n"
      "v_lshlrev_b32 v2, 16, v100\n"
      "v_and_b32 v3, s16, v100\n"
      "v_add_f32 v64, v64, v2\n"
      "v_add_f32 v65, v65, v3\n"
      "v_lshlrev_b32 v2, 16, v101\n"
      "v_and_b32 v3, s16, v101\n"
      "v_add_f32 v66, v66, v2\n"
      "v_add_f32 v67, v67, v3\n"
      "v_lshlrev_b32 v2, 16, v102\n"
      "v_and_b32 v3, s16, v102\n"
      "v_add_f32 v68, v68, v2\n"
      "v_add_f32 v69, v69, v3\n"
      "v_lshlrev_b32 v2, 16, v103\n"
      "v_and_b32 v3, s16, v103\n"
      "v_add_f32 v70, v70, v2\n"
      "v_add_f32 v71, v71, v3\n"
      "s_waitcnt vmcnt(0)\n"
      "SC32_%=:\n"
      "v_max3_u32 v2, v104, v105, v106\n"
      "v_max3_u32 v3, v107, v108, v109\n"
      "v_max3_u32 v2, v110, v111, v2\n"
      "v_max_u32 v2, v2, v3\n"
      "v_cmp_eq_u32 vcc, -1, v2\n"
      "s_nop 1\n"
      "s_cbranch_vccz SO33_%=\n"
      "s_cmp_eq_u32 s87, 0\n"
      "s_cbranch_scc1 SO33_%=\n"
      "s_sub_u32 s87, s87, 1\n"
      "buffer_load_dwordx4 v[104:107], v80, s[60:63], s14 offen offset:0 sc1\n"
      "buffer_load_dwordx4 v[108:111], v80, s[60:63], s14 offen offset:1024 sc1\n"
      "s_waitcnt vmcnt(0)\n"
      "s_branch SC32_%=\n"
      "SO33_%=:\n"
      "v_lshlrev_b32 v2, 16, v104\n"
      "v_and_b32 v3, s16, v104\n"
      "v_add_f32 v56, v56, v2\n"
      "v_add_f32 v57, v57, v3\n"
      "v_lshlrev_b32 v2, 16, v105\n"
      "v_and_b32 v3, s16, v105\n"
      "v_add_f32 v58, v58, v2\n"
      "v_add_f32 v59, v59, v3\n"
      "v_lshlrev_b32 v2, 16, v106\n"
      "v_and_b32 v3, s16, v106\n"
      "v_add_f32 v60, v60, v2\n"
      "v_add_f32 v61, v61, v3\n"
      "v_lshlrev_b32 v2, 16, v107\n"
      "v_and_b32 v3, s16, v107\n"
      "v_add_f32 v62, v62, v2\n"
      "v_add_f32 v63, v63, v3\n"
      "v_mul_f32 v56, s17, v56\n"
      "v_mul_f32 v57, s17, v57\n"
      "v_mul_f32 v58, s17, v58\n"
      "v_mul_f32 v59, s17, v59\n"
      "v_mul_f32 v60, s17, v60\n"
      "v_mul_f32 v61, s17, v61\n"
      "v_mul_f32 v62, s17, v62\n"
      "v_mul_f32 v63, s17, v63\n"
      "v_cvt_pk_bf16_f32 v72, v56, v57\n"
      "v_cvt_pk_bf16_f32 v73, v58, v59\n"
      "v_cvt_pk_bf16_f32 v74, v60, v61\n"
      "v_cvt_pk_bf16_f32 v75, v62, v63\n"
      "s_nop 0\n"
      "v_permlane16_swap_b32 v72, v74\n"
      "v_permlane16_swap_b32 v73, v75\n"
      "ds_write_b128 v81, v[72:75] offset:0\n"
      "v_lshlrev_b32 v2, 16, v108\n"
      "v_and_b32 v3, s16, v108\n"
      "v_add_f32 v64, v64, v2\n"
      "v_add_f32 v65, v65, v3\n"
      "v_lshlrev_b32 v2, 16, v109\n"
      "v_and_b32 v3, s16, v109\n"
      "v_add_f32 v66, v66, v2\n"
      "v_add_f32 v67, v67, v3\n"
      "v_lshlrev_b32 v2, 16, v110\n"
      "v_and_b32 v3, s16, v110\n"
      "v_add_f32 v68, v68, v2\n"
      "v_add_f32 v69, v69, v3\n"
      "v_lshlrev_b32 v2, 16, v111\n"
      "v_and_b32 v3, s16, v111\n"
      "v_add_f32 v70, v70, v2\n"
      "v_add_f32 v71, v71, v3\n"
      "v_mul_f32 v64, s17, v64\n"
      "v_mul_f32 v65, s17, v65\n"
      "v_mul_f32 v66, s17, v66\n"
      "v_mul_f32 v67, s17, v67\n"
      "v_mul_f32 v68, s17, v68\n"
      "v_mul_f32 v69, s17, v69\n"
      "v_mul_f32 v70, s17, v70\n"
      "v_mul_f32 v71, s17, v71\n"
      "v_cvt_pk_bf16_f32 v76, v64, v65\n"
      "v_cvt_pk_bf16_f32 v77, v66, v67\n"
      "v_cvt_pk_bf16_f32 v78, v68, v69\n"
      "v_cvt_pk_bf16_f32 v79, v70, v71\n"
      "s_nop 0\n"
      "v_permlane16_swap_b32 v76, v78\n"
      "v_permlane16_swap_b32 v77, v79\n"
      "ds_write_b128 v81, v[76:79] offset:4096\n"
      "s_waitcnt lgkmcnt(0)\n"
      "s_barrier\n"
      "ds_read_b128 v[8:11], v82 offset:0\n"
      "ds_read_b128 v[12:15], v82 offset:1024\n"
      "ds_read_b128 v[16:19], v85\n"
      "s_waitcnt lgkmcnt(2)\n"
      "buffer_store_dwordx4 v[8:11], v83, s[56:59], s20 offen sc1\n"
      "s_waitcnt lgkmcnt(1)\n"
      "buffer_store_dwordx4 v[12:15], v83, s[56:59], s18 offen sc1\n"
      "s_mov_b64 exec, 0xffff\n"
      "s_waitcnt lgkmcnt(0)\n"
      "buffer_store_dwordx4 v[16:19], v83, s[56:59], s21 offen sc1\n"
      "s_mov_b64 exec, -1\n"
      "KEND_%=:\n"
      :
      : "s"(__builtin_amdgcn_kernarg_segment_ptr()), "s"(blockIdx.x), "v"(threadIdx.x)
      : "v1","v2","v3","v4","v5","v6","v7","v8","v9","v10","v11","v12","v13","v14","v15","v16","v17","v18","v19","v20","v21","v22","v23","v24","v25","v26","v27","v28","v29","v30","v31","v32","v33","v34","v35","v36","v37","v38","v39","v40","v41","v42","v43","v44","v45","v46","v47","v48","v49","v50","v51","v52","v53","v54","v55","v56","v57","v58","v59","v60","v61","v62","v63","v64","v65","v66","v67","v68","v69","v70","v71","v72","v73","v74","v75","v76","v77","v78","v79","v80","v81","v82","v83","v84","v85","v86","v87","v88","v89","v90","v91","v92","v93","v94","v95","v96","v97","v98","v99","v100","v101","v102","v103","v104","v105","v106","v107","v108","v109","v110","v111","v112","v113","v114","v115","v116","v117","v118","v119","v120","v121","v122","v123","v124","v125","v126","v127","v128","v129","v130","v131","v132","v133","v134","v135","v136","v137","v138","v139","v140","v141","v142","v143","v144","v145","v146","v147","v148","v149","v150","v151","v152","v153","v154","v155","v156","v157","v158","v159","v160","v161","v162","v163","v164","v165","v166","v167","v168","v169","v170","v171","v172","v173","v174","v175","v176","v177","v178","v179","v180","v181","v182","v183","v184","v185","v186","v187","v188","v189","v190","v191","v192","v193","v194","v195","v196","v197","v198","v199","v200","v201","v202","v203","v204","v205","v206","v207","v208","v209","v210","v211","v212","v213","v214","v215","v216","v217","v218","v219","v220","v221","v222","v223","v224","v225","v226","v227","v228","v229","v230","v231","v232","v233","v234","v235","v236","v237","v238","v239","v240","v241","v242","v243","v244","v245","v246","v247","v248","v249","v250","v251","v252","v253","v254","v255","a0","a1","a2","a3","a4","a5","a6","a7","a8","a9","a10","a11","a12","a13","a14","a15","a16","a17","a18","a19","a20","a21","a22","a23","a24","a25","a26","a27","a28","a29","a30","a31","a32","a33","a34","a35","a36","a37","a38","a39","a40","a41","a42","a43","a44","a45","a46","a47","a48","a49","a50","a51","a52","a53","a54","a55","a56","a57","a58","a59","a60","a61","a62","a63","a64","a65","a66","a67","a68","a69","a70","a71","a72","a73","a74","a75","a76","a77","a78","a79","a80","a81","a82","a83","a84","a85","a86","a87","a88","a89","a90","a91","a92","a93","a94","a95","a96","a97","a98","a99","a100","a101","a102","a103","a104","a105","a106","a107","a108","a109","a110","a111","a112","a113","a114","a115","a116","a117","a118","a119","a120","a121","a122","a123","a124","a125","a126","a127","a128","a129","a130","a131","a132","a133","a134","a135","a136","a137","a138","a139","a140","a141","a142","a143","a144","a145","a146","a147","a148","a149","a150","a151","a152","a153","a154","a155","a156","a157","a158","a159","a160","a161","a162","a163","a164","a165","a166","a167","a168","a169","a170","a171","a172","a173","a174","a175","a176","a177","a178","a179","a180","a181","a182","a183","a184","a185","a186","a187","a188","a189","a190","a191","a192","a193","a194","a195","a196","a197","a198","a199","a200","a201","a202","a203","a204","a205","a206","a207","a208","a209","a210","a211","a212","a213","a214","a215","a216","a217","a218","a219","a220","a221","a222","a223","a224","a225","a226","a227","a228","a229","a230","a231","a232","a233","a234","a235","a236","a237","a238","a239","a240","a241","a242","a243","a244","a245","a246","a247","a248","a249","a250","a251","a252","a253","a254","a255","s4","s5","s6","s7","s8","s9","s10","s11","s12","s13","s14","s15","s16","s17","s18","s19","s20","s21","s22","s23","s24","s25","s26","s27","s28","s29","s30","s31","s34","s35","s36","s37","s38","s39","s40","s41","s42","s43","s44","s45","s46","s47","s48","s49","s50","s51","s52","s53","s54","s55","s56","s57","s58","s59","s60","s61","s62","s63","s64","s65","s66","s67","s68","s69","s70","s71","s72","s73","s74","s75","s76","s77","s78","s79","s80","s81","s82","s83","s84","s85","s86","s87","s88","s89","s90","s91","s92","s93","s94","s95","s96","s97","s98","s99","vcc","memory");
}

extern "C" void kernel_launch(void* const* d_in, const int* in_sizes, int n_in, void* d_out, int out_size, void* d_ws,
                              size_t ws_size, hipStream_t stream) {
  (void)hipFuncSetAttribute((const void*)fused_block_kernel, hipFuncAttributeMaxDynamicSharedMemorySize, 163840);
  fused_block_kernel<<<256, 256, 163840, stream>>>(d_in[0], d_in[1], d_in[2], d_in[4], d_in[5], d_out, d_ws);
}
```
